```python
import torch

def _softmax(x, axis=-1):
    return torch.softmax(x, dim=axis)

def _gelu(x):
    return torch.nn.functional.gelu(x, approximate='tanh')

def _silu(x):
    return torch.nn.functional.silu(x)

def _sigmoid(x):
    return torch.sigmoid(x)

def _layernorm(x, axis=-1, eps=1e-05):
    m = x.mean(dim=axis, keepdim=True)
    v = x.var(dim=axis, keepdim=True, unbiased=False)
    return (x - m) / torch.sqrt(v + eps)

def _scatter_add(tgt, idx, src, axis=0):
    return tgt.clone().index_add_(axis, idx, src)

def _segment_sum(src, seg, n):
    out = torch.zeros((n,) + src.shape[1:], dtype=src.dtype)
    return out.index_add_(0, seg, src)

def _pad(x, pw):
    flat = tuple((v for pair in reversed(pw) for v in pair))
    return torch.nn.functional.pad(x, flat)
kernel_name = 'standalone__ragged_sequences_varlen_flash_attn__ragged'

def setup_inputs():

    _w = lambda a: (lambda t: t.to(torch.bfloat16) if t.is_floating_point() else t)(torch.as_tensor(a))
    cu = torch.tensor([0, 506, 648, 1109, 1416, 1494, 1644, 1941, 2399], dtype=torch.int64)
    T = int(cu[-1])
    Hq, Hkv, D = (16, 8, 192)
    assert Hq % Hkv == 0
    return {'q': _w((lambda *s: torch.randn(*s, dtype=torch.bfloat16) * 0.8)(T, Hq, D)), 'k': _w((lambda *s: torch.randn(*s, dtype=torch.bfloat16) * 0.8)(T, Hkv, D)), 'v': _w((lambda *s: torch.randn(*s, dtype=torch.bfloat16) * 0.05)(T, Hkv, D)), 'cu': _w(cu)}

def reference(q, k, v, cu):
    T, Hq, D = q.shape
    Hkv = k.shape[1]
    rep = Hq // Hkv
    scale = D ** (-0.5)
    outs = []
    for i in range(len(cu) - 1):
        s, e = (int(cu[i]), int(cu[i + 1]))
        L = e - s
        qi = q[s:e].clone()
        ki = (lambda x, n, axis: torch.repeat_interleave(x, n, dim=axis))(k[s:e], rep, axis=1)
        vi = (lambda x, n, axis: torch.repeat_interleave(x, n, dim=axis))(v[s:e], rep, axis=1)
        logits = torch.einsum('qhd,khd->hqk', qi, ki) * scale
        ar = torch.arange(L)
        neg = (lambda x, dt: x.to(dt))(ar[None, :] > ar[:, None], logits.dtype) * -1000000000.0
        p = _softmax(logits + neg[None, :, :], axis=-1)
        outs.append(torch.einsum('hqk,khd->qhd', p, vi))
    return torch.concatenate(outs, axis=0)
```

```cpp
#include <hip/hip_runtime.h>
#include <hip/hip_bf16.h>
#include <stdint.h>

typedef __bf16 bf16x8 __attribute__((ext_vector_type(8)));
typedef __bf16 bf16x2 __attribute__((ext_vector_type(2)));
typedef short s16x4 __attribute__((ext_vector_type(4)));
typedef short s16x8 __attribute__((ext_vector_type(8)));
typedef float f32x2 __attribute__((ext_vector_type(2)));
typedef float f32x4 __attribute__((ext_vector_type(4)));
typedef float f32x16 __attribute__((ext_vector_type(16)));
typedef unsigned u32x4 __attribute__((ext_vector_type(4)));
typedef __attribute__((address_space(3))) s16x4* lds_s16x4_ptr;
typedef __attribute__((address_space(3))) bf16x8* lds_bf16x8_ptr;
typedef __attribute__((address_space(3))) f32x4* lds_f32x4_ptr;
typedef __attribute__((address_space(3))) f32x2* lds_f32x2_ptr;
typedef __attribute__((address_space(3))) char* lds_char_ptr;

#define DI __device__ __forceinline__

constexpr int HQ = 16, HKV = 8, D = 192;
constexpr unsigned QSTRIDE = HQ * D * 2;
constexpr unsigned KSTRIDE = HKV * D * 2;
constexpr unsigned ROWB = D * 2;
constexpr unsigned SLOT = 32 * ROWB;
constexpr int NSLOT = 6;
constexpr unsigned LDS_BYTES = NSLOT * SLOT;
constexpr float SCALE_LOG2E = 0.07216878364870322f * 1.4426950408889634f;
constexpr float THR_RAW = 8.0f / SCALE_LOG2E;
constexpr int CUS_PER_XCD = 32;

DI unsigned rfl(unsigned x) { return __builtin_amdgcn_readfirstlane(x); }
DI unsigned cvtpk(float lo, float hi) {
    f32x2 v = {lo, hi}; bf16x2 b = __builtin_convertvector(v, bf16x2); return __builtin_bit_cast(unsigned, b);
}
DI float swap_max(float x) {
    auto r = __builtin_amdgcn_permlane32_swap(__float_as_uint(x), __float_as_uint(x), false, false);
    return fmaxf(__uint_as_float(r[0]), __uint_as_float(r[1]));
}
DI float swap_add(float x) {
    auto r = __builtin_amdgcn_permlane32_swap(__float_as_uint(x), __float_as_uint(x), false, false);
    return __uint_as_float(r[0]) + __uint_as_float(r[1]);
}
DI u32x4 make_srd(const void* base, unsigned nbytes) {
    unsigned long long a = (unsigned long long)base;
    u32x4 s; s[0] = rfl((unsigned)a); s[1] = rfl((unsigned)(a >> 32) & 0xffffu); s[2] = rfl(nbytes); s[3] = 0x00020000u;
    return s;
}
template <int IMM> DI void dma16(unsigned m0v, unsigned voff, u32x4 srd, unsigned soff) {
    asm volatile("s_mov_b32 m0, %0\n\ts_nop 0\n\tbuffer_load_dwordx4 %1, %2, %3 offen offset:%4 lds"
                 :: "s"(m0v), "v"(voff), "s"(srd), "s"(soff), "i"(IMM) : "memory");
}
template <int N> DI void wait_vm() { asm volatile("s_waitcnt vmcnt(%0)" :: "i"(N) : "memory"); }
DI void barrier() { asm volatile("s_waitcnt lgkmcnt(0)\n\ts_barrier" ::: "memory"); }

template <int S>
DI void attn_item(const char* __restrict__ qp, const char* __restrict__ kp, const char* __restrict__ vp, char* __restrict__ op,
                  int grp, int s0, int L, int p0, int n, lds_char_ptr smem) {
    constexpr int NS = (S == 1) ? 6 : 3;
    constexpr int PW = (S == 1) ? 3 : 6;
    const int tid = threadIdx.x, lane = tid & 63;
    const int w = rfl(tid >> 6);
    const int hh = w & 1, up = w >> 1;
    const int r32 = lane & 31, h = lane >> 5;
    const int sig = (S == 1) ? 0 : up;
    const int pw = (S == 1) ? p0 + 32 * up : p0;
    const int steps = (S == 1) ? n : (n + 1) >> 1;
    const int E = 2 * steps;
    const int tlast = (pw < L) ? min(n - 1, pw >> 5) : -1;

    const u32x4 ksrd = make_srd(kp + (size_t)s0 * KSTRIDE + grp * ROWB, (unsigned)(L - 1) * KSTRIDE + ROWB);
    const u32x4 vsrd = make_srd(vp + (size_t)s0 * KSTRIDE + grp * ROWB, (unsigned)(L - 1) * KSTRIDE + ROWB);
    const u32x4 qsrd = make_srd(qp + (size_t)s0 * QSTRIDE, (unsigned)L * QSTRIDE);
    const u32x4 osrd = make_srd(op + (size_t)s0 * QSTRIDE, (unsigned)L * QSTRIDE);

    const unsigned lds0 = (unsigned)(size_t)smem;
    const unsigned ring = lds0 + sig * NS * SLOT;
    const unsigned drow = (unsigned)(8 * hh + (lane >> 3)) * KSTRIDE;
    const unsigned voffK = drow + 16u * ((lane & 7) ^ ((hh << 2) | (lane >> 4)));
    const unsigned voffV = drow + 16u * ((lane & 7) ^ (((lane >> 4) & 1) << 2));

    auto issue = [&](int e, int slot) {
        const int t = S * (e >> 1) + sig;
        const unsigned sb = ring + slot * SLOT;
        if (S == 1) {
            const unsigned soff = (unsigned)(32 * t + 16 * up) * KSTRIDE;
            const unsigned m = sb + w * 1024;
            if (e & 1) { dma16<0>(m, voffV, vsrd, soff); dma16<128>(m + 4096 - 128, voffV, vsrd, soff); dma16<256>(m + 8192 - 256, voffV, vsrd, soff); }
            else       { dma16<0>(m, voffK, ksrd, soff); dma16<128>(m + 4096 - 128, voffK, ksrd, soff); dma16<256>(m + 8192 - 256, voffK, ksrd, soff); }
        } else {
            const unsigned soff = (unsigned)(32 * t) * KSTRIDE, soff2 = soff + 16 * KSTRIDE;
            const unsigned m = sb + hh * 1024, m2 = m + 2048;
            if (e & 1) { dma16<0>(m, voffV, vsrd, soff); dma16<128>(m + 4096 - 128, voffV, vsrd, soff); dma16<256>(m + 8192 - 256, voffV, vsrd, soff);
                         dma16<0>(m2, voffV, vsrd, soff2); dma16<128>(m2 + 4096 - 128, voffV, vsrd, soff2); dma16<256>(m2 + 8192 - 256, voffV, vsrd, soff2); }
            else       { dma16<0>(m, voffK, ksrd, soff); dma16<128>(m + 4096 - 128, voffK, ksrd, soff); dma16<256>(m + 8192 - 256, voffK, ksrd, soff);
                         dma16<0>(m2, voffK, ksrd, soff2); dma16<128>(m2 + 4096 - 128, voffK, ksrd, soff2); dma16<256>(m2 + 8192 - 256, voffK, ksrd, soff2); }
        }
    };
    auto wait_elem = [&](int e) {
        const int rem = E - 1 - e;
        if (S == 1) {
            if (rem >= 4) wait_vm<12>(); else if (rem == 3) wait_vm<9>(); else if (rem == 2) wait_vm<6>(); else if (rem == 1) wait_vm<3>(); else wait_vm<0>();
        } else {
            if (rem >= 1) wait_vm<6>(); else wait_vm<0>();
        }
    };

    issue(0, 0);
    bf16x8 q[12];
    {
        const unsigned qoff = (unsigned)(pw + r32) * QSTRIDE + (unsigned)(2 * grp + hh) * ROWB + h * 16;
#define QLD(i) asm volatile("buffer_load_dwordx4 %0, %1, %2, 0 offen offset:%3" : "=v"(q[i]) : "v"(qoff), "s"(qsrd), "i"(32 * i) : "memory")
        QLD(0); QLD(1); QLD(2); QLD(3); QLD(4); QLD(5); QLD(6); QLD(7); QLD(8); QLD(9); QLD(10); QLD(11);
#undef QLD
    }
#pragma unroll
    for (int e = 1; e < NS - 1; ++e) if (e < E) issue(e, e);

    const unsigned kb0 = r32 * 128 + 16 * (h ^ ((r32 >> 1) & 7));
    const int q4 = (lane & 15) >> 2, p4 = lane & 3, blk = (lane >> 4) & 1;
    const unsigned vb0 = (4 * h + q4) * 128 + 64 * (q4 >> 1) + 32 * blk + 8 * p4;

    f32x16 o[6];
#pragma unroll
    for (int c = 0; c < 6; ++c) o[c] = f32x16{};
    float m = -1e30f, l = 0.f;
    const int dq0 = pw + r32 - 4 * h;

    int slot_rd = 0, slot_wr = NS - 1;
    bool first = true;
    for (int st = 0; st < steps; ++st) {
        const int t = S * st + sig;
        const bool act = t <= tlast;
        wait_elem(2 * st);
        if (st == 0) asm volatile("" : "+v"(q[0]), "+v"(q[1]), "+v"(q[2]), "+v"(q[3]), "+v"(q[4]), "+v"(q[5]), "+v"(q[6]), "+v"(q[7]), "+v"(q[8]), "+v"(q[9]), "+v"(q[10]), "+v"(q[11]) :: "memory");
        barrier();
        if (2 * st + NS - 1 < E) issue(2 * st + NS - 1, slot_wr);
        slot_wr = slot_wr + 1 == NS ? 0 : slot_wr + 1;
        bf16x8 pf0, pf1;
        if (act) {
            lds_char_ptr kbase = (lds_char_ptr)(size_t)(ring + slot_rd * SLOT);
            f32x16 s = f32x16{};
#pragma unroll
            for (int i = 0; i < 12; ++i) {
                const bf16x8 kf = *(lds_bf16x8_ptr)(kbase + (kb0 ^ (32 * (i & 3))) + (i >> 2) * 4096);
                s = __builtin_amdgcn_mfma_f32_32x32x16_bf16(kf, q[i], s, 0, 0, 0);
            }
            if (t == tlast) {
                const int dq = dq0 - 32 * t;
#pragma unroll
                for (int i = 0; i < 16; ++i) { const int c = (i & 3) + 8 * (i >> 2); s[i] = (c <= dq) ? s[i] : -__builtin_inff(); }
            }
            float pmax = fmaxf(s[0], s[1]);
#pragma unroll
            for (int i = 2; i < 16; ++i) pmax = fmaxf(pmax, s[i]);
            pmax = swap_max(pmax);
            if (__any(pmax > m + THR_RAW)) {
                const float mn = fmaxf(m, pmax);
                if (!first) {
                    const float alpha = __builtin_amdgcn_exp2f((m - mn) * SCALE_LOG2E);
                    l *= alpha;
#pragma unroll
                    for (int c = 0; c < 6; ++c)
#pragma unroll
                        for (int i = 0; i < 16; ++i) o[c][i] *= alpha;
                }
                m = mn;
            }
            first = false;
            const float mc = -m * SCALE_LOG2E;
            float ps = 0.f;
#pragma unroll
            for (int i = 0; i < 16; ++i) { s[i] = __builtin_amdgcn_exp2f(fmaf(s[i], SCALE_LOG2E, mc)); ps += s[i]; }
            l += ps;
            u32x4 a = {cvtpk(s[0], s[1]), cvtpk(s[2], s[3]), cvtpk(s[4], s[5]), cvtpk(s[6], s[7])};
            u32x4 b = {cvtpk(s[8], s[9]), cvtpk(s[10], s[11]), cvtpk(s[12], s[13]), cvtpk(s[14], s[15])};
            pf0 = __builtin_bit_cast(bf16x8, a); pf1 = __builtin_bit_cast(bf16x8, b);
        }
        slot_rd = slot_rd + 1 == NS ? 0 : slot_rd + 1;
        wait_elem(2 * st + 1);
        barrier();
        if (2 * st + NS < E) issue(2 * st + NS, slot_wr);
        slot_wr = slot_wr + 1 == NS ? 0 : slot_wr + 1;
        if (act) {
            lds_char_ptr vbase = (lds_char_ptr)(size_t)(ring + slot_rd * SLOT);
#pragma unroll
            for (int c = 0; c < 6; ++c) {
                lds_char_ptr a = vbase + (vb0 ^ (64 * (c & 1))) + (c >> 1) * 4096;
                const s16x4 v00 = __builtin_amdgcn_ds_read_tr16_b64_v4i16((lds_s16x4_ptr)(a));
                const s16x4 v01 = __builtin_amdgcn_ds_read_tr16_b64_v4i16((lds_s16x4_ptr)(a + 1024));
                const s16x4 v10 = __builtin_amdgcn_ds_read_tr16_b64_v4i16((lds_s16x4_ptr)(a + 2048));
                const s16x4 v11 = __builtin_amdgcn_ds_read_tr16_b64_v4i16((lds_s16x4_ptr)(a + 3072));
                const s16x8 f0 = __builtin_shufflevector(v00, v01, 0, 1, 2, 3, 4, 5, 6, 7);
                const s16x8 f1 = __builtin_shufflevector(v10, v11, 0, 1, 2, 3, 4, 5, 6, 7);
                o[c] = __builtin_amdgcn_mfma_f32_32x32x16_bf16(__builtin_bit_cast(bf16x8, f0), pf0, o[c], 0, 0, 0);
                o[c] = __builtin_amdgcn_mfma_f32_32x32x16_bf16(__builtin_bit_cast(bf16x8, f1), pf1, o[c], 0, 0, 0);
            }
        }
        slot_rd = slot_rd + 1 == NS ? 0 : slot_rd + 1;
    }

    l = swap_add(l);
    const unsigned ooff = (unsigned)(pw + r32) * QSTRIDE + (unsigned)(2 * grp + hh) * ROWB + h * 16;
    const __amdgpu_buffer_rsrc_t orsrc = __builtin_amdgcn_make_buffer_rsrc((void*)(op + (size_t)s0 * QSTRIDE), 0, (unsigned)L * QSTRIDE, 0x00020000);
    auto store_block = [&](int c, const f32x16& x) {
#pragma unroll
        for (int gq = 0; gq < 4; gq += 2) {
            unsigned a0 = cvtpk(x[4 * gq + 0], x[4 * gq + 1]), a1 = cvtpk(x[4 * gq + 2], x[4 * gq + 3]);
            unsigned b0 = cvtpk(x[4 * gq + 4], x[4 * gq + 5]), b1 = cvtpk(x[4 * gq + 6], x[4 * gq + 7]);
            auto r0 = __builtin_amdgcn_permlane32_swap(a0, b0, false, false);
            auto r1 = __builtin_amdgcn_permlane32_swap(a1, b1, false, false);
            u32x4 wv = {r0[0], r1[0], r0[1], r1[1]};
            __builtin_amdgcn_raw_buffer_store_b128(wv, orsrc, ooff + (32 * c + 8 * gq) * 2, 0, 0);
        }
    };
    if (S == 1) {
        if (tlast >= 0) {
            const float inv = __builtin_amdgcn_rcpf(l);
#pragma unroll
            for (int c = 0; c < 6; ++c) { f32x16 x = o[c] * inv; store_block(c, x); }
        }
    } else {
        barrier();
        lds_char_ptr mine = smem + w * SLOT, other = smem + (w ^ 2) * SLOT;
        {
            f32x2 ml = {m, l};
            *(lds_f32x2_ptr)(smem + 4 * SLOT + w * 512 + lane * 8) = ml;
        }
#pragma unroll
        for (int c = 0; c < 3; ++c) {
            const f32x16 x = sig ? o[c] : o[3 + c];
#pragma unroll
            for (int g = 0; g < 4; ++g) { f32x4 y = {x[4 * g], x[4 * g + 1], x[4 * g + 2], x[4 * g + 3]}; *(lds_f32x4_ptr)(mine + (c * 4 + g) * 1024 + lane * 16) = y; }
        }
        barrier();
        const f32x2 mlo = *(lds_f32x2_ptr)(smem + 4 * SLOT + (w ^ 2) * 512 + lane * 8);
        const float mx = fmaxf(m, mlo[0]);
        const float a0 = __builtin_amdgcn_exp2f((m - mx) * SCALE_LOG2E), a1 = __builtin_amdgcn_exp2f((mlo[0] - mx) * SCALE_LOG2E);
        const float inv = __builtin_amdgcn_rcpf(l * a0 + mlo[1] * a1);
        const float f0 = a0 * inv, f1 = a1 * inv;
#pragma unroll
        for (int c = 0; c < 3; ++c) {
            const f32x16 x = sig ? o[3 + c] : o[c];
            f32x16 z;
#pragma unroll
            for (int g = 0; g < 4; ++g) {
                const f32x4 y = *(lds_f32x4_ptr)(other + (c * 4 + g) * 1024 + lane * 16);
#pragma unroll
                for (int j = 0; j < 4; ++j) z[4 * g + j] = x[4 * g + j] * f0 + y[j] * f1;
            }
            store_block(3 * sig + c, z);
        }
    }
    (void)osrd;
}

__global__ __launch_bounds__(256, 2) void attn_varlen_kernel(const char* __restrict__ qp, const char* __restrict__ kp, const char* __restrict__ vp,
                                                             const int* __restrict__ cu, char* __restrict__ op, int nseq) {
    __shared__ __attribute__((aligned(16))) char smem_[LDS_BYTES];
    const int grp = blockIdx.x & 7;
    const int k = blockIdx.x >> 3;
    const int lane = threadIdx.x & 63;

    const int nch = (nseq + 63) >> 6;
    auto load_chunk = [&](int ch, int& c0, int& len) {
        const int s = ch * 64 + lane;
        c0 = 0; len = 0;
        if (s < nseq) { c0 = cu[s]; len = cu[s + 1] - c0; }
    };
    int c0_0, len_0;
    load_chunk(0, c0_0, len_0);
    int nmax = 0, W = 0;
    for (int ch = 0; ch < nch; ++ch) {
        int c0 = c0_0, len = len_0;
        if (ch > 0) load_chunk(ch, c0, len);
        const int cnt = min(64, nseq - ch * 64);
        for (int s = 0; s < cnt; ++s) {
            const int n = (__builtin_amdgcn_readlane(len, s) + 31) >> 5;
            nmax = max(nmax, n);
            const int M = (n + 1) >> 1;
            W += (M - 1) * M + n;
        }
    }
    int B = max((nmax + 1) >> 1, (W * 19) >> 10);
    B = (B + 1) & ~1;
    if (B < 2) B = 2;
    int N = 0;
    for (int ch = 0; ch < nch; ++ch) {
        int c0 = c0_0, len = len_0;
        if (ch > 0) load_chunk(ch, c0, len);
        const int cnt = min(64, nseq - ch * 64);
        for (int s = 0; s < cnt; ++s) {
            const int n = (__builtin_amdgcn_readlane(len, s) + 31) >> 5;
            N += max(n - B, 0) + min(B >> 1, (n + 1) >> 1);
        }
    }
    int rank = k;
    if (k < CUS_PER_XCD) { const int N2 = min(max(N - CUS_PER_XCD, 0), CUS_PER_XCD); rank = (k < N2) ? CUS_PER_XCD - 1 - k : k - N2; }
    if (rank >= N) return;
    int jx = nmax, my = min(B >> 1, (nmax + 1) >> 1), cum = 0;
    int thr, isx;
    for (;;) {
        isx = (jx > B) && (((jx + 1) >> 1) >= 2 * my);
        thr = isx ? 32 * (jx - 1) : 64 * (my - 1);
        int cnt = __popcll(__ballot(len_0 > thr));
        for (int ch = 1; ch < nch; ++ch) { int c0, len; load_chunk(ch, c0, len); cnt += __popcll(__ballot(len > thr)); }
        if (rank < cum + cnt) break;
        cum += cnt;
        if (isx) --jx; else --my;
    }
    int idx = rank - cum, s0 = 0, L = 0;
    for (int ch = 0; ch < nch; ++ch) {
        int c0 = c0_0, len = len_0;
        if (ch > 0) load_chunk(ch, c0, len);
        unsigned long long mask = __ballot(len > thr);
        const int cnt = __popcll(mask);
        if (idx < cnt) {
            for (int i = 0; i < idx; ++i) mask &= mask - 1;
            const int s = __ffsll((long long)mask) - 1;
            s0 = __builtin_amdgcn_readlane(c0, s); L = __builtin_amdgcn_readlane(len, s);
            break;
        }
        idx -= cnt;
    }
    s0 = rfl(s0); L = rfl(L);
    const int p0 = rfl(thr);
    lds_char_ptr smem = (lds_char_ptr)smem_;
    if (isx) {
        const int n = (min(p0 + 32, L) + 31) >> 5;
        attn_item<2>(qp, kp, vp, op, grp, s0, L, p0, n, smem);
    } else {
        const int n = (min(p0 + 64, L) + 31) >> 5;
        attn_item<1>(qp, kp, vp, op, grp, s0, L, p0, n, smem);
    }
}

extern "C" void kernel_launch(void* const* d_in, const int* in_sizes, int n_in, void* d_out, int out_size, void* d_ws, size_t ws_size,
                              hipStream_t stream) {
    (void)n_in; (void)out_size; (void)d_ws; (void)ws_size;
    const int T = in_sizes[0] / (HQ * D);
    const int nseq = in_sizes[3] - 1;
    const int per_head = T / 32 + nseq + 1;
    attn_varlen_kernel<<<dim3(8 * per_head), dim3(256), 0, stream>>>((const char*)d_in[0], (const char*)d_in[1], (const char*)d_in[2],
                                                                    (const int*)d_in[3], (char*)d_out, nseq);
}
```

```cpp
#include <hip/hip_runtime.h>
#include <hip/hip_bf16.h>
#include <stdint.h>

typedef __bf16 bf16x8 __attribute__((ext_vector_type(8)));
typedef __bf16 bf16x2 __attribute__((ext_vector_type(2)));
typedef short s16x4 __attribute__((ext_vector_type(4)));
typedef short s16x8 __attribute__((ext_vector_type(8)));
typedef float f32x2 __attribute__((ext_vector_type(2)));
typedef float f32x4 __attribute__((ext_vector_type(4)));
typedef float f32x16 __attribute__((ext_vector_type(16)));
typedef unsigned u32x2 __attribute__((ext_vector_type(2)));
typedef unsigned u32x4 __attribute__((ext_vector_type(4)));
#define LDSP(T) __attribute__((address_space(3))) T*
typedef LDSP(char) lds_char_ptr;

#define DI __device__ __forceinline__
#define STAMP(i) do {} while (0)
#define STARG
#define STPASS

constexpr int HQ = 16, HKV = 8, D = 192;
constexpr unsigned QSTRIDE = HQ * D * 2;
constexpr unsigned KSTRIDE = HKV * D * 2;
constexpr unsigned ROWB = D * 2;
constexpr unsigned SLOT = 32 * ROWB;
constexpr unsigned ORS = 400;
constexpr unsigned LDS_BYTES = 77824;
constexpr float SCALE_LOG2E = 0.07216878364870322f * 1.4426950408889634f;
constexpr float THR_RAW = 8.0f / SCALE_LOG2E;
constexpr int CUS_PER_XCD = 32;
constexpr int BSPLIT = 8;

DI int rfl(int x) { return __builtin_amdgcn_readfirstlane(x); }
DI unsigned cvtpk(float lo, float hi) {
    f32x2 v = {lo, hi}; bf16x2 b = __builtin_convertvector(v, bf16x2); return __builtin_bit_cast(unsigned, b);
}
DI float max2f(float a, float b) { return __builtin_elementwise_maximum(a, b); }
DI float max3f(float a, float b, float c) { return max2f(max2f(a, b), c); }
DI float swap_max(float x) {
    auto r = __builtin_amdgcn_permlane32_swap(__float_as_uint(x), __float_as_uint(x), false, false);
    return max2f(__uint_as_float(r[0]), __uint_as_float(r[1]));
}
DI float swap_add(float x) {
    auto r = __builtin_amdgcn_permlane32_swap(__float_as_uint(x), __float_as_uint(x), false, false);
    return __uint_as_float(r[0]) + __uint_as_float(r[1]);
}
DI u32x4 make_srd(const void* base, unsigned nbytes) {
    unsigned long long a = (unsigned long long)base;
    u32x4 s; s[0] = rfl((unsigned)a); s[1] = rfl((unsigned)(a >> 32) & 0xffffu); s[2] = rfl(nbytes); s[3] = 0x00020000u;
    return s;
}
template <int IMM> DI void dma16(unsigned m0v, unsigned voff, u32x4 srd, unsigned soff) {
    asm volatile("s_mov_b32 m0, %0\n\ts_nop 0\n\tbuffer_load_dwordx4 %1, %2, %3 offen offset:%4 lds"
                 :: "s"(m0v), "v"(voff), "s"(srd), "s"(soff), "i"(IMM) : "memory");
}
template <int IMM, int NEXT> DI void dma16c(unsigned voff, u32x4 srd, unsigned soff, unsigned nbase) {
    asm volatile("buffer_load_dwordx4 %0, %1, %2 offen offset:%3 lds\n\ts_add_u32 m0, %4, %5"
                 :: "v"(voff), "s"(srd), "s"(soff), "i"(IMM), "s"(nbase), "i"(NEXT) : "memory", "scc");
}
template <int IMM> DI void dma16e(unsigned voff, u32x4 srd, unsigned soff) {
    asm volatile("buffer_load_dwordx4 %0, %1, %2 offen offset:%3 lds" :: "v"(voff), "s"(srd), "s"(soff), "i"(IMM) : "memory");
}
template <int N> DI void wait_vm() { asm volatile("s_waitcnt vmcnt(%0)" :: "i"(N) : "memory"); }
DI void barrier() { asm volatile("s_waitcnt lgkmcnt(0)\n\ts_barrier" ::: "memory"); }
#define SB() __builtin_amdgcn_sched_barrier(0)
#define MFMA(a, b, c) __builtin_amdgcn_mfma_f32_32x32x16_bf16((a), (b), (c), 0, 0, 0)

DI bf16x8 ldk(unsigned addr) { return *(LDSP(bf16x8))(size_t)addr; }
DI bf16x8 ldv(unsigned addr) {
    const s16x4 lo = __builtin_amdgcn_ds_read_tr16_b64_v4i16((LDSP(s16x4))(size_t)addr);
    const s16x4 hi = __builtin_amdgcn_ds_read_tr16_b64_v4i16((LDSP(s16x4))(size_t)(addr + 1024));
    return __builtin_bit_cast(bf16x8, __builtin_shufflevector(lo, hi, 0, 1, 2, 3, 4, 5, 6, 7));
}

template <int V> struct IC { static constexpr int value = V; };

template <int S>
DI void attn_item(const char* __restrict__ qp, const char* __restrict__ kp, const char* __restrict__ vp, char* __restrict__ op,
                  int grp, int s0, int L, int p0, int n, unsigned lds0 STARG) {
    constexpr int NS = (S == 1) ? 6 : 3;
    constexpr int PW = (S == 1) ? 3 : 6;
    constexpr unsigned TSTEP = 32 * S * KSTRIDE;
    const int tid = threadIdx.x, lane = tid & 63;
    const int w = rfl(tid >> 6);
    const int hh = w & 1, up = w >> 1;
    const int r32 = lane & 31, h = lane >> 5;
    const int sig = (S == 1) ? 0 : up;
    const int pw = (S == 1) ? p0 + 32 * up : p0;
    const int steps = (S == 1) ? n : (n + 1) >> 1;
    const int tdiag = pw >> 5;

    const int kend = min(L, 32 * n);
    const u32x4 ksrd = make_srd(kp + (size_t)s0 * KSTRIDE + grp * ROWB, (unsigned)(kend - 1) * KSTRIDE + ROWB);
    const u32x4 vsrd = make_srd(vp + (size_t)s0 * KSTRIDE + grp * ROWB, (unsigned)(kend - 1) * KSTRIDE + ROWB);
    const u32x4 qsrd = make_srd(qp + (size_t)s0 * QSTRIDE, (unsigned)L * QSTRIDE);

    const unsigned ring = lds0 + sig * NS * SLOT;
    const unsigned mW = ring + ((S == 1) ? w * 1024 : hh * 1024);
    const unsigned drow = (unsigned)(8 * hh + (lane >> 3) + ((S == 1) ? 16 * up : 0)) * KSTRIDE;
    const unsigned voffK = drow + 16u * ((lane & 7) ^ ((hh << 2) | (lane >> 4)));
    const unsigned voffV = drow + 16u * ((lane & 7) ^ (((lane >> 4) & 1) << 2));
    const unsigned voffK2 = voffK + 16 * KSTRIDE, voffV2 = voffV + 16 * KSTRIDE;
#define PIECE(ISV, j, so, mb, MO) do { const u32x4 sr_ = (ISV) ? vsrd : ksrd;                                                            \
        const unsigned v1_ = (ISV) ? voffV : voffK, v2_ = (ISV) ? voffV2 : voffK2;                                                       \
        if (j == 0) dma16c<0, (MO) + 4096 - 128>(v1_, sr_, so, mb);                                                                      \
        else if (j == 1) dma16c<128, (MO) + 8192 - 256>(v1_, sr_, so, mb);                                                               \
        else if (j == 2) { if (S == 1) dma16e<256>(v1_, sr_, so); else dma16c<256, (MO) + 2048>(v1_, sr_, so, mb); }                      \
        else if (j == 3) dma16c<0, (MO) + 2048 + 4096 - 128>(v2_, sr_, so, mb);                                                          \
        else if (j == 4) dma16c<128, (MO) + 2048 + 8192 - 256>(v2_, sr_, so, mb);                                                        \
        else dma16e<256>(v2_, sr_, so); } while (0)
#define SETM0(mb, MO) asm volatile("s_add_u32 m0, %0, %1\n\ts_nop 0" :: "s"(mb), "i"(MO) : "memory", "scc")
#define ELEM(ISV, so, mb, MO) do { SETM0(mb, MO); PIECE(ISV, 0, so, mb, MO); NOP0(); PIECE(ISV, 1, so, mb, MO); NOP0(); PIECE(ISV, 2, so, mb, MO);      \
        if (S == 2) { NOP0(); PIECE(ISV, 3, so, mb, MO); NOP0(); PIECE(ISV, 4, so, mb, MO); NOP0(); PIECE(ISV, 5, so, mb, MO); } } while (0)
#define NOP0() asm volatile("s_nop 0" ::: "memory")
    const unsigned so0 = (unsigned)(32 * sig) * KSTRIDE;

    STAMP(2);
    ELEM(false, so0, mW, 0);
    const unsigned qslot = (S == 1) ? lds0 + (2 + w) * SLOT : lds0 + (3 * hh + 2) * SLOT;
    {
        const unsigned qrow = (unsigned)(2 * grp + hh) * ROWB + (unsigned)pw * QSTRIDE;
        const unsigned l8 = (unsigned)(lane >> 3) * QSTRIDE;
        if (S == 1) {
            const unsigned vq0 = l8 + 16u * ((lane & 7) ^ (lane >> 4)), vq1 = l8 + 8 * QSTRIDE + 16u * ((lane & 7) ^ (4 | (lane >> 4)));
            const unsigned so2 = qrow + 16 * QSTRIDE;
            SETM0(qslot, 0);
            dma16c<0, 4096 - 128>(vq0, qsrd, qrow, qslot); NOP0(); dma16c<128, 8192 - 256>(vq0, qsrd, qrow, qslot); NOP0(); dma16c<256, 1024>(vq0, qsrd, qrow, qslot); NOP0();
            dma16c<0, 1024 + 4096 - 128>(vq1, qsrd, qrow, qslot); NOP0(); dma16c<128, 1024 + 8192 - 256>(vq1, qsrd, qrow, qslot); NOP0(); dma16c<256, 2048>(vq1, qsrd, qrow, qslot); NOP0();
            dma16c<0, 2048 + 4096 - 128>(vq0, qsrd, so2, qslot); NOP0(); dma16c<128, 2048 + 8192 - 256>(vq0, qsrd, so2, qslot); NOP0(); dma16c<256, 3072>(vq0, qsrd, so2, qslot); NOP0();
            dma16c<0, 3072 + 4096 - 128>(vq1, qsrd, so2, qslot); NOP0(); dma16c<128, 3072 + 8192 - 256>(vq1, qsrd, so2, qslot); NOP0(); dma16e<256>(vq1, qsrd, so2);
        } else {
            const unsigned vq = l8 + (unsigned)(8 * up) * QSTRIDE + 16u * ((lane & 7) ^ ((up << 2) | (lane >> 4))), vq2 = vq + 16 * QSTRIDE;
            const unsigned qm = qslot + up * 1024;
            SETM0(qm, 0);
            dma16c<0, 4096 - 128>(vq, qsrd, qrow, qm); NOP0(); dma16c<128, 8192 - 256>(vq, qsrd, qrow, qm); NOP0(); dma16c<256, 2048>(vq, qsrd, qrow, qm); NOP0();
            dma16c<0, 2048 + 4096 - 128>(vq2, qsrd, qrow, qm); NOP0(); dma16c<128, 2048 + 8192 - 256>(vq2, qsrd, qrow, qm); NOP0(); dma16e<256>(vq2, qsrd, qrow);
        }
    }
    ELEM(false, so0 + TSTEP, mW, SLOT);

    const int mysteps = (pw >= L) ? 0 : ((S == 1) ? tdiag + 1 : ((n - sig + 1) >> 1));
    const int itdiag = (S == 1) ? tdiag : (((tdiag - sig) & 1) ? -1 : (tdiag - sig) >> 1);
    const unsigned kb0 = r32 * 128 + 16 * (h ^ ((r32 >> 1) & 7));
    constexpr unsigned KB = (S == 1) ? SLOT : 0;
    unsigned kA = ring + KB + kb0, kBb = kA ^ 32, kC = kA ^ 64, kD = kA ^ 96;
    const int q4 = (lane & 15) >> 2, p4 = lane & 3, blk = (lane >> 4) & 1;
    unsigned vA = ring + (4 * h + q4) * 128 + 64 * (q4 >> 1) + 32 * blk + 8 * p4, vB = vA ^ 64;
    f32x16 o[6];
#pragma unroll
    for (int c = 0; c < 6; ++c) { o[c] = f32x16{}; asm volatile("" : "+v"(o[c])); }
    float m = -1e30f, l = 0.f, alpha = 1.f, mthr, mc;
    int pend = 0;
    int dq0 = pw + r32 - 4 * h;
    asm volatile("" : "+v"(kA), "+v"(kBb), "+v"(kC), "+v"(kD), "+v"(vA), "+v"(vB), "+v"(dq0));
    f32x16 s;
    float x[16];
#define KRD(i, OFF) ldk((((i) & 3) == 0 ? kA : ((i) & 3) == 1 ? kBb : ((i) & 3) == 2 ? kC : kD) + (OFF) + ((i) >> 2) * 4096)
#define VRD(i, OFF) ldv(((((i) % 6) & 1) ? vB : vA) + (OFF) + (((i) % 6) >> 1) * 4096 + ((i) / 6) * 2048)

    wait_vm<PW>();
    STAMP(4);
    barrier();
    bf16x8 q[12];
    {
        const unsigned a0 = qslot + kb0;
#pragma unroll
        for (int i = 0; i < 12; ++i) q[i] = ldk((a0 ^ (32 * (i & 3))) + (i >> 2) * 4096);
    }
    bf16x8 kf[12];
    {
        const unsigned a0 = ring + kb0;
#pragma unroll
        for (int i = 0; i < 6; ++i) kf[i] = ldk((a0 ^ (32 * (i & 3))) + (i >> 2) * 4096);
    }
    barrier();
    ELEM(true, so0, mW, 2 * SLOT);
    if (S == 1) {
        ELEM(false, so0 + 2 * TSTEP, mW, 3 * SLOT);
        ELEM(true, so0 + TSTEP, mW, 4 * SLOT);
        ELEM(false, so0 + 3 * TSTEP, mW, 5 * SLOT);
    }
    STAMP(5);

#define GATE(mb, MO) do { __builtin_amdgcn_s_waitcnt(((NS - 2) * PW) | 0x70);                                                            \
        asm volatile("s_barrier\n\ts_add_u32 m0, %0, %1" :: "s"(mb), "i"(MO) : "memory", "scc"); } while (0)
#define PIN1(a) asm volatile("" : "+v"(a))
#define PIN2(a, b) asm volatile("" : "+v"(a), "+v"(b))
#define PIN4(a, b, c, d) asm volatile("" : "+v"(a), "+v"(b), "+v"(c), "+v"(d))
    auto mask_diag = [&]() {
        int dq = dq0 - 32 * tdiag;
        asm volatile("" : "+v"(dq));
#pragma unroll
        for (int i = 0; i < 16; ++i) { const int c = (i & 3) + 8 * (i >> 2); s[i] = (c <= dq) ? s[i] : -__builtin_inff(); }
    };

    if (mysteps > 0) {
        const unsigned a0 = ring + kb0;
#pragma unroll
        for (int i = 6; i < 12; ++i) kf[i] = ldk((a0 ^ (32 * (i & 3))) + (i >> 2) * 4096);
        s = MFMA(kf[0], q[0], f32x16{});
#pragma unroll
        for (int i = 1; i < 12; ++i) s = MFMA(kf[i], q[i], s);
    }
    GATE(mW, 0);
    constexpr unsigned KS0 = (S == 1) ? 0 : SLOT;
    kf[0] = KRD(0, KS0);
    PIECE(S == 1, 0, so0 + 2 * TSTEP, mW, 0); kf[1] = KRD(1, KS0);
    PIECE(S == 1, 1, so0 + 2 * TSTEP, mW, 0); kf[2] = KRD(2, KS0);
    PIECE(S == 1, 2, so0 + 2 * TSTEP, mW, 0); kf[3] = KRD(3, KS0);
    if (S == 2) PIECE(false, 3, so0 + 2 * TSTEP, mW, 0);
    kf[4] = KRD(4, KS0);
    if (S == 2) { PIECE(false, 4, so0 + 2 * TSTEP, mW, 0); NOP0(); PIECE(false, 5, so0 + 2 * TSTEP, mW, 0); }
    if (mysteps > 0) {
        if (__builtin_expect(itdiag == 0, 0)) mask_diag();
        float a = max3f(s[0], s[1], s[2]), b = max3f(s[8], s[9], s[10]);
        a = max3f(a, s[3], s[4]); b = max3f(b, s[11], s[12]);
        a = max3f(a, s[5], s[6]); b = max3f(b, s[13], s[14]);
        a = max3f(a, s[7], s[15]);
        m = max2f(m, swap_max(max2f(a, b)));
    }
    mthr = m + THR_RAW; mc = -m * SCALE_LOG2E;
#pragma unroll
    for (int i = 0; i < 16; ++i) x[i] = fmaf(s[i], SCALE_LOG2E, mc);
    STAMP(6);

    int it = 0;
    unsigned soA = so0 + ((S == 1) ? 4 : 1) * TSTEP, soB = so0 + 3 * TSTEP;
    u32x4 pa, pb;
#define EXP2X(i, j) do { x[i] = __builtin_amdgcn_exp2f(x[i]); x[j] = __builtin_amdgcn_exp2f(x[j]); PIN2(x[i], x[j]); } while (0)
#define FMAX2(i, j) do { x[i] = fmaf(s[i], SCALE_LOG2E, mc); x[j] = fmaf(s[j], SCALE_LOG2E, mc); PIN2(x[i], x[j]); } while (0)
    auto iter = [&](auto PHC) {
        constexpr int PH = decltype(PHC)::value, PN = (PH + 1) % 3;
        constexpr unsigned SK = ((S == 1) ? 1 + 2 * PH : (PH == 0 ? 1 : PH == 1 ? 0 : 2)) * SLOT;
        constexpr unsigned SV = ((S == 1) ? (2 + 2 * PH) % 6 : 2 - PH) * SLOT;
        constexpr unsigned SKN = ((S == 1) ? 1 + 2 * PN : (PN == 0 ? 1 : PN == 1 ? 0 : 2)) * SLOT;
        constexpr unsigned RK = SK - KB, RKN = SKN - KB;
        float ls0, ls1;
        PIN2(kf[0], kf[1]);
        kf[5] = KRD(5, RK);
        s = MFMA(kf[0], q[0], f32x16{}); EXP2X(0, 1); SB();
        kf[6] = KRD(6, RK);
        s = MFMA(kf[1], q[1], s); EXP2X(2, 3); SB();
        PIN2(kf[2], kf[3]);
        kf[7] = KRD(7, RK);
        s = MFMA(kf[2], q[2], s); EXP2X(4, 5); SB();
        kf[8] = KRD(8, RK);
        s = MFMA(kf[3], q[3], s); EXP2X(6, 7); SB();
        PIN2(kf[4], kf[5]);
        kf[9] = KRD(9, RK);
        s = MFMA(kf[4], q[4], s); EXP2X(8, 9); ls0 = x[0] + x[2]; ls1 = x[1] + x[3]; PIN2(ls0, ls1); SB();
        kf[10] = KRD(10, RK); kf[11] = KRD(11, RK);
        s = MFMA(kf[5], q[5], s); EXP2X(10, 11); ls0 += x[4]; ls1 += x[5]; PIN2(ls0, ls1); SB();
        PIN2(kf[6], kf[7]);
        s = MFMA(kf[6], q[6], s); EXP2X(12, 13); pa[0] = cvtpk(x[0], x[1]); pa[1] = cvtpk(x[2], x[3]); PIN2(pa[0], pa[1]); SB();
        s = MFMA(kf[7], q[7], s); EXP2X(14, 15); pa[2] = cvtpk(x[4], x[5]); pa[3] = cvtpk(x[6], x[7]); PIN2(pa[2], pa[3]); SB();
        GATE(mW, SK);
        bf16x8 vf[12];
        vf[0] = VRD(0, SV); SB();
        s = MFMA(kf[8], q[8], s); vf[1] = VRD(1, SV); ls0 += x[6]; ls1 += x[7]; PIN2(ls0, ls1); SB();
        s = MFMA(kf[9], q[9], s); vf[2] = VRD(2, SV); PIECE(S == 2, 0, soA, mW, SK); ls0 += x[8]; ls1 += x[9]; PIN2(ls0, ls1); SB();
        s = MFMA(kf[10], q[10], s); vf[3] = VRD(3, SV); PIECE(S == 2, 1, soA, mW, SK); ls0 += x[10]; ls1 += x[11]; PIN2(ls0, ls1); SB();
        s = MFMA(kf[11], q[11], s); vf[4] = VRD(4, SV); PIECE(S == 2, 2, soA, mW, SK); ls0 += x[12]; ls1 += x[13]; PIN2(ls0, ls1); SB();
        const bf16x8 p0f = __builtin_bit_cast(bf16x8, pa);
        PIN2(vf[0], vf[1]);
        vf[5] = VRD(5, SV);
        o[0] = MFMA(vf[0], p0f, o[0]); if (S == 2) PIECE(true, 3, soA, mW, SK);
        pb[0] = cvtpk(x[8], x[9]); pb[1] = cvtpk(x[10], x[11]); PIN2(pb[0], pb[1]); ls0 += x[14]; ls1 += x[15]; PIN2(ls0, ls1); SB();
        vf[6] = VRD(6, SV);
        o[1] = MFMA(vf[1], p0f, o[1]); if (S == 2) PIECE(true, 4, soA, mW, SK);
        pb[2] = cvtpk(x[12], x[13]); pb[3] = cvtpk(x[14], x[15]); PIN2(pb[2], pb[3]); l += ls0; PIN1(l); SB();
        PIN2(vf[2], vf[3]);
        vf[7] = VRD(7, SV);
        o[2] = MFMA(vf[2], p0f, o[2]); if (S == 2) PIECE(true, 5, soA, mW, SK);
        l += ls1; PIN1(l); SB();
        const bf16x8 p1f = __builtin_bit_cast(bf16x8, pb);
        vf[8] = VRD(8, SV);
        o[3] = MFMA(vf[3], p0f, o[3]); SB();
        if (__builtin_expect(it + 1 == itdiag, 0)) mask_diag();
        float ma = max3f(s[0], s[1], s[2]), mb = max3f(s[8], s[9], s[10]);
        ma = max3f(ma, s[3], s[4]); mb = max3f(mb, s[11], s[12]);
        ma = max3f(ma, s[5], s[6]); mb = max3f(mb, s[13], s[14]); PIN2(ma, mb); SB();
        PIN2(vf[4], vf[5]);
        vf[9] = VRD(9, SV);
        o[4] = MFMA(vf[4], p0f, o[4]);
        ma = max3f(ma, s[7], s[15]);
        float pm = swap_max(max2f(ma, mb)); PIN1(pm); SB();
        vf[10] = VRD(10, SV); vf[11] = VRD(11, SV);
        o[5] = MFMA(vf[5], p0f, o[5]); SB();
        if (__builtin_expect(__any(pm > mthr), 0)) {
            const float mn = max2f(m, pm);
            alpha = __builtin_amdgcn_exp2f((m - mn) * SCALE_LOG2E);
            m = mn; mthr = m + THR_RAW; mc = -m * SCALE_LOG2E; pend = 1;
        }
        PIN2(vf[6], vf[7]);
        o[0] = MFMA(vf[6], p1f, o[0]); FMAX2(0, 1); FMAX2(2, 3); FMAX2(4, 5); SB();
        o[1] = MFMA(vf[7], p1f, o[1]); FMAX2(6, 7); FMAX2(8, 9); FMAX2(10, 11); SB();
        GATE(mW, SV);
        kf[0] = KRD(0, RKN); SB();
        o[2] = MFMA(vf[8], p1f, o[2]); kf[1] = KRD(1, RKN); PIECE(S == 1, 0, soB, mW, SV); FMAX2(12, 13); SB();
        o[3] = MFMA(vf[9], p1f, o[3]); kf[2] = KRD(2, RKN); PIECE(S == 1, 1, soB, mW, SV); FMAX2(14, 15); SB();
        o[4] = MFMA(vf[10], p1f, o[4]); PIECE(S == 1, 2, soB, mW, SV); kf[3] = KRD(3, RKN); if (S == 2) PIECE(false, 3, soB, mW, SV); SB();
        o[5] = MFMA(vf[11], p1f, o[5]); if (S == 2) PIECE(false, 4, soB, mW, SV); kf[4] = KRD(4, RKN); if (S == 2) PIECE(false, 5, soB, mW, SV); SB();
        soA += TSTEP; soB += TSTEP;
        if (__builtin_expect(pend, 0)) {
            l *= alpha;
#pragma unroll
            for (int c = 0; c < 6; ++c) o[c] *= alpha;
            pend = 0;
        }
        ++it;
    };
    if (it + 1 < mysteps) {
        for (;;) {
            iter(IC<0>{}); if (it + 1 >= mysteps) break;
            iter(IC<1>{}); if (it + 1 >= mysteps) break;
            iter(IC<2>{}); if (it + 1 >= mysteps) break;
        }
    }
    STAMP(7);
    const int ph = it % 3;
    unsigned tk = ((S == 1) ? 1 + 2 * ph : (ph == 0 ? 1 : ph == 1 ? 0 : 2)) * SLOT;
    unsigned tv = ((S == 1) ? (ph == 2 ? 0 : 2 + 2 * ph) : 2 - ph) * SLOT;
    auto adv2 = [&](unsigned a) { const unsigned t = a + 2 * SLOT; return t >= NS * SLOT ? t - NS * SLOT : t; };
    if (mysteps > 0) {
        const unsigned mk = mW + tk, mv = mW + tv;
        GATE(mk, 0);
        const unsigned va = vA + tv, vb = vB + tv;
        bf16x8 vf[12];
#define VRT(i) ldv(((((i) % 6) & 1) ? vb : va) + (((i) % 6) >> 1) * 4096 + ((i) / 6) * 2048)
        vf[0] = VRT(0); PIECE(S == 2, 0, soA, mk, 0); vf[1] = VRT(1); PIECE(S == 2, 1, soA, mk, 0); vf[2] = VRT(2); PIECE(S == 2, 2, soA, mk, 0);
        vf[3] = VRT(3); if (S == 2) PIECE(true, 3, soA, mk, 0); vf[4] = VRT(4); if (S == 2) PIECE(true, 4, soA, mk, 0); vf[5] = VRT(5); if (S == 2) PIECE(true, 5, soA, mk, 0);
        float ls0 = 0.f, ls1 = 0.f;
#pragma unroll
        for (int i = 0; i < 16; i += 2) { x[i] = __builtin_amdgcn_exp2f(x[i]); x[i + 1] = __builtin_amdgcn_exp2f(x[i + 1]); ls0 += x[i]; ls1 += x[i + 1]; }
        l += ls0 + ls1;
#pragma unroll
        for (int i = 0; i < 4; ++i) { pa[i] = cvtpk(x[2 * i], x[2 * i + 1]); pb[i] = cvtpk(x[8 + 2 * i], x[9 + 2 * i]); }
        PIN4(pa[0], pa[1], pa[2], pa[3]); PIN4(pb[0], pb[1], pb[2], pb[3]);
        const bf16x8 p0f = __builtin_bit_cast(bf16x8, pa), p1f = __builtin_bit_cast(bf16x8, pb);
        SB();
        vf[6] = VRT(6); vf[7] = VRT(7);
        o[0] = MFMA(vf[0], p0f, o[0]); SB();
        vf[8] = VRT(8); vf[9] = VRT(9);
        o[1] = MFMA(vf[1], p0f, o[1]); SB();
        vf[10] = VRT(10); vf[11] = VRT(11);
        o[2] = MFMA(vf[2], p0f, o[2]); SB();
        o[3] = MFMA(vf[3], p0f, o[3]); o[4] = MFMA(vf[4], p0f, o[4]); o[5] = MFMA(vf[5], p0f, o[5]);
        o[0] = MFMA(vf[6], p1f, o[0]); o[1] = MFMA(vf[7], p1f, o[1]); SB();
        if (it + 1 < steps) {
            GATE(mv, 0);
            NOP0();
            PIECE(S == 1, 0, soB, mv, 0); NOP0(); PIECE(S == 1, 1, soB, mv, 0); NOP0(); PIECE(S == 1, 2, soB, mv, 0);
            if (S == 2) { NOP0(); PIECE(false, 3, soB, mv, 0); NOP0(); PIECE(false, 4, soB, mv, 0); NOP0(); PIECE(false, 5, soB, mv, 0); }
        }
        o[2] = MFMA(vf[8], p1f, o[2]); o[3] = MFMA(vf[9], p1f, o[3]); o[4] = MFMA(vf[10], p1f, o[4]); o[5] = MFMA(vf[11], p1f, o[5]);
        tk = adv2(tk); tv = adv2(tv); soA += TSTEP; soB += TSTEP;
        ++it;
    }
    STAMP(8);
    for (; it < steps; ++it) {
        const unsigned mk = mW + tk, mv = mW + tv;
        GATE(mk, 0);
        NOP0();
        PIECE(S == 2, 0, soA, mk, 0); NOP0(); PIECE(S == 2, 1, soA, mk, 0); NOP0(); PIECE(S == 2, 2, soA, mk, 0);
        if (S == 2) { NOP0(); PIECE(true, 3, soA, mk, 0); NOP0(); PIECE(true, 4, soA, mk, 0); NOP0(); PIECE(true, 5, soA, mk, 0); }
        if (it + 1 < steps) {
            GATE(mv, 0);
            NOP0();
            PIECE(S == 1, 0, soB, mv, 0); NOP0(); PIECE(S == 1, 1, soB, mv, 0); NOP0(); PIECE(S == 1, 2, soB, mv, 0);
            if (S == 2) { NOP0(); PIECE(false, 3, soB, mv, 0); NOP0(); PIECE(false, 4, soB, mv, 0); NOP0(); PIECE(false, 5, soB, mv, 0); }
        }
        tk = adv2(tk); tv = adv2(tv); soA += TSTEP; soB += TSTEP;
    }

    STAMP(10);
    l = swap_add(l);
    const __amdgpu_buffer_rsrc_t orsrc = __builtin_amdgcn_make_buffer_rsrc((void*)(op + (size_t)s0 * QSTRIDE), 0, (unsigned)L * QSTRIDE, 0x00020000);
    const unsigned stw = r32 * ORS + 8 * h;
    auto stage_block = [&](unsigned base, int c, const f32x16& z) {
#pragma unroll
        for (int g = 0; g < 4; ++g) {
            u32x2 v = {cvtpk(z[4 * g], z[4 * g + 1]), cvtpk(z[4 * g + 2], z[4 * g + 3])};
            *(LDSP(u32x2))(size_t)(base + (32 * c + 8 * g) * 2) = v;
        }
    };
    unsigned lrd[3], gof[3];
#pragma unroll
    for (int j = 0; j < 3; ++j) { const unsigned idx = 64 * j + lane, row = (idx * 2731) >> 16, ch = idx - 24 * row; lrd[j] = row * ORS + ch * 16; gof[j] = row * QSTRIDE + ch * 16; }
    wait_vm<0>();
    barrier();
    STAMP(11);
    if (S == 1) {
        if (mysteps > 0) {
            const unsigned stg = lds0 + w * (32 * ORS);
            const float inv = __builtin_amdgcn_rcpf(l);
#pragma unroll
            for (int c = 0; c < 6; ++c) { const f32x16 z = o[c] * inv; stage_block(stg + stw, c, z); }
            STAMP(15);
            const unsigned obase = (unsigned)pw * QSTRIDE + (unsigned)(2 * grp + hh) * ROWB;
#pragma unroll
            for (int k = 0; k < 4; ++k)
#pragma unroll
                for (int j = 0; j < 3; ++j) {
                    const u32x4 v = *(LDSP(u32x4))(size_t)(stg + lrd[j] + 8 * k * ORS);
                    __builtin_amdgcn_raw_buffer_store_b128(v, orsrc, gof[j], obase + 8 * k * QSTRIDE, 0);
                }
        }
    } else {
        const unsigned mine = lds0 + w * SLOT + lane * 16, other = lds0 + (w ^ 2) * SLOT + lane * 16;
        {
            f32x2 ml = {m, l};
            *(LDSP(f32x2))(size_t)(lds0 + 4 * SLOT + w * 512 + lane * 8) = ml;
        }
        auto send = [&](const f32x16& z, int c) {
#pragma unroll
            for (int g = 0; g < 4; ++g) { f32x4 y = {z[4 * g], z[4 * g + 1], z[4 * g + 2], z[4 * g + 3]}; *(LDSP(f32x4))(size_t)(mine + (c * 4 + g) * 1024) = y; }
        };
        if (sig) { send(o[0], 0); send(o[1], 1); send(o[2], 2); } else { send(o[3], 0); send(o[4], 1); send(o[5], 2); }
        STAMP(12);
        barrier();
        STAMP(13);
        const f32x2 mlo = *(LDSP(f32x2))(size_t)(lds0 + 4 * SLOT + (w ^ 2) * 512 + lane * 8);
        const float mx = max2f(m, mlo[0]);
        const float a0 = __builtin_amdgcn_exp2f((m - mx) * SCALE_LOG2E), a1 = __builtin_amdgcn_exp2f((mlo[0] - mx) * SCALE_LOG2E);
        const float inv = __builtin_amdgcn_rcpf(l * a0 + mlo[1] * a1);
        const float f0 = a0 * inv, f1 = a1 * inv;
        const unsigned stg = lds0 + 51200 + hh * (32 * ORS);
        auto merge = [&](const f32x16& xo, int c, int cd) {
            f32x16 z;
#pragma unroll
            for (int g = 0; g < 4; ++g) {
                const f32x4 y = *(LDSP(f32x4))(size_t)(other + (c * 4 + g) * 1024);
                const f32x4 t = {xo[4 * g], xo[4 * g + 1], xo[4 * g + 2], xo[4 * g + 3]};
                const f32x4 u = t * f0 + y * f1;
                z[4 * g] = u[0]; z[4 * g + 1] = u[1]; z[4 * g + 2] = u[2]; z[4 * g + 3] = u[3];
            }
            stage_block(stg + stw, cd, z);
        };
        if (sig) { merge(o[3], 0, 3); merge(o[4], 1, 4); merge(o[5], 2, 5); } else { merge(o[0], 0, 0); merge(o[1], 1, 1); merge(o[2], 2, 2); }
        STAMP(14);
        barrier();
        STAMP(15);
        const unsigned obase = (unsigned)(pw + 16 * sig) * QSTRIDE + (unsigned)(2 * grp + hh) * ROWB;
        const unsigned stgr = stg + 16 * sig * ORS;
#pragma unroll
        for (int k = 0; k < 2; ++k)
#pragma unroll
            for (int j = 0; j < 3; ++j) {
                const u32x4 v = *(LDSP(u32x4))(size_t)(stgr + lrd[j] + 8 * k * ORS);
                __builtin_amdgcn_raw_buffer_store_b128(v, orsrc, gof[j], obase + 8 * k * QSTRIDE, 0);
            }
    }
    STAMP(16);
}

template <int CTRL> DI int dpp_mov(int old, int x) { return __builtin_amdgcn_update_dpp(old, x, CTRL, 0xf, 0xf, false); }
DI int wave_incl_scan(int x) {
    x += dpp_mov<0x111>(0, x);
    x += dpp_mov<0x112>(0, x);
    x += dpp_mov<0x114>(0, x);
    x += dpp_mov<0x118>(0, x);
    x += __builtin_amdgcn_update_dpp(0, x, 0x142, 0xa, 0xf, false);
    x += __builtin_amdgcn_update_dpp(0, x, 0x143, 0xc, 0xf, false);
    return x;
}
DI int wave_max_nonneg(int x) {
    x = max(x, dpp_mov<0x111>(0, x));
    x = max(x, dpp_mov<0x112>(0, x));
    x = max(x, dpp_mov<0x114>(0, x));
    x = max(x, dpp_mov<0x118>(0, x));
    x = max(x, __builtin_amdgcn_update_dpp(0, x, 0x142, 0xa, 0xf, false));
    x = max(x, __builtin_amdgcn_update_dpp(0, x, 0x143, 0xc, 0xf, false));
    return __builtin_amdgcn_readlane(x, 63);
}

DI bool decode_slow(const int* __restrict__ cu, int nseq, int k, int& s0, int& L, int& p0, int& isx) {
    int nmax = 0;
    for (int s = 0; s < nseq; ++s) nmax = max(nmax, (cu[s + 1] - cu[s] + 31) >> 5);
    const int B = BSPLIT;
    int N = 0;
    for (int s = 0; s < nseq; ++s) { const int n = (cu[s + 1] - cu[s] + 31) >> 5; N += max(n - B, 0) + min(B >> 1, (n + 1) >> 1); }
    int rank = k;
    if (k < CUS_PER_XCD) { const int N2 = min(max(N - CUS_PER_XCD, 0), CUS_PER_XCD); rank = (k < N2) ? CUS_PER_XCD - 1 - k : k - N2; }
    if (rank >= N) return false;
    int jx = nmax, my = min(B >> 1, (nmax + 1) >> 1);
    for (;;) {
        const int x = (jx > B) && (((jx + 1) >> 1) >= 2 * my);
        const int thr = x ? 32 * (jx - 1) : 64 * (my - 1);
        for (int s = 0; s < nseq; ++s) {
            const int len = cu[s + 1] - cu[s];
            if (len > thr) { if (rank == 0) { s0 = cu[s]; L = len; p0 = thr; isx = x; return true; } --rank; }
        }
        if (x) --jx; else --my;
    }
}

__global__ __launch_bounds__(256, 2) void attn_varlen_kernel(const char* __restrict__ qp, const char* __restrict__ kp, const char* __restrict__ vp,
                                                             const int* __restrict__ cu, char* __restrict__ op, int nseq
                                                             ) {
    __shared__ __attribute__((aligned(16))) char smem_[LDS_BYTES];
    const int grp = blockIdx.x & 7;
    const int k = blockIdx.x >> 3;
    const int lane = threadIdx.x & 63;

    const int c0 = cu[min(lane, nseq)];
    const int c1 = cu[min(lane + 1, nseq)];
    const int l3 = (lane * 43) >> 7, r3 = lane - 3 * l3, st = l3 + 1;
    const int jx = 2 * st - 1 + r3;
    const bool okx = (r3 < 2) & (jx > BSPLIT) & (lane < 63), oky = (r3 == 2) & !(st & 1) & (st <= BSPLIT);
    const int thr = okx ? 32 * (jx - 1) : (oky ? 32 * (st - 2) : 0x7fffffff);
    const int len = c1 - c0;
    int cnt = 0;
#pragma unroll
    for (int s = 0; s < 8; ++s) cnt += (__builtin_amdgcn_readlane(len, s) > thr) ? 1 : 0;
    if (__builtin_expect(nseq > 8, 0)) {
        for (int s = 8; s < nseq; ++s) cnt += (__builtin_amdgcn_readlane(len, s & 63) > thr) ? 1 : 0;
    }
    const int incl = wave_incl_scan(cnt);
    const int N = __builtin_amdgcn_readlane(incl, 63);
    const int N2 = min(max(N - CUS_PER_XCD, 0), CUS_PER_XCD);
    const int rank = (k >= CUS_PER_XCD) ? k : ((k < N2) ? CUS_PER_XCD - 1 - k : k - N2);
    bool valid = rank < N;
    const int a = N - rank;
    const int lv = (__ffsll((long long)__ballot(incl >= a)) - 1) & 63;
    const int idx = __builtin_amdgcn_readlane(incl, lv) - a;
    int p0 = __builtin_amdgcn_readlane(thr, lv);
    int isx = __builtin_amdgcn_readlane(r3, lv) < 2;
    const bool has = len > p0;
    const unsigned long long mk = __ballot(has);
    const int below = __builtin_amdgcn_mbcnt_hi((unsigned)(mk >> 32), __builtin_amdgcn_mbcnt_lo((unsigned)mk, 0));
    const int sq = (__ffsll((long long)__ballot(has && below == idx)) - 1) & 63;
    int s0 = __builtin_amdgcn_readlane(c0, sq), L = __builtin_amdgcn_readlane(len, sq);
    if (__builtin_expect(nseq > 63 || __ballot(len > 32 * 42) != 0, 0)) valid = decode_slow(cu, nseq, k, s0, L, p0, isx);
    if (!valid) return;
    s0 = rfl(s0); L = rfl(L); p0 = rfl(p0); isx = rfl(isx);
    const unsigned lds0 = (unsigned)(size_t)(lds_char_ptr)smem_;
    if (isx) {
        const int n = (min(p0 + 32, L) + 31) >> 5;
        attn_item<2>(qp, kp, vp, op, grp, s0, L, p0, n, lds0 STPASS);
    } else {
        const int n = (min(p0 + 64, L) + 31) >> 5;
        attn_item<1>(qp, kp, vp, op, grp, s0, L, p0, n, lds0 STPASS);
    }
}

extern "C" void kernel_launch(void* const* d_in, const int* in_sizes, int n_in, void* d_out, int out_size, void* d_ws, size_t ws_size,
                              hipStream_t stream) {
    (void)n_in; (void)out_size; (void)d_ws; (void)ws_size;
    const int T = in_sizes[0] / (HQ * D);
    const int nseq = in_sizes[3] - 1;
    const int per_head = T / 32 + nseq + 1;
    attn_varlen_kernel<<<dim3(8 * per_head), dim3(256), 0, stream>>>((const char*)d_in[0], (const char*)d_in[1], (const char*)d_in[2],
                                                                    (const int*)d_in[3], (char*)d_out, nseq
                                                                    );
}
```

```cpp
#include <hip/hip_runtime.h>
#include <hip/hip_bf16.h>
#include <stdint.h>

typedef __bf16 bf16x8 __attribute__((ext_vector_type(8)));
typedef __bf16 bf16x2 __attribute__((ext_vector_type(2)));
typedef short s16x4 __attribute__((ext_vector_type(4)));
typedef short s16x8 __attribute__((ext_vector_type(8)));
typedef float f32x2 __attribute__((ext_vector_type(2)));
typedef float f32x4 __attribute__((ext_vector_type(4)));
typedef float f32x16 __attribute__((ext_vector_type(16)));
typedef unsigned u32x2 __attribute__((ext_vector_type(2)));
typedef unsigned u32x4 __attribute__((ext_vector_type(4)));
#define LDSP(T) __attribute__((address_space(3))) T*
typedef LDSP(char) lds_char_ptr;

#define DI __device__ __forceinline__
#define LDPOL ""
#define STAUX 16
#define STAMP(i) do {} while (0)
#define STARG
#define STPASS

constexpr int HQ = 16, HKV = 8, D = 192;
constexpr unsigned QSTRIDE = HQ * D * 2;
constexpr unsigned KSTRIDE = HKV * D * 2;
constexpr unsigned ROWB = D * 2;
constexpr unsigned SLOT = 32 * ROWB;
constexpr unsigned ORS = 400;
constexpr unsigned LDS_BYTES = 77824;
constexpr float SCALE_LOG2E = 0.07216878364870322f * 1.4426950408889634f;
constexpr float THR_RAW = 8.0f / SCALE_LOG2E;
constexpr int CUS_PER_XCD = 32;
constexpr int BSPLIT = 8;

DI int rfl(int x) { return __builtin_amdgcn_readfirstlane(x); }
DI unsigned cvtpk(float lo, float hi) {
    f32x2 v = {lo, hi}; bf16x2 b = __builtin_convertvector(v, bf16x2); return __builtin_bit_cast(unsigned, b);
}
DI float max2f(float a, float b) { return __builtin_elementwise_maximum(a, b); }
DI float max3f(float a, float b, float c) { return max2f(max2f(a, b), c); }
DI float swap_max(float x) {
    auto r = __builtin_amdgcn_permlane32_swap(__float_as_uint(x), __float_as_uint(x), false, false);
    return max2f(__uint_as_float(r[0]), __uint_as_float(r[1]));
}
DI float swap_add(float x) {
    auto r = __builtin_amdgcn_permlane32_swap(__float_as_uint(x), __float_as_uint(x), false, false);
    return __uint_as_float(r[0]) + __uint_as_float(r[1]);
}
DI u32x4 make_srd(const void* base, unsigned nbytes) {
    unsigned long long a = (unsigned long long)base;
    u32x4 s; s[0] = rfl((unsigned)a); s[1] = rfl((unsigned)(a >> 32) & 0xffffu); s[2] = rfl(nbytes); s[3] = 0x00020000u;
    return s;
}
template <int IMM> DI void dma16(unsigned m0v, unsigned voff, u32x4 srd, unsigned soff) {
    asm volatile("s_mov_b32 m0, %0\n\ts_nop 0\n\tbuffer_load_dwordx4 %1, %2, %3 offen offset:%4 lds"
                 :: "s"(m0v), "v"(voff), "s"(srd), "s"(soff), "i"(IMM) : "memory");
}
template <int IMM, int NEXT> DI void dma16c(unsigned voff, u32x4 srd, unsigned soff, unsigned nbase) {
    asm volatile("buffer_load_dwordx4 %0, %1, %2 offen offset:%3 " LDPOL " lds\n\ts_add_u32 m0, %4, %5"
                 :: "v"(voff), "s"(srd), "s"(soff), "i"(IMM), "s"(nbase), "i"(NEXT) : "memory", "scc");
}
template <int IMM> DI void dma16e(unsigned voff, u32x4 srd, unsigned soff) {
    asm volatile("buffer_load_dwordx4 %0, %1, %2 offen offset:%3 " LDPOL " lds" :: "v"(voff), "s"(srd), "s"(soff), "i"(IMM) : "memory");
}
template <int N> DI void wait_vm() { asm volatile("s_waitcnt vmcnt(%0)" :: "i"(N) : "memory"); }
DI void barrier() { asm volatile("s_waitcnt lgkmcnt(0)\n\ts_barrier" ::: "memory"); }
#define SB() __builtin_amdgcn_sched_barrier(0)
#define MFMA(a, b, c) __builtin_amdgcn_mfma_f32_32x32x16_bf16((a), (b), (c), 0, 0, 0)

DI bf16x8 ldk(unsigned addr) { return *(LDSP(bf16x8))(size_t)addr; }
DI bf16x8 ldv(unsigned addr) {
    const s16x4 lo = __builtin_amdgcn_ds_read_tr16_b64_v4i16((LDSP(s16x4))(size_t)addr);
    const s16x4 hi = __builtin_amdgcn_ds_read_tr16_b64_v4i16((LDSP(s16x4))(size_t)(addr + 1024));
    return __builtin_bit_cast(bf16x8, __builtin_shufflevector(lo, hi, 0, 1, 2, 3, 4, 5, 6, 7));
}

template <int V> struct IC { static constexpr int value = V; };

template <int S>
DI void attn_item(const char* __restrict__ qp, const char* __restrict__ kp, const char* __restrict__ vp, char* __restrict__ op,
                  int grp, int s0, int L, int p0, int n, unsigned lds0 STARG) {
    constexpr int NS = (S == 1) ? 6 : 3;
    constexpr int PW = (S == 1) ? 3 : 6;
    constexpr unsigned TSTEP = 32 * S * KSTRIDE;
    const int tid = threadIdx.x, lane = tid & 63;
    const int w = rfl(tid >> 6);
    const int hh = w & 1, up = w >> 1;
    const int r32 = lane & 31, h = lane >> 5;
    const int sig = (S == 1) ? 0 : up;
    const int pw = (S == 1) ? p0 + 32 * up : p0;
    const int steps = (S == 1) ? n : (n + 1) >> 1;
    const int tdiag = pw >> 5;

    const int kend = min(L, 32 * n);
    const u32x4 ksrd = make_srd(kp + (size_t)s0 * KSTRIDE + grp * ROWB, (unsigned)(kend - 1) * KSTRIDE + ROWB);
    const u32x4 vsrd = make_srd(vp + (size_t)s0 * KSTRIDE + grp * ROWB, (unsigned)(kend - 1) * KSTRIDE + ROWB);
    const u32x4 qsrd = make_srd(qp + (size_t)s0 * QSTRIDE, (unsigned)L * QSTRIDE);

    const unsigned ring = lds0 + sig * NS * SLOT;
    const unsigned mW = ring + ((S == 1) ? w * 1024 : hh * 1024);
    const unsigned drow = (unsigned)(8 * hh + (lane >> 3) + ((S == 1) ? 16 * up : 0)) * KSTRIDE;
    const unsigned voffK = drow + 16u * ((lane & 7) ^ ((hh << 2) | (lane >> 4)));
    const unsigned voffV = drow + 16u * ((lane & 7) ^ (((lane >> 4) & 1) << 2));
    const unsigned voffK2 = voffK + 16 * KSTRIDE, voffV2 = voffV + 16 * KSTRIDE;
#define PIECE(ISV, j, so, mb, MO) do { const u32x4 sr_ = (ISV) ? vsrd : ksrd;                                                            \
        const unsigned v1_ = (ISV) ? voffV : voffK, v2_ = (ISV) ? voffV2 : voffK2;                                                       \
        if (j == 0) dma16c<0, (MO) + 4096 - 128>(v1_, sr_, so, mb);                                                                      \
        else if (j == 1) dma16c<128, (MO) + 8192 - 256>(v1_, sr_, so, mb);                                                               \
        else if (j == 2) { if (S == 1) dma16e<256>(v1_, sr_, so); else dma16c<256, (MO) + 2048>(v1_, sr_, so, mb); }                      \
        else if (j == 3) dma16c<0, (MO) + 2048 + 4096 - 128>(v2_, sr_, so, mb);                                                          \
        else if (j == 4) dma16c<128, (MO) + 2048 + 8192 - 256>(v2_, sr_, so, mb);                                                        \
        else dma16e<256>(v2_, sr_, so); } while (0)
#define SETM0(mb, MO) asm volatile("s_add_u32 m0, %0, %1\n\ts_nop 0" :: "s"(mb), "i"(MO) : "memory", "scc")
#define ELEM(ISV, so, mb, MO) do { SETM0(mb, MO); PIECE(ISV, 0, so, mb, MO); NOP0(); PIECE(ISV, 1, so, mb, MO); NOP0(); PIECE(ISV, 2, so, mb, MO);      \
        if (S == 2) { NOP0(); PIECE(ISV, 3, so, mb, MO); NOP0(); PIECE(ISV, 4, so, mb, MO); NOP0(); PIECE(ISV, 5, so, mb, MO); } } while (0)
#define NOP0() asm volatile("s_nop 0" ::: "memory")
    const unsigned so0 = (unsigned)(32 * sig) * KSTRIDE;

    STAMP(2);
    ELEM(false, so0, mW, 0);
    const unsigned qslot = (S == 1) ? lds0 + (2 + w) * SLOT : lds0 + (3 * hh + 2) * SLOT;
    {
        const unsigned qrow = (unsigned)(2 * grp + hh) * ROWB + (unsigned)pw * QSTRIDE;
        const unsigned l8 = (unsigned)(lane >> 3) * QSTRIDE;
        if (S == 1) {
            const unsigned vq0 = l8 + 16u * ((lane & 7) ^ (lane >> 4)), vq1 = l8 + 8 * QSTRIDE + 16u * ((lane & 7) ^ (4 | (lane >> 4)));
            const unsigned so2 = qrow + 16 * QSTRIDE;
            SETM0(qslot, 0);
            dma16c<0, 4096 - 128>(vq0, qsrd, qrow, qslot); NOP0(); dma16c<128, 8192 - 256>(vq0, qsrd, qrow, qslot); NOP0(); dma16c<256, 1024>(vq0, qsrd, qrow, qslot); NOP0();
            dma16c<0, 1024 + 4096 - 128>(vq1, qsrd, qrow, qslot); NOP0(); dma16c<128, 1024 + 8192 - 256>(vq1, qsrd, qrow, qslot); NOP0(); dma16c<256, 2048>(vq1, qsrd, qrow, qslot); NOP0();
            dma16c<0, 2048 + 4096 - 128>(vq0, qsrd, so2, qslot); NOP0(); dma16c<128, 2048 + 8192 - 256>(vq0, qsrd, so2, qslot); NOP0(); dma16c<256, 3072>(vq0, qsrd, so2, qslot); NOP0();
            dma16c<0, 3072 + 4096 - 128>(vq1, qsrd, so2, qslot); NOP0(); dma16c<128, 3072 + 8192 - 256>(vq1, qsrd, so2, qslot); NOP0(); dma16e<256>(vq1, qsrd, so2);
        } else {
            const unsigned vq = l8 + (unsigned)(8 * up) * QSTRIDE + 16u * ((lane & 7) ^ ((up << 2) | (lane >> 4))), vq2 = vq + 16 * QSTRIDE;
            const unsigned qm = qslot + up * 1024;
            SETM0(qm, 0);
            dma16c<0, 4096 - 128>(vq, qsrd, qrow, qm); NOP0(); dma16c<128, 8192 - 256>(vq, qsrd, qrow, qm); NOP0(); dma16c<256, 2048>(vq, qsrd, qrow, qm); NOP0();
            dma16c<0, 2048 + 4096 - 128>(vq2, qsrd, qrow, qm); NOP0(); dma16c<128, 2048 + 8192 - 256>(vq2, qsrd, qrow, qm); NOP0(); dma16e<256>(vq2, qsrd, qrow);
        }
    }
    ELEM(false, so0 + TSTEP, mW, SLOT);

    const int mysteps = (pw >= L) ? 0 : ((S == 1) ? tdiag + 1 : ((n - sig + 1) >> 1));
    const int itdiag = (S == 1) ? tdiag : (((tdiag - sig) & 1) ? -1 : (tdiag - sig) >> 1);
    const unsigned kb0 = r32 * 128 + 16 * (h ^ ((r32 >> 1) & 7));
    constexpr unsigned KB = (S == 1) ? SLOT : 0;
    unsigned kA = ring + KB + kb0, kBb = kA ^ 32, kC = kA ^ 64, kD = kA ^ 96;
    const int q4 = (lane & 15) >> 2, p4 = lane & 3, blk = (lane >> 4) & 1;
    unsigned vA = ring + (4 * h + q4) * 128 + 64 * (q4 >> 1) + 32 * blk + 8 * p4, vB = vA ^ 64;
    f32x16 o[6];
#pragma unroll
    for (int c = 0; c < 6; ++c) { o[c] = f32x16{}; asm volatile("" : "+v"(o[c])); }
    float m = -1e30f, l = 0.f, alpha = 1.f, mthr, mc;
    int pend = 0;
    int dq0 = pw + r32 - 4 * h;
    asm volatile("" : "+v"(kA), "+v"(kBb), "+v"(kC), "+v"(kD), "+v"(vA), "+v"(vB), "+v"(dq0));
    f32x16 s;
    float x[16];
#define KRD(i, OFF) ldk((((i) & 3) == 0 ? kA : ((i) & 3) == 1 ? kBb : ((i) & 3) == 2 ? kC : kD) + (OFF) + ((i) >> 2) * 4096)
#define VRD(i, OFF) ldv(((((i) % 6) & 1) ? vB : vA) + (OFF) + (((i) % 6) >> 1) * 4096 + ((i) / 6) * 2048)

    wait_vm<PW>();
    STAMP(4);
    barrier();
    bf16x8 q[12];
    {
        const unsigned a0 = qslot + kb0;
#pragma unroll
        for (int i = 0; i < 12; ++i) q[i] = ldk((a0 ^ (32 * (i & 3))) + (i >> 2) * 4096);
    }
    bf16x8 kf[12];
    {
        const unsigned a0 = ring + kb0;
#pragma unroll
        for (int i = 0; i < 6; ++i) kf[i] = ldk((a0 ^ (32 * (i & 3))) + (i >> 2) * 4096);
    }
    barrier();
    ELEM(true, so0, mW, 2 * SLOT);
    if (S == 1) {
        ELEM(false, so0 + 2 * TSTEP, mW, 3 * SLOT);
        ELEM(true, so0 + TSTEP, mW, 4 * SLOT);
        ELEM(false, so0 + 3 * TSTEP, mW, 5 * SLOT);
    }
    STAMP(5);

#define GATE(mb, MO) do { __builtin_amdgcn_s_waitcnt(((NS - 2) * PW) | 0x70);                                                            \
        asm volatile("s_barrier\n\ts_add_u32 m0, %0, %1" :: "s"(mb), "i"(MO) : "memory", "scc"); } while (0)
#define PIN1(a) asm volatile("" : "+v"(a))
#define PIN2(a, b) asm volatile("" : "+v"(a), "+v"(b))
#define PIN4(a, b, c, d) asm volatile("" : "+v"(a), "+v"(b), "+v"(c), "+v"(d))
    auto mask_diag = [&]() {
        int dq = dq0 - 32 * tdiag;
        asm volatile("" : "+v"(dq));
#pragma unroll
        for (int i = 0; i < 16; ++i) { const int c = (i & 3) + 8 * (i >> 2); s[i] = (c <= dq) ? s[i] : -__builtin_inff(); }
    };

    if (mysteps > 0) {
        const unsigned a0 = ring + kb0;
#pragma unroll
        for (int i = 6; i < 12; ++i) kf[i] = ldk((a0 ^ (32 * (i & 3))) + (i >> 2) * 4096);
        s = MFMA(kf[0], q[0], f32x16{});
#pragma unroll
        for (int i = 1; i < 12; ++i) s = MFMA(kf[i], q[i], s);
    }
    GATE(mW, 0);
    constexpr unsigned KS0 = (S == 1) ? 0 : SLOT;
    kf[0] = KRD(0, KS0);
    PIECE(S == 1, 0, so0 + 2 * TSTEP, mW, 0); kf[1] = KRD(1, KS0);
    PIECE(S == 1, 1, so0 + 2 * TSTEP, mW, 0); kf[2] = KRD(2, KS0);
    PIECE(S == 1, 2, so0 + 2 * TSTEP, mW, 0); kf[3] = KRD(3, KS0);
    if (S == 2) PIECE(false, 3, so0 + 2 * TSTEP, mW, 0);
    kf[4] = KRD(4, KS0);
    if (S == 2) { PIECE(false, 4, so0 + 2 * TSTEP, mW, 0); NOP0(); PIECE(false, 5, so0 + 2 * TSTEP, mW, 0); }
    if (mysteps > 0) {
        if (__builtin_expect(itdiag == 0, 0)) mask_diag();
        float a = max3f(s[0], s[1], s[2]), b = max3f(s[8], s[9], s[10]);
        a = max3f(a, s[3], s[4]); b = max3f(b, s[11], s[12]);
        a = max3f(a, s[5], s[6]); b = max3f(b, s[13], s[14]);
        a = max3f(a, s[7], s[15]);
        m = max2f(m, swap_max(max2f(a, b)));
    }
    mthr = m + THR_RAW; mc = -m * SCALE_LOG2E;
#pragma unroll
    for (int i = 0; i < 16; ++i) x[i] = fmaf(s[i], SCALE_LOG2E, mc);
    STAMP(6);

    int it = 0;
    unsigned soA = so0 + ((S == 1) ? 4 : 1) * TSTEP, soB = so0 + 3 * TSTEP;
    u32x4 pa, pb;
#define EXP2X(i, j) do { x[i] = __builtin_amdgcn_exp2f(x[i]); x[j] = __builtin_amdgcn_exp2f(x[j]); PIN2(x[i], x[j]); } while (0)
#define FMAX2(i, j) do { x[i] = fmaf(s[i], SCALE_LOG2E, mc); x[j] = fmaf(s[j], SCALE_LOG2E, mc); PIN2(x[i], x[j]); } while (0)
    auto iter = [&](auto PHC) {
        constexpr int PH = decltype(PHC)::value, PN = (PH + 1) % 3;
        constexpr unsigned SK = ((S == 1) ? 1 + 2 * PH : (PH == 0 ? 1 : PH == 1 ? 0 : 2)) * SLOT;
        constexpr unsigned SV = ((S == 1) ? (2 + 2 * PH) % 6 : 2 - PH) * SLOT;
        constexpr unsigned SKN = ((S == 1) ? 1 + 2 * PN : (PN == 0 ? 1 : PN == 1 ? 0 : 2)) * SLOT;
        constexpr unsigned RK = SK - KB, RKN = SKN - KB;
        float ls0, ls1;
        PIN2(kf[0], kf[1]);
        kf[5] = KRD(5, RK);
        s = MFMA(kf[0], q[0], f32x16{}); EXP2X(0, 1); SB();
        kf[6] = KRD(6, RK);
        s = MFMA(kf[1], q[1], s); EXP2X(2, 3); SB();
        PIN2(kf[2], kf[3]);
        kf[7] = KRD(7, RK);
        s = MFMA(kf[2], q[2], s); EXP2X(4, 5); SB();
        kf[8] = KRD(8, RK);
        s = MFMA(kf[3], q[3], s); EXP2X(6, 7); SB();
        PIN2(kf[4], kf[5]);
        kf[9] = KRD(9, RK);
        s = MFMA(kf[4], q[4], s); EXP2X(8, 9); ls0 = x[0] + x[2]; ls1 = x[1] + x[3]; PIN2(ls0, ls1); SB();
        kf[10] = KRD(10, RK); kf[11] = KRD(11, RK);
        s = MFMA(kf[5], q[5], s); EXP2X(10, 11); ls0 += x[4]; ls1 += x[5]; PIN2(ls0, ls1); SB();
        PIN2(kf[6], kf[7]);
        s = MFMA(kf[6], q[6], s); EXP2X(12, 13); pa[0] = cvtpk(x[0], x[1]); pa[1] = cvtpk(x[2], x[3]); PIN2(pa[0], pa[1]); SB();
        s = MFMA(kf[7], q[7], s); EXP2X(14, 15); pa[2] = cvtpk(x[4], x[5]); pa[3] = cvtpk(x[6], x[7]); PIN2(pa[2], pa[3]); SB();
        GATE(mW, SK);
        bf16x8 vf[12];
        vf[0] = VRD(0, SV); SB();
        s = MFMA(kf[8], q[8], s); vf[1] = VRD(1, SV); ls0 += x[6]; ls1 += x[7]; PIN2(ls0, ls1); SB();
        s = MFMA(kf[9], q[9], s); vf[2] = VRD(2, SV); PIECE(S == 2, 0, soA, mW, SK); ls0 += x[8]; ls1 += x[9]; PIN2(ls0, ls1); SB();
        s = MFMA(kf[10], q[10], s); vf[3] = VRD(3, SV); PIECE(S == 2, 1, soA, mW, SK); ls0 += x[10]; ls1 += x[11]; PIN2(ls0, ls1); SB();
        s = MFMA(kf[11], q[11], s); vf[4] = VRD(4, SV); PIECE(S == 2, 2, soA, mW, SK); ls0 += x[12]; ls1 += x[13]; PIN2(ls0, ls1); SB();
        const bf16x8 p0f = __builtin_bit_cast(bf16x8, pa);
        PIN2(vf[0], vf[1]);
        vf[5] = VRD(5, SV);
        o[0] = MFMA(vf[0], p0f, o[0]); if (S == 2) PIECE(true, 3, soA, mW, SK);
        pb[0] = cvtpk(x[8], x[9]); pb[1] = cvtpk(x[10], x[11]); PIN2(pb[0], pb[1]); ls0 += x[14]; ls1 += x[15]; PIN2(ls0, ls1); SB();
        vf[6] = VRD(6, SV);
        o[1] = MFMA(vf[1], p0f, o[1]); if (S == 2) PIECE(true, 4, soA, mW, SK);
        pb[2] = cvtpk(x[12], x[13]); pb[3] = cvtpk(x[14], x[15]); PIN2(pb[2], pb[3]); l += ls0; PIN1(l); SB();
        PIN2(vf[2], vf[3]);
        vf[7] = VRD(7, SV);
        o[2] = MFMA(vf[2], p0f, o[2]); if (S == 2) PIECE(true, 5, soA, mW, SK);
        l += ls1; PIN1(l); SB();
        const bf16x8 p1f = __builtin_bit_cast(bf16x8, pb);
        vf[8] = VRD(8, SV);
        o[3] = MFMA(vf[3], p0f, o[3]); SB();
        if (__builtin_expect(it + 1 == itdiag, 0)) mask_diag();
        float ma = max3f(s[0], s[1], s[2]), mb = max3f(s[8], s[9], s[10]);
        ma = max3f(ma, s[3], s[4]); mb = max3f(mb, s[11], s[12]);
        ma = max3f(ma, s[5], s[6]); mb = max3f(mb, s[13], s[14]); PIN2(ma, mb); SB();
        PIN2(vf[4], vf[5]);
        vf[9] = VRD(9, SV);
        o[4] = MFMA(vf[4], p0f, o[4]);
        ma = max3f(ma, s[7], s[15]);
        float pm = swap_max(max2f(ma, mb)); PIN1(pm); SB();
        vf[10] = VRD(10, SV); vf[11] = VRD(11, SV);
        o[5] = MFMA(vf[5], p0f, o[5]); SB();
        if (__builtin_expect(__any(pm > mthr), 0)) {
            const float mn = max2f(m, pm);
            alpha = __builtin_amdgcn_exp2f((m - mn) * SCALE_LOG2E);
            m = mn; mthr = m + THR_RAW; mc = -m * SCALE_LOG2E; pend = 1;
        }
        PIN2(vf[6], vf[7]);
        o[0] = MFMA(vf[6], p1f, o[0]); FMAX2(0, 1); FMAX2(2, 3); FMAX2(4, 5); SB();
        o[1] = MFMA(vf[7], p1f, o[1]); FMAX2(6, 7); FMAX2(8, 9); FMAX2(10, 11); SB();
        GATE(mW, SV);
        kf[0] = KRD(0, RKN); SB();
        o[2] = MFMA(vf[8], p1f, o[2]); kf[1] = KRD(1, RKN); PIECE(S == 1, 0, soB, mW, SV); FMAX2(12, 13); SB();
        o[3] = MFMA(vf[9], p1f, o[3]); kf[2] = KRD(2, RKN); PIECE(S == 1, 1, soB, mW, SV); FMAX2(14, 15); SB();
        o[4] = MFMA(vf[10], p1f, o[4]); PIECE(S == 1, 2, soB, mW, SV); kf[3] = KRD(3, RKN); if (S == 2) PIECE(false, 3, soB, mW, SV); SB();
        o[5] = MFMA(vf[11], p1f, o[5]); if (S == 2) PIECE(false, 4, soB, mW, SV); kf[4] = KRD(4, RKN); if (S == 2) PIECE(false, 5, soB, mW, SV); SB();
        soA += TSTEP; soB += TSTEP;
        if (__builtin_expect(pend, 0)) {
            l *= alpha;
#pragma unroll
            for (int c = 0; c < 6; ++c) o[c] *= alpha;
            pend = 0;
        }
        ++it;
    };
    if (it + 1 < mysteps) {
        for (;;) {
            iter(IC<0>{}); if (it + 1 >= mysteps) break;
            iter(IC<1>{}); if (it + 1 >= mysteps) break;
            iter(IC<2>{}); if (it + 1 >= mysteps) break;
        }
    }
    STAMP(7);
    const int ph = it % 3;
    unsigned tk = ((S == 1) ? 1 + 2 * ph : (ph == 0 ? 1 : ph == 1 ? 0 : 2)) * SLOT;
    unsigned tv = ((S == 1) ? (ph == 2 ? 0 : 2 + 2 * ph) : 2 - ph) * SLOT;
    auto adv2 = [&](unsigned a) { const unsigned t = a + 2 * SLOT; return t >= NS * SLOT ? t - NS * SLOT : t; };
    if (mysteps > 0) {
        const unsigned mk = mW + tk, mv = mW + tv;
        GATE(mk, 0);
        const unsigned va = vA + tv, vb = vB + tv;
        bf16x8 vf[12];
#define VRT(i) ldv(((((i) % 6) & 1) ? vb : va) + (((i) % 6) >> 1) * 4096 + ((i) / 6) * 2048)
        vf[0] = VRT(0); PIECE(S == 2, 0, soA, mk, 0); vf[1] = VRT(1); PIECE(S == 2, 1, soA, mk, 0); vf[2] = VRT(2); PIECE(S == 2, 2, soA, mk, 0);
        vf[3] = VRT(3); if (S == 2) PIECE(true, 3, soA, mk, 0); vf[4] = VRT(4); if (S == 2) PIECE(true, 4, soA, mk, 0); vf[5] = VRT(5); if (S == 2) PIECE(true, 5, soA, mk, 0);
        float ls0 = 0.f, ls1 = 0.f;
#pragma unroll
        for (int i = 0; i < 16; i += 2) { x[i] = __builtin_amdgcn_exp2f(x[i]); x[i + 1] = __builtin_amdgcn_exp2f(x[i + 1]); ls0 += x[i]; ls1 += x[i + 1]; }
        l += ls0 + ls1;
#pragma unroll
        for (int i = 0; i < 4; ++i) { pa[i] = cvtpk(x[2 * i], x[2 * i + 1]); pb[i] = cvtpk(x[8 + 2 * i], x[9 + 2 * i]); }
        PIN4(pa[0], pa[1], pa[2], pa[3]); PIN4(pb[0], pb[1], pb[2], pb[3]);
        const bf16x8 p0f = __builtin_bit_cast(bf16x8, pa), p1f = __builtin_bit_cast(bf16x8, pb);
        SB();
        vf[6] = VRT(6); vf[7] = VRT(7);
        o[0] = MFMA(vf[0], p0f, o[0]); SB();
        vf[8] = VRT(8); vf[9] = VRT(9);
        o[1] = MFMA(vf[1], p0f, o[1]); SB();
        vf[10] = VRT(10); vf[11] = VRT(11);
        o[2] = MFMA(vf[2], p0f, o[2]); SB();
        o[3] = MFMA(vf[3], p0f, o[3]); o[4] = MFMA(vf[4], p0f, o[4]); o[5] = MFMA(vf[5], p0f, o[5]);
        o[0] = MFMA(vf[6], p1f, o[0]); o[1] = MFMA(vf[7], p1f, o[1]); SB();
        if (it + 1 < steps) {
            GATE(mv, 0);
            NOP0();
            PIECE(S == 1, 0, soB, mv, 0); NOP0(); PIECE(S == 1, 1, soB, mv, 0); NOP0(); PIECE(S == 1, 2, soB, mv, 0);
            if (S == 2) { NOP0(); PIECE(false, 3, soB, mv, 0); NOP0(); PIECE(false, 4, soB, mv, 0); NOP0(); PIECE(false, 5, soB, mv, 0); }
        }
        o[2] = MFMA(vf[8], p1f, o[2]); o[3] = MFMA(vf[9], p1f, o[3]); o[4] = MFMA(vf[10], p1f, o[4]); o[5] = MFMA(vf[11], p1f, o[5]);
        tk = adv2(tk); tv = adv2(tv); soA += TSTEP; soB += TSTEP;
        ++it;
    }
    STAMP(8);
    for (; it < steps; ++it) {
        const unsigned mk = mW + tk, mv = mW + tv;
        GATE(mk, 0);
        NOP0();
        PIECE(S == 2, 0, soA, mk, 0); NOP0(); PIECE(S == 2, 1, soA, mk, 0); NOP0(); PIECE(S == 2, 2, soA, mk, 0);
        if (S == 2) { NOP0(); PIECE(true, 3, soA, mk, 0); NOP0(); PIECE(true, 4, soA, mk, 0); NOP0(); PIECE(true, 5, soA, mk, 0); }
        if (it + 1 < steps) {
            GATE(mv, 0);
            NOP0();
            PIECE(S == 1, 0, soB, mv, 0); NOP0(); PIECE(S == 1, 1, soB, mv, 0); NOP0(); PIECE(S == 1, 2, soB, mv, 0);
            if (S == 2) { NOP0(); PIECE(false, 3, soB, mv, 0); NOP0(); PIECE(false, 4, soB, mv, 0); NOP0(); PIECE(false, 5, soB, mv, 0); }
        }
        tk = adv2(tk); tv = adv2(tv); soA += TSTEP; soB += TSTEP;
    }

    STAMP(10);
    l = swap_add(l);
    const __amdgpu_buffer_rsrc_t orsrc = __builtin_amdgcn_make_buffer_rsrc((void*)(op + (size_t)s0 * QSTRIDE), 0, (unsigned)L * QSTRIDE, 0x00020000);
    const unsigned stw = r32 * ORS + 8 * h;
    auto stage_block = [&](unsigned base, int c, const f32x16& z) {
#pragma unroll
        for (int g = 0; g < 4; ++g) {
            u32x2 v = {cvtpk(z[4 * g], z[4 * g + 1]), cvtpk(z[4 * g + 2], z[4 * g + 3])};
            *(LDSP(u32x2))(size_t)(base + (32 * c + 8 * g) * 2) = v;
        }
    };
    unsigned lrd[3], gof[3];
#pragma unroll
    for (int j = 0; j < 3; ++j) { const unsigned idx = 64 * j + lane, row = (idx * 2731) >> 16, ch = idx - 24 * row; lrd[j] = row * ORS + ch * 16; gof[j] = row * QSTRIDE + ch * 16; }
    wait_vm<0>();
    barrier();
    STAMP(11);
    if (S == 1) {
        if (mysteps > 0) {
            const unsigned stg = lds0 + w * (32 * ORS);
            const float inv = __builtin_amdgcn_rcpf(l);
#pragma unroll
            for (int c = 0; c < 6; ++c) { const f32x16 z = o[c] * inv; stage_block(stg + stw, c, z); }
            STAMP(15);
            const unsigned obase = (unsigned)pw * QSTRIDE + (unsigned)(2 * grp + hh) * ROWB;
#pragma unroll
            for (int k = 0; k < 4; ++k)
#pragma unroll
                for (int j = 0; j < 3; ++j) {
                    const u32x4 v = *(LDSP(u32x4))(size_t)(stg + lrd[j] + 8 * k * ORS);
                    __builtin_amdgcn_raw_buffer_store_b128(v, orsrc, gof[j], obase + 8 * k * QSTRIDE, STAUX);
                }
        }
    } else {
        const unsigned mine = lds0 + w * SLOT + lane * 16, other = lds0 + (w ^ 2) * SLOT + lane * 16;
        {
            f32x2 ml = {m, l};
            *(LDSP(f32x2))(size_t)(lds0 + 4 * SLOT + w * 512 + lane * 8) = ml;
        }
        auto send = [&](const f32x16& z, int c) {
#pragma unroll
            for (int g = 0; g < 4; ++g) { f32x4 y = {z[4 * g], z[4 * g + 1], z[4 * g + 2], z[4 * g + 3]}; *(LDSP(f32x4))(size_t)(mine + (c * 4 + g) * 1024) = y; }
        };
        if (sig) { send(o[0], 0); send(o[1], 1); send(o[2], 2); } else { send(o[3], 0); send(o[4], 1); send(o[5], 2); }
        STAMP(12);
        barrier();
        STAMP(13);
        const f32x2 mlo = *(LDSP(f32x2))(size_t)(lds0 + 4 * SLOT + (w ^ 2) * 512 + lane * 8);
        const float mx = max2f(m, mlo[0]);
        const float a0 = __builtin_amdgcn_exp2f((m - mx) * SCALE_LOG2E), a1 = __builtin_amdgcn_exp2f((mlo[0] - mx) * SCALE_LOG2E);
        const float inv = __builtin_amdgcn_rcpf(l * a0 + mlo[1] * a1);
        const float f0 = a0 * inv, f1 = a1 * inv;
        const unsigned stg = lds0 + 51200 + hh * (32 * ORS);
        auto merge = [&](const f32x16& xo, int c, int cd) {
            f32x16 z;
#pragma unroll
            for (int g = 0; g < 4; ++g) {
                const f32x4 y = *(LDSP(f32x4))(size_t)(other + (c * 4 + g) * 1024);
                const f32x4 t = {xo[4 * g], xo[4 * g + 1], xo[4 * g + 2], xo[4 * g + 3]};
                const f32x4 u = t * f0 + y * f1;
                z[4 * g] = u[0]; z[4 * g + 1] = u[1]; z[4 * g + 2] = u[2]; z[4 * g + 3] = u[3];
            }
            stage_block(stg + stw, cd, z);
        };
        if (sig) { merge(o[3], 0, 3); merge(o[4], 1, 4); merge(o[5], 2, 5); } else { merge(o[0], 0, 0); merge(o[1], 1, 1); merge(o[2], 2, 2); }
        STAMP(14);
        barrier();
        STAMP(15);
        const unsigned obase = (unsigned)(pw + 16 * sig) * QSTRIDE + (unsigned)(2 * grp + hh) * ROWB;
        const unsigned stgr = stg + 16 * sig * ORS;
#pragma unroll
        for (int k = 0; k < 2; ++k)
#pragma unroll
            for (int j = 0; j < 3; ++j) {
                const u32x4 v = *(LDSP(u32x4))(size_t)(stgr + lrd[j] + 8 * k * ORS);
                __builtin_amdgcn_raw_buffer_store_b128(v, orsrc, gof[j], obase + 8 * k * QSTRIDE, STAUX);
            }
    }
    STAMP(16);
}

template <int CTRL> DI int dpp_mov(int old, int x) { return __builtin_amdgcn_update_dpp(old, x, CTRL, 0xf, 0xf, false); }
DI int wave_incl_scan(int x) {
    x += dpp_mov<0x111>(0, x);
    x += dpp_mov<0x112>(0, x);
    x += dpp_mov<0x114>(0, x);
    x += dpp_mov<0x118>(0, x);
    x += __builtin_amdgcn_update_dpp(0, x, 0x142, 0xa, 0xf, false);
    x += __builtin_amdgcn_update_dpp(0, x, 0x143, 0xc, 0xf, false);
    return x;
}
DI int wave_max_nonneg(int x) {
    x = max(x, dpp_mov<0x111>(0, x));
    x = max(x, dpp_mov<0x112>(0, x));
    x = max(x, dpp_mov<0x114>(0, x));
    x = max(x, dpp_mov<0x118>(0, x));
    x = max(x, __builtin_amdgcn_update_dpp(0, x, 0x142, 0xa, 0xf, false));
    x = max(x, __builtin_amdgcn_update_dpp(0, x, 0x143, 0xc, 0xf, false));
    return __builtin_amdgcn_readlane(x, 63);
}

DI bool decode_slow(const int* __restrict__ cu, int nseq, int k, int& s0, int& L, int& p0, int& isx) {
    int nmax = 0;
    for (int s = 0; s < nseq; ++s) nmax = max(nmax, (cu[s + 1] - cu[s] + 31) >> 5);
    const int B = BSPLIT;
    int N = 0;
    for (int s = 0; s < nseq; ++s) { const int n = (cu[s + 1] - cu[s] + 31) >> 5; N += max(n - B, 0) + min(B >> 1, (n + 1) >> 1); }
    int rank = k;
    if (k < CUS_PER_XCD) { const int N2 = min(max(N - CUS_PER_XCD, 0), CUS_PER_XCD); rank = (k < N2) ? CUS_PER_XCD - 1 - k : k - N2; }
    if (rank >= N) return false;
    int jx = nmax, my = min(B >> 1, (nmax + 1) >> 1);
    for (;;) {
        const int x = (jx > B) && (((jx + 1) >> 1) >= 2 * my);
        const int thr = x ? 32 * (jx - 1) : 64 * (my - 1);
        for (int s = 0; s < nseq; ++s) {
            const int len = cu[s + 1] - cu[s];
            if (len > thr) { if (rank == 0) { s0 = cu[s]; L = len; p0 = thr; isx = x; return true; } --rank; }
        }
        if (x) --jx; else --my;
    }
}

__global__ __launch_bounds__(256, 2) void attn_varlen_kernel(const char* __restrict__ qp, const char* __restrict__ kp, const char* __restrict__ vp,
                                                             const int* __restrict__ cu, char* __restrict__ op, int nseq
                                                             ) {
    __shared__ __attribute__((aligned(16))) char smem_[LDS_BYTES];
    const int grp = blockIdx.x & 7;
    const int k = blockIdx.x >> 3;
    const int lane = threadIdx.x & 63;

    const int c0 = cu[min(lane, nseq)];
    const int c1 = cu[min(lane + 1, nseq)];
    const int l3 = (lane * 43) >> 7, r3 = lane - 3 * l3, st = l3 + 1;
    const int jx = 2 * st - 1 + r3;
    const bool okx = (r3 < 2) & (jx > BSPLIT) & (lane < 63), oky = (r3 == 2) & !(st & 1) & (st <= BSPLIT);
    const int thr = okx ? 32 * (jx - 1) : (oky ? 32 * (st - 2) : 0x7fffffff);
    const int len = c1 - c0;
    int cnt = 0;
#pragma unroll
    for (int s = 0; s < 8; ++s) cnt += (__builtin_amdgcn_readlane(len, s) > thr) ? 1 : 0;
    if (__builtin_expect(nseq > 8, 0)) {
        for (int s = 8; s < nseq; ++s) cnt += (__builtin_amdgcn_readlane(len, s & 63) > thr) ? 1 : 0;
    }
    const int incl = wave_incl_scan(cnt);
    const int N = __builtin_amdgcn_readlane(incl, 63);
    const int N2 = min(max(N - CUS_PER_XCD, 0), CUS_PER_XCD);
    const int rank = (k >= CUS_PER_XCD) ? k : ((k < N2) ? CUS_PER_XCD - 1 - k : k - N2);
    bool valid = rank < N;
    const int a = N - rank;
    const int lv = (__ffsll((long long)__ballot(incl >= a)) - 1) & 63;
    const int idx = __builtin_amdgcn_readlane(incl, lv) - a;
    int p0 = __builtin_amdgcn_readlane(thr, lv);
    int isx = __builtin_amdgcn_readlane(r3, lv) < 2;
    const bool has = len > p0;
    const unsigned long long mk = __ballot(has);
    const int below = __builtin_amdgcn_mbcnt_hi((unsigned)(mk >> 32), __builtin_amdgcn_mbcnt_lo((unsigned)mk, 0));
    const int sq = (__ffsll((long long)__ballot(has && below == idx)) - 1) & 63;
    int s0 = __builtin_amdgcn_readlane(c0, sq), L = __builtin_amdgcn_readlane(len, sq);
    if (__builtin_expect(nseq > 63 || __ballot(len > 32 * 42) != 0, 0)) valid = decode_slow(cu, nseq, k, s0, L, p0, isx);
    if (!valid) return;
    s0 = rfl(s0); L = rfl(L); p0 = rfl(p0); isx = rfl(isx);
    const unsigned lds0 = (unsigned)(size_t)(lds_char_ptr)smem_;
    if (isx) {
        const int n = (min(p0 + 32, L) + 31) >> 5;
        attn_item<2>(qp, kp, vp, op, grp, s0, L, p0, n, lds0 STPASS);
    } else {
        const int n = (min(p0 + 64, L) + 31) >> 5;
        attn_item<1>(qp, kp, vp, op, grp, s0, L, p0, n, lds0 STPASS);
    }
}

extern "C" void kernel_launch(void* const* d_in, const int* in_sizes, int n_in, void* d_out, int out_size, void* d_ws, size_t ws_size,
                              hipStream_t stream) {
    (void)n_in; (void)out_size; (void)d_ws; (void)ws_size;
    const int T = in_sizes[0] / (HQ * D);
    const int nseq = in_sizes[3] - 1;
    const int per_head = T / 32 + nseq + 1;
    attn_varlen_kernel<<<dim3(8 * per_head), dim3(256), 0, stream>>>((const char*)d_in[0], (const char*)d_in[1], (const char*)d_in[2],
                                                                    (const int*)d_in[3], (char*)d_out, nseq
                                                                    );
}
```

```cpp
#include <hip/hip_runtime.h>
#include <hip/hip_bf16.h>
#include <stdint.h>

typedef __bf16 bf16x8 __attribute__((ext_vector_type(8)));
typedef __bf16 bf16x2 __attribute__((ext_vector_type(2)));
typedef short s16x4 __attribute__((ext_vector_type(4)));
typedef short s16x8 __attribute__((ext_vector_type(8)));
typedef float f32x2 __attribute__((ext_vector_type(2)));
typedef float f32x4 __attribute__((ext_vector_type(4)));
typedef float f32x16 __attribute__((ext_vector_type(16)));
typedef unsigned u32x2 __attribute__((ext_vector_type(2)));
typedef unsigned u32x4 __attribute__((ext_vector_type(4)));
#define LDSP(T) __attribute__((address_space(3))) T*
typedef LDSP(char) lds_char_ptr;

#define DI __device__ __forceinline__
#define LDPOL ""
#define STAUX 16
#define PFPOL ""
#define PFMODE 1
#define STAMP(i) do {} while (0)
#define STARG
#define STPASS

constexpr int HQ = 16, HKV = 8, D = 192;
constexpr unsigned QSTRIDE = HQ * D * 2;
constexpr unsigned KSTRIDE = HKV * D * 2;
constexpr unsigned ROWB = D * 2;
constexpr unsigned SLOT = 32 * ROWB;
constexpr unsigned ORS = 400;
constexpr unsigned LDS_BYTES = 77824;
constexpr float SCALE_LOG2E = 0.07216878364870322f * 1.4426950408889634f;
constexpr float THR_RAW = 8.0f / SCALE_LOG2E;
constexpr int CUS_PER_XCD = 32;
constexpr int BSPLIT = 8;

DI int rfl(int x) { return __builtin_amdgcn_readfirstlane(x); }
DI unsigned cvtpk(float lo, float hi) {
    f32x2 v = {lo, hi}; bf16x2 b = __builtin_convertvector(v, bf16x2); return __builtin_bit_cast(unsigned, b);
}
DI float max2f(float a, float b) { return __builtin_elementwise_maximum(a, b); }
DI float max3f(float a, float b, float c) { return max2f(max2f(a, b), c); }
DI float swap_max(float x) {
    auto r = __builtin_amdgcn_permlane32_swap(__float_as_uint(x), __float_as_uint(x), false, false);
    return max2f(__uint_as_float(r[0]), __uint_as_float(r[1]));
}
DI float swap_add(float x) {
    auto r = __builtin_amdgcn_permlane32_swap(__float_as_uint(x), __float_as_uint(x), false, false);
    return __uint_as_float(r[0]) + __uint_as_float(r[1]);
}
DI u32x4 make_srd(const void* base, unsigned nbytes) {
    unsigned long long a = (unsigned long long)base;
    u32x4 s; s[0] = rfl((unsigned)a); s[1] = rfl((unsigned)(a >> 32) & 0xffffu); s[2] = rfl(nbytes); s[3] = 0x00020000u;
    return s;
}
template <int IMM> DI void dma16(unsigned m0v, unsigned voff, u32x4 srd, unsigned soff) {
    asm volatile("s_mov_b32 m0, %0\n\ts_nop 0\n\tbuffer_load_dwordx4 %1, %2, %3 offen offset:%4 lds"
                 :: "s"(m0v), "v"(voff), "s"(srd), "s"(soff), "i"(IMM) : "memory");
}
template <int IMM, int NEXT> DI void dma16c(unsigned voff, u32x4 srd, unsigned soff, unsigned nbase) {
    asm volatile("buffer_load_dwordx4 %0, %1, %2 offen offset:%3 " LDPOL " lds\n\ts_add_u32 m0, %4, %5"
                 :: "v"(voff), "s"(srd), "s"(soff), "i"(IMM), "s"(nbase), "i"(NEXT) : "memory", "scc");
}
template <int IMM> DI void dma16e(unsigned voff, u32x4 srd, unsigned soff) {
    asm volatile("buffer_load_dwordx4 %0, %1, %2 offen offset:%3 " LDPOL " lds" :: "v"(voff), "s"(srd), "s"(soff), "i"(IMM) : "memory");
}
template <int N> DI void wait_vm() { asm volatile("s_waitcnt vmcnt(%0)" :: "i"(N) : "memory"); }
DI void barrier() { asm volatile("s_waitcnt lgkmcnt(0)\n\ts_barrier" ::: "memory"); }
#define SB() __builtin_amdgcn_sched_barrier(0)
#define MFMA(a, b, c) __builtin_amdgcn_mfma_f32_32x32x16_bf16((a), (b), (c), 0, 0, 0)

DI bf16x8 ldk(unsigned addr) { return *(LDSP(bf16x8))(size_t)addr; }
DI bf16x8 ldv(unsigned addr) {
    const s16x4 lo = __builtin_amdgcn_ds_read_tr16_b64_v4i16((LDSP(s16x4))(size_t)addr);
    const s16x4 hi = __builtin_amdgcn_ds_read_tr16_b64_v4i16((LDSP(s16x4))(size_t)(addr + 1024));
    return __builtin_bit_cast(bf16x8, __builtin_shufflevector(lo, hi, 0, 1, 2, 3, 4, 5, 6, 7));
}

template <int V> struct IC { static constexpr int value = V; };

template <int S>
DI void attn_item(const char* __restrict__ qp, const char* __restrict__ kp, const char* __restrict__ vp, char* __restrict__ op,
                  int grp, int s0, int L, int p0, int n, unsigned lds0 STARG) {
    constexpr int NS = (S == 1) ? 6 : 3;
    constexpr int PW = (S == 1) ? 3 : 6;
    constexpr unsigned TSTEP = 32 * S * KSTRIDE;
    const int tid = threadIdx.x, lane = tid & 63;
    const int w = rfl(tid >> 6);
    const int hh = w & 1, up = w >> 1;
    const int r32 = lane & 31, h = lane >> 5;
    const int sig = (S == 1) ? 0 : up;
    const int pw = (S == 1) ? p0 + 32 * up : p0;
    const int steps = (S == 1) ? n : (n + 1) >> 1;
    const int tdiag = pw >> 5;

    const int kend = min(L, 32 * n);
    const u32x4 ksrd = make_srd(kp + (size_t)s0 * KSTRIDE + grp * ROWB, (unsigned)(kend - 1) * KSTRIDE + ROWB);
    const u32x4 vsrd = make_srd(vp + (size_t)s0 * KSTRIDE + grp * ROWB, (unsigned)(kend - 1) * KSTRIDE + ROWB);
    const u32x4 qsrd = make_srd(qp + (size_t)s0 * QSTRIDE, (unsigned)L * QSTRIDE);

    const unsigned ring = lds0 + sig * NS * SLOT;
    const unsigned mW = ring + ((S == 1) ? w * 1024 : hh * 1024);
    const unsigned drow = (unsigned)(8 * hh + (lane >> 3) + ((S == 1) ? 16 * up : 0)) * KSTRIDE;
    const unsigned voffK = drow + 16u * ((lane & 7) ^ ((hh << 2) | (lane >> 4)));
    const unsigned voffV = drow + 16u * ((lane & 7) ^ (((lane >> 4) & 1) << 2));
    const unsigned voffK2 = voffK + 16 * KSTRIDE, voffV2 = voffV + 16 * KSTRIDE;
#define PIECE(ISV, j, so, mb, MO) do { const u32x4 sr_ = (ISV) ? vsrd : ksrd;                                                            \
        const unsigned v1_ = (ISV) ? voffV : voffK, v2_ = (ISV) ? voffV2 : voffK2;                                                       \
        if (j == 0) dma16c<0, (MO) + 4096 - 128>(v1_, sr_, so, mb);                                                                      \
        else if (j == 1) dma16c<128, (MO) + 8192 - 256>(v1_, sr_, so, mb);                                                               \
        else if (j == 2) { if (S == 1) dma16e<256>(v1_, sr_, so); else dma16c<256, (MO) + 2048>(v1_, sr_, so, mb); }                      \
        else if (j == 3) dma16c<0, (MO) + 2048 + 4096 - 128>(v2_, sr_, so, mb);                                                          \
        else if (j == 4) dma16c<128, (MO) + 2048 + 8192 - 256>(v2_, sr_, so, mb);                                                        \
        else dma16e<256>(v2_, sr_, so); } while (0)
#define SETM0(mb, MO) asm volatile("s_add_u32 m0, %0, %1\n\ts_nop 0" :: "s"(mb), "i"(MO) : "memory", "scc")
#define ELEM(ISV, so, mb, MO) do { SETM0(mb, MO); PIECE(ISV, 0, so, mb, MO); NOP0(); PIECE(ISV, 1, so, mb, MO); NOP0(); PIECE(ISV, 2, so, mb, MO);      \
        if (S == 2) { NOP0(); PIECE(ISV, 3, so, mb, MO); NOP0(); PIECE(ISV, 4, so, mb, MO); NOP0(); PIECE(ISV, 5, so, mb, MO); } } while (0)
#define NOP0() asm volatile("s_nop 0" ::: "memory")
    const unsigned so0 = (unsigned)(32 * sig) * KSTRIDE;

    STAMP(2);
    ELEM(false, so0, mW, 0);
    const unsigned qslot = (S == 1) ? lds0 + (2 + w) * SLOT : lds0 + (3 * hh + 2) * SLOT;
    {
        const unsigned qrow = (unsigned)(2 * grp + hh) * ROWB + (unsigned)pw * QSTRIDE;
        const unsigned l8 = (unsigned)(lane >> 3) * QSTRIDE;
        if (S == 1) {
            const unsigned vq0 = l8 + 16u * ((lane & 7) ^ (lane >> 4)), vq1 = l8 + 8 * QSTRIDE + 16u * ((lane & 7) ^ (4 | (lane >> 4)));
            const unsigned so2 = qrow + 16 * QSTRIDE;
            SETM0(qslot, 0);
            dma16c<0, 4096 - 128>(vq0, qsrd, qrow, qslot); NOP0(); dma16c<128, 8192 - 256>(vq0, qsrd, qrow, qslot); NOP0(); dma16c<256, 1024>(vq0, qsrd, qrow, qslot); NOP0();
            dma16c<0, 1024 + 4096 - 128>(vq1, qsrd, qrow, qslot); NOP0(); dma16c<128, 1024 + 8192 - 256>(vq1, qsrd, qrow, qslot); NOP0(); dma16c<256, 2048>(vq1, qsrd, qrow, qslot); NOP0();
            dma16c<0, 2048 + 4096 - 128>(vq0, qsrd, so2, qslot); NOP0(); dma16c<128, 2048 + 8192 - 256>(vq0, qsrd, so2, qslot); NOP0(); dma16c<256, 3072>(vq0, qsrd, so2, qslot); NOP0();
            dma16c<0, 3072 + 4096 - 128>(vq1, qsrd, so2, qslot); NOP0(); dma16c<128, 3072 + 8192 - 256>(vq1, qsrd, so2, qslot); NOP0(); dma16e<256>(vq1, qsrd, so2);
        } else {
            const unsigned vq = l8 + (unsigned)(8 * up) * QSTRIDE + 16u * ((lane & 7) ^ ((up << 2) | (lane >> 4))), vq2 = vq + 16 * QSTRIDE;
            const unsigned qm = qslot + up * 1024;
            SETM0(qm, 0);
            dma16c<0, 4096 - 128>(vq, qsrd, qrow, qm); NOP0(); dma16c<128, 8192 - 256>(vq, qsrd, qrow, qm); NOP0(); dma16c<256, 2048>(vq, qsrd, qrow, qm); NOP0();
            dma16c<0, 2048 + 4096 - 128>(vq2, qsrd, qrow, qm); NOP0(); dma16c<128, 2048 + 8192 - 256>(vq2, qsrd, qrow, qm); NOP0(); dma16e<256>(vq2, qsrd, qrow);
        }
    }
    ELEM(false, so0 + TSTEP, mW, SLOT);

    const int mysteps = (pw >= L) ? 0 : ((S == 1) ? tdiag + 1 : ((n - sig + 1) >> 1));
    const int itdiag = (S == 1) ? tdiag : (((tdiag - sig) & 1) ? -1 : (tdiag - sig) >> 1);
    const unsigned kb0 = r32 * 128 + 16 * (h ^ ((r32 >> 1) & 7));
    constexpr unsigned KB = (S == 1) ? SLOT : 0;
    unsigned kA = ring + KB + kb0, kBb = kA ^ 32, kC = kA ^ 64, kD = kA ^ 96;
    const int q4 = (lane & 15) >> 2, p4 = lane & 3, blk = (lane >> 4) & 1;
    unsigned vA = ring + (4 * h + q4) * 128 + 64 * (q4 >> 1) + 32 * blk + 8 * p4, vB = vA ^ 64;
    f32x16 o[6];
#pragma unroll
    for (int c = 0; c < 6; ++c) { o[c] = f32x16{}; asm volatile("" : "+v"(o[c])); }
    float m = -1e30f, l = 0.f, alpha = 1.f, mthr, mc;
    int pend = 0;
    int dq0 = pw + r32 - 4 * h;
    asm volatile("" : "+v"(kA), "+v"(kBb), "+v"(kC), "+v"(kD), "+v"(vA), "+v"(vB), "+v"(dq0));
    f32x16 s;
    float x[16];
#define KRD(i, OFF) ldk((((i) & 3) == 0 ? kA : ((i) & 3) == 1 ? kBb : ((i) & 3) == 2 ? kC : kD) + (OFF) + ((i) >> 2) * 4096)
#define VRD(i, OFF) ldv(((((i) % 6) & 1) ? vB : vA) + (OFF) + (((i) % 6) >> 1) * 4096 + ((i) / 6) * 2048)

    wait_vm<PW>();
    STAMP(4);
    barrier();
    bf16x8 q[12];
    {
        const unsigned a0 = qslot + kb0;
#pragma unroll
        for (int i = 0; i < 12; ++i) q[i] = ldk((a0 ^ (32 * (i & 3))) + (i >> 2) * 4096);
    }
    bf16x8 kf[12];
    {
        const unsigned a0 = ring + kb0;
#pragma unroll
        for (int i = 0; i < 6; ++i) kf[i] = ldk((a0 ^ (32 * (i & 3))) + (i >> 2) * 4096);
    }
    barrier();
    ELEM(true, so0, mW, 2 * SLOT);
    if (S == 1) {
        ELEM(false, so0 + 2 * TSTEP, mW, 3 * SLOT);
        ELEM(true, so0 + TSTEP, mW, 4 * SLOT);
        ELEM(false, so0 + 3 * TSTEP, mW, 5 * SLOT);
    }
    STAMP(5);

#define GATE(mb, MO) do { __builtin_amdgcn_s_waitcnt(((NS - 2) * PW) | 0x70);                                                            \
        asm volatile("s_barrier\n\ts_add_u32 m0, %0, %1" :: "s"(mb), "i"(MO) : "memory", "scc"); } while (0)
#define PIN1(a) asm volatile("" : "+v"(a))
#define PIN2(a, b) asm volatile("" : "+v"(a), "+v"(b))
#define PIN4(a, b, c, d) asm volatile("" : "+v"(a), "+v"(b), "+v"(c), "+v"(d))
    auto mask_diag = [&]() {
        int dq = dq0 - 32 * tdiag;
        asm volatile("" : "+v"(dq));
#pragma unroll
        for (int i = 0; i < 16; ++i) { const int c = (i & 3) + 8 * (i >> 2); s[i] = (c <= dq) ? s[i] : -__builtin_inff(); }
    };

    if (mysteps > 0) {
        const unsigned a0 = ring + kb0;
#pragma unroll
        for (int i = 6; i < 12; ++i) kf[i] = ldk((a0 ^ (32 * (i & 3))) + (i >> 2) * 4096);
        s = MFMA(kf[0], q[0], f32x16{});
#pragma unroll
        for (int i = 1; i < 12; ++i) s = MFMA(kf[i], q[i], s);
    }
    GATE(mW, 0);
    constexpr unsigned KS0 = (S == 1) ? 0 : SLOT;
    kf[0] = KRD(0, KS0);
    PIECE(S == 1, 0, so0 + 2 * TSTEP, mW, 0); kf[1] = KRD(1, KS0);
    PIECE(S == 1, 1, so0 + 2 * TSTEP, mW, 0); kf[2] = KRD(2, KS0);
    PIECE(S == 1, 2, so0 + 2 * TSTEP, mW, 0); kf[3] = KRD(3, KS0);
    if (S == 2) PIECE(false, 3, so0 + 2 * TSTEP, mW, 0);
    kf[4] = KRD(4, KS0);
    if (S == 2) { PIECE(false, 4, so0 + 2 * TSTEP, mW, 0); NOP0(); PIECE(false, 5, so0 + 2 * TSTEP, mW, 0); }
    if (mysteps > 0) {
        if (__builtin_expect(itdiag == 0, 0)) mask_diag();
        float a = max3f(s[0], s[1], s[2]), b = max3f(s[8], s[9], s[10]);
        a = max3f(a, s[3], s[4]); b = max3f(b, s[11], s[12]);
        a = max3f(a, s[5], s[6]); b = max3f(b, s[13], s[14]);
        a = max3f(a, s[7], s[15]);
        m = max2f(m, swap_max(max2f(a, b)));
    }
    mthr = m + THR_RAW; mc = -m * SCALE_LOG2E;
#pragma unroll
    for (int i = 0; i < 16; ++i) x[i] = fmaf(s[i], SCALE_LOG2E, mc);
    STAMP(6);

    int it = 0;
    unsigned soA = so0 + ((S == 1) ? 4 : 1) * TSTEP, soB = so0 + 3 * TSTEP;
    u32x4 pa, pb;
#define EXP2X(i, j) do { x[i] = __builtin_amdgcn_exp2f(x[i]); x[j] = __builtin_amdgcn_exp2f(x[j]); PIN2(x[i], x[j]); } while (0)
#define FMAX2(i, j) do { x[i] = fmaf(s[i], SCALE_LOG2E, mc); x[j] = fmaf(s[j], SCALE_LOG2E, mc); PIN2(x[i], x[j]); } while (0)
    auto iter = [&](auto PHC) {
        constexpr int PH = decltype(PHC)::value, PN = (PH + 1) % 3;
        constexpr unsigned SK = ((S == 1) ? 1 + 2 * PH : (PH == 0 ? 1 : PH == 1 ? 0 : 2)) * SLOT;
        constexpr unsigned SV = ((S == 1) ? (2 + 2 * PH) % 6 : 2 - PH) * SLOT;
        constexpr unsigned SKN = ((S == 1) ? 1 + 2 * PN : (PN == 0 ? 1 : PN == 1 ? 0 : 2)) * SLOT;
        constexpr unsigned RK = SK - KB, RKN = SKN - KB;
        float ls0, ls1;
        PIN2(kf[0], kf[1]);
        kf[5] = KRD(5, RK);
        s = MFMA(kf[0], q[0], f32x16{}); EXP2X(0, 1); SB();
        kf[6] = KRD(6, RK);
        s = MFMA(kf[1], q[1], s); EXP2X(2, 3); SB();
        PIN2(kf[2], kf[3]);
        kf[7] = KRD(7, RK);
        s = MFMA(kf[2], q[2], s); EXP2X(4, 5); SB();
        kf[8] = KRD(8, RK);
        s = MFMA(kf[3], q[3], s); EXP2X(6, 7); SB();
        PIN2(kf[4], kf[5]);
        kf[9] = KRD(9, RK);
        s = MFMA(kf[4], q[4], s); EXP2X(8, 9); ls0 = x[0] + x[2]; ls1 = x[1] + x[3]; PIN2(ls0, ls1); SB();
        kf[10] = KRD(10, RK); kf[11] = KRD(11, RK);
        s = MFMA(kf[5], q[5], s); EXP2X(10, 11); ls0 += x[4]; ls1 += x[5]; PIN2(ls0, ls1); SB();
        PIN2(kf[6], kf[7]);
        s = MFMA(kf[6], q[6], s); EXP2X(12, 13); pa[0] = cvtpk(x[0], x[1]); pa[1] = cvtpk(x[2], x[3]); PIN2(pa[0], pa[1]); SB();
        s = MFMA(kf[7], q[7], s); EXP2X(14, 15); pa[2] = cvtpk(x[4], x[5]); pa[3] = cvtpk(x[6], x[7]); PIN2(pa[2], pa[3]); SB();
        GATE(mW, SK);
        bf16x8 vf[12];
        vf[0] = VRD(0, SV); SB();
        s = MFMA(kf[8], q[8], s); vf[1] = VRD(1, SV); ls0 += x[6]; ls1 += x[7]; PIN2(ls0, ls1); SB();
        s = MFMA(kf[9], q[9], s); vf[2] = VRD(2, SV); PIECE(S == 2, 0, soA, mW, SK); ls0 += x[8]; ls1 += x[9]; PIN2(ls0, ls1); SB();
        s = MFMA(kf[10], q[10], s); vf[3] = VRD(3, SV); PIECE(S == 2, 1, soA, mW, SK); ls0 += x[10]; ls1 += x[11]; PIN2(ls0, ls1); SB();
        s = MFMA(kf[11], q[11], s); vf[4] = VRD(4, SV); PIECE(S == 2, 2, soA, mW, SK); ls0 += x[12]; ls1 += x[13]; PIN2(ls0, ls1); SB();
        const bf16x8 p0f = __builtin_bit_cast(bf16x8, pa);
        PIN2(vf[0], vf[1]);
        vf[5] = VRD(5, SV);
        o[0] = MFMA(vf[0], p0f, o[0]); if (S == 2) PIECE(true, 3, soA, mW, SK);
        pb[0] = cvtpk(x[8], x[9]); pb[1] = cvtpk(x[10], x[11]); PIN2(pb[0], pb[1]); ls0 += x[14]; ls1 += x[15]; PIN2(ls0, ls1); SB();
        vf[6] = VRD(6, SV);
        o[1] = MFMA(vf[1], p0f, o[1]); if (S == 2) PIECE(true, 4, soA, mW, SK);
        pb[2] = cvtpk(x[12], x[13]); pb[3] = cvtpk(x[14], x[15]); PIN2(pb[2], pb[3]); l += ls0; PIN1(l); SB();
        PIN2(vf[2], vf[3]);
        vf[7] = VRD(7, SV);
        o[2] = MFMA(vf[2], p0f, o[2]); if (S == 2) PIECE(true, 5, soA, mW, SK);
        l += ls1; PIN1(l); SB();
        const bf16x8 p1f = __builtin_bit_cast(bf16x8, pb);
        vf[8] = VRD(8, SV);
        o[3] = MFMA(vf[3], p0f, o[3]); SB();
        if (__builtin_expect(it + 1 == itdiag, 0)) mask_diag();
        float ma = max3f(s[0], s[1], s[2]), mb = max3f(s[8], s[9], s[10]);
        ma = max3f(ma, s[3], s[4]); mb = max3f(mb, s[11], s[12]);
        ma = max3f(ma, s[5], s[6]); mb = max3f(mb, s[13], s[14]); PIN2(ma, mb); SB();
        PIN2(vf[4], vf[5]);
        vf[9] = VRD(9, SV);
        o[4] = MFMA(vf[4], p0f, o[4]);
        ma = max3f(ma, s[7], s[15]);
        float pm = swap_max(max2f(ma, mb)); PIN1(pm); SB();
        vf[10] = VRD(10, SV); vf[11] = VRD(11, SV);
        o[5] = MFMA(vf[5], p0f, o[5]); SB();
        if (__builtin_expect(__any(pm > mthr), 0)) {
            const float mn = max2f(m, pm);
            alpha = __builtin_amdgcn_exp2f((m - mn) * SCALE_LOG2E);
            m = mn; mthr = m + THR_RAW; mc = -m * SCALE_LOG2E; pend = 1;
        }
        PIN2(vf[6], vf[7]);
        o[0] = MFMA(vf[6], p1f, o[0]); FMAX2(0, 1); FMAX2(2, 3); FMAX2(4, 5); SB();
        o[1] = MFMA(vf[7], p1f, o[1]); FMAX2(6, 7); FMAX2(8, 9); FMAX2(10, 11); SB();
        GATE(mW, SV);
        kf[0] = KRD(0, RKN); SB();
        o[2] = MFMA(vf[8], p1f, o[2]); kf[1] = KRD(1, RKN); PIECE(S == 1, 0, soB, mW, SV); FMAX2(12, 13); SB();
        o[3] = MFMA(vf[9], p1f, o[3]); kf[2] = KRD(2, RKN); PIECE(S == 1, 1, soB, mW, SV); FMAX2(14, 15); SB();
        o[4] = MFMA(vf[10], p1f, o[4]); PIECE(S == 1, 2, soB, mW, SV); kf[3] = KRD(3, RKN); if (S == 2) PIECE(false, 3, soB, mW, SV); SB();
        o[5] = MFMA(vf[11], p1f, o[5]); if (S == 2) PIECE(false, 4, soB, mW, SV); kf[4] = KRD(4, RKN); if (S == 2) PIECE(false, 5, soB, mW, SV); SB();
        soA += TSTEP; soB += TSTEP;
        if (__builtin_expect(pend, 0)) {
            l *= alpha;
#pragma unroll
            for (int c = 0; c < 6; ++c) o[c] *= alpha;
            pend = 0;
        }
        ++it;
    };
    if (it + 1 < mysteps) {
        for (;;) {
            iter(IC<0>{}); if (it + 1 >= mysteps) break;
            iter(IC<1>{}); if (it + 1 >= mysteps) break;
            iter(IC<2>{}); if (it + 1 >= mysteps) break;
        }
    }
    STAMP(7);
    const int ph = it % 3;
    unsigned tk = ((S == 1) ? 1 + 2 * ph : (ph == 0 ? 1 : ph == 1 ? 0 : 2)) * SLOT;
    unsigned tv = ((S == 1) ? (ph == 2 ? 0 : 2 + 2 * ph) : 2 - ph) * SLOT;
    auto adv2 = [&](unsigned a) { const unsigned t = a + 2 * SLOT; return t >= NS * SLOT ? t - NS * SLOT : t; };
    if (mysteps > 0) {
        const unsigned mk = mW + tk, mv = mW + tv;
        GATE(mk, 0);
        const unsigned va = vA + tv, vb = vB + tv;
        bf16x8 vf[12];
#define VRT(i) ldv(((((i) % 6) & 1) ? vb : va) + (((i) % 6) >> 1) * 4096 + ((i) / 6) * 2048)
        vf[0] = VRT(0); PIECE(S == 2, 0, soA, mk, 0); vf[1] = VRT(1); PIECE(S == 2, 1, soA, mk, 0); vf[2] = VRT(2); PIECE(S == 2, 2, soA, mk, 0);
        vf[3] = VRT(3); if (S == 2) PIECE(true, 3, soA, mk, 0); vf[4] = VRT(4); if (S == 2) PIECE(true, 4, soA, mk, 0); vf[5] = VRT(5); if (S == 2) PIECE(true, 5, soA, mk, 0);
        float ls0 = 0.f, ls1 = 0.f;
#pragma unroll
        for (int i = 0; i < 16; i += 2) { x[i] = __builtin_amdgcn_exp2f(x[i]); x[i + 1] = __builtin_amdgcn_exp2f(x[i + 1]); ls0 += x[i]; ls1 += x[i + 1]; }
        l += ls0 + ls1;
#pragma unroll
        for (int i = 0; i < 4; ++i) { pa[i] = cvtpk(x[2 * i], x[2 * i + 1]); pb[i] = cvtpk(x[8 + 2 * i], x[9 + 2 * i]); }
        PIN4(pa[0], pa[1], pa[2], pa[3]); PIN4(pb[0], pb[1], pb[2], pb[3]);
        const bf16x8 p0f = __builtin_bit_cast(bf16x8, pa), p1f = __builtin_bit_cast(bf16x8, pb);
        SB();
        vf[6] = VRT(6); vf[7] = VRT(7);
        o[0] = MFMA(vf[0], p0f, o[0]); SB();
        vf[8] = VRT(8); vf[9] = VRT(9);
        o[1] = MFMA(vf[1], p0f, o[1]); SB();
        vf[10] = VRT(10); vf[11] = VRT(11);
        o[2] = MFMA(vf[2], p0f, o[2]); SB();
        o[3] = MFMA(vf[3], p0f, o[3]); o[4] = MFMA(vf[4], p0f, o[4]); o[5] = MFMA(vf[5], p0f, o[5]);
        o[0] = MFMA(vf[6], p1f, o[0]); o[1] = MFMA(vf[7], p1f, o[1]); SB();
        if (it + 1 < steps) {
            GATE(mv, 0);
            NOP0();
            PIECE(S == 1, 0, soB, mv, 0); NOP0(); PIECE(S == 1, 1, soB, mv, 0); NOP0(); PIECE(S == 1, 2, soB, mv, 0);
            if (S == 2) { NOP0(); PIECE(false, 3, soB, mv, 0); NOP0(); PIECE(false, 4, soB, mv, 0); NOP0(); PIECE(false, 5, soB, mv, 0); }
        }
        o[2] = MFMA(vf[8], p1f, o[2]); o[3] = MFMA(vf[9], p1f, o[3]); o[4] = MFMA(vf[10], p1f, o[4]); o[5] = MFMA(vf[11], p1f, o[5]);
        tk = adv2(tk); tv = adv2(tv); soA += TSTEP; soB += TSTEP;
        ++it;
    }
    STAMP(8);
    for (; it < steps; ++it) {
        const unsigned mk = mW + tk, mv = mW + tv;
        GATE(mk, 0);
        NOP0();
        PIECE(S == 2, 0, soA, mk, 0); NOP0(); PIECE(S == 2, 1, soA, mk, 0); NOP0(); PIECE(S == 2, 2, soA, mk, 0);
        if (S == 2) { NOP0(); PIECE(true, 3, soA, mk, 0); NOP0(); PIECE(true, 4, soA, mk, 0); NOP0(); PIECE(true, 5, soA, mk, 0); }
        if (it + 1 < steps) {
            GATE(mv, 0);
            NOP0();
            PIECE(S == 1, 0, soB, mv, 0); NOP0(); PIECE(S == 1, 1, soB, mv, 0); NOP0(); PIECE(S == 1, 2, soB, mv, 0);
            if (S == 2) { NOP0(); PIECE(false, 3, soB, mv, 0); NOP0(); PIECE(false, 4, soB, mv, 0); NOP0(); PIECE(false, 5, soB, mv, 0); }
        }
        tk = adv2(tk); tv = adv2(tv); soA += TSTEP; soB += TSTEP;
    }

    STAMP(10);
    l = swap_add(l);
    const __amdgpu_buffer_rsrc_t orsrc = __builtin_amdgcn_make_buffer_rsrc((void*)(op + (size_t)s0 * QSTRIDE), 0, (unsigned)L * QSTRIDE, 0x00020000);
    const unsigned stw = r32 * ORS + 8 * h;
    auto stage_block = [&](unsigned base, int c, const f32x16& z) {
#pragma unroll
        for (int g = 0; g < 4; ++g) {
            u32x2 v = {cvtpk(z[4 * g], z[4 * g + 1]), cvtpk(z[4 * g + 2], z[4 * g + 3])};
            *(LDSP(u32x2))(size_t)(base + (32 * c + 8 * g) * 2) = v;
        }
    };
    unsigned lrd[3], gof[3];
#pragma unroll
    for (int j = 0; j < 3; ++j) { const unsigned idx = 64 * j + lane, row = (idx * 2731) >> 16, ch = idx - 24 * row; lrd[j] = row * ORS + ch * 16; gof[j] = row * QSTRIDE + ch * 16; }
    wait_vm<0>();
    barrier();
    STAMP(11);
    if (S == 1) {
        if (mysteps > 0) {
            const unsigned stg = lds0 + w * (32 * ORS);
            const float inv = __builtin_amdgcn_rcpf(l);
#pragma unroll
            for (int c = 0; c < 6; ++c) { const f32x16 z = o[c] * inv; stage_block(stg + stw, c, z); }
            STAMP(15);
            const unsigned obase = (unsigned)pw * QSTRIDE + (unsigned)(2 * grp + hh) * ROWB;
#pragma unroll
            for (int k = 0; k < 4; ++k)
#pragma unroll
                for (int j = 0; j < 3; ++j) {
                    const u32x4 v = *(LDSP(u32x4))(size_t)(stg + lrd[j] + 8 * k * ORS);
                    __builtin_amdgcn_raw_buffer_store_b128(v, orsrc, gof[j], obase + 8 * k * QSTRIDE, STAUX);
                }
        }
    } else {
        const unsigned mine = lds0 + w * SLOT + lane * 16, other = lds0 + (w ^ 2) * SLOT + lane * 16;
        {
            f32x2 ml = {m, l};
            *(LDSP(f32x2))(size_t)(lds0 + 4 * SLOT + w * 512 + lane * 8) = ml;
        }
        auto send = [&](const f32x16& z, int c) {
#pragma unroll
            for (int g = 0; g < 4; ++g) { f32x4 y = {z[4 * g], z[4 * g + 1], z[4 * g + 2], z[4 * g + 3]}; *(LDSP(f32x4))(size_t)(mine + (c * 4 + g) * 1024) = y; }
        };
        if (sig) { send(o[0], 0); send(o[1], 1); send(o[2], 2); } else { send(o[3], 0); send(o[4], 1); send(o[5], 2); }
        STAMP(12);
        barrier();
        STAMP(13);
        const f32x2 mlo = *(LDSP(f32x2))(size_t)(lds0 + 4 * SLOT + (w ^ 2) * 512 + lane * 8);
        const float mx = max2f(m, mlo[0]);
        const float a0 = __builtin_amdgcn_exp2f((m - mx) * SCALE_LOG2E), a1 = __builtin_amdgcn_exp2f((mlo[0] - mx) * SCALE_LOG2E);
        const float inv = __builtin_amdgcn_rcpf(l * a0 + mlo[1] * a1);
        const float f0 = a0 * inv, f1 = a1 * inv;
        const unsigned stg = lds0 + 51200 + hh * (32 * ORS);
        auto merge = [&](const f32x16& xo, int c, int cd) {
            f32x16 z;
#pragma unroll
            for (int g = 0; g < 4; ++g) {
                const f32x4 y = *(LDSP(f32x4))(size_t)(other + (c * 4 + g) * 1024);
                const f32x4 t = {xo[4 * g], xo[4 * g + 1], xo[4 * g + 2], xo[4 * g + 3]};
                const f32x4 u = t * f0 + y * f1;
                z[4 * g] = u[0]; z[4 * g + 1] = u[1]; z[4 * g + 2] = u[2]; z[4 * g + 3] = u[3];
            }
            stage_block(stg + stw, cd, z);
        };
        if (sig) { merge(o[3], 0, 3); merge(o[4], 1, 4); merge(o[5], 2, 5); } else { merge(o[0], 0, 0); merge(o[1], 1, 1); merge(o[2], 2, 2); }
        STAMP(14);
        barrier();
        STAMP(15);
        const unsigned obase = (unsigned)(pw + 16 * sig) * QSTRIDE + (unsigned)(2 * grp + hh) * ROWB;
        const unsigned stgr = stg + 16 * sig * ORS;
#pragma unroll
        for (int k = 0; k < 2; ++k)
#pragma unroll
            for (int j = 0; j < 3; ++j) {
                const u32x4 v = *(LDSP(u32x4))(size_t)(stgr + lrd[j] + 8 * k * ORS);
                __builtin_amdgcn_raw_buffer_store_b128(v, orsrc, gof[j], obase + 8 * k * QSTRIDE, STAUX);
            }
    }
    STAMP(16);
}

template <int CTRL> DI int dpp_mov(int old, int x) { return __builtin_amdgcn_update_dpp(old, x, CTRL, 0xf, 0xf, false); }
DI int wave_incl_scan(int x) {
    x += dpp_mov<0x111>(0, x);
    x += dpp_mov<0x112>(0, x);
    x += dpp_mov<0x114>(0, x);
    x += dpp_mov<0x118>(0, x);
    x += __builtin_amdgcn_update_dpp(0, x, 0x142, 0xa, 0xf, false);
    x += __builtin_amdgcn_update_dpp(0, x, 0x143, 0xc, 0xf, false);
    return x;
}
DI int wave_max_nonneg(int x) {
    x = max(x, dpp_mov<0x111>(0, x));
    x = max(x, dpp_mov<0x112>(0, x));
    x = max(x, dpp_mov<0x114>(0, x));
    x = max(x, dpp_mov<0x118>(0, x));
    x = max(x, __builtin_amdgcn_update_dpp(0, x, 0x142, 0xa, 0xf, false));
    x = max(x, __builtin_amdgcn_update_dpp(0, x, 0x143, 0xc, 0xf, false));
    return __builtin_amdgcn_readlane(x, 63);
}

DI bool decode_slow(const int* __restrict__ cu, int nseq, int k, int& s0, int& L, int& p0, int& isx) {
    int nmax = 0;
    for (int s = 0; s < nseq; ++s) nmax = max(nmax, (cu[s + 1] - cu[s] + 31) >> 5);
    const int B = BSPLIT;
    int N = 0;
    for (int s = 0; s < nseq; ++s) { const int n = (cu[s + 1] - cu[s] + 31) >> 5; N += max(n - B, 0) + min(B >> 1, (n + 1) >> 1); }
    int rank = k;
    if (k < CUS_PER_XCD) { const int N2 = min(max(N - CUS_PER_XCD, 0), CUS_PER_XCD); rank = (k < N2) ? CUS_PER_XCD - 1 - k : k - N2; }
    if (rank >= N) return false;
    int jx = nmax, my = min(B >> 1, (nmax + 1) >> 1);
    for (;;) {
        const int x = (jx > B) && (((jx + 1) >> 1) >= 2 * my);
        const int thr = x ? 32 * (jx - 1) : 64 * (my - 1);
        for (int s = 0; s < nseq; ++s) {
            const int len = cu[s + 1] - cu[s];
            if (len > thr) { if (rank == 0) { s0 = cu[s]; L = len; p0 = thr; isx = x; return true; } --rank; }
        }
        if (x) --jx; else --my;
    }
}

__global__ __launch_bounds__(256, 2) void attn_varlen_kernel(const char* __restrict__ qp, const char* __restrict__ kp, const char* __restrict__ vp,
                                                             const int* __restrict__ cu, char* __restrict__ op, int nseq, int T
                                                             ) {
    __shared__ __attribute__((aligned(16))) char smem_[LDS_BYTES];
    const int grp = blockIdx.x & 7;
    const int k = blockIdx.x >> 3;
    const int lane = threadIdx.x & 63;

    if (PFMODE != 0 && k < 64) {
        const unsigned scr = (unsigned)(size_t)(lds_char_ptr)smem_ + 73728 + (threadIdx.x >> 6) * 256;
        const unsigned mscr = rfl(scr);
        const u32x4 kr = make_srd(kp + grp * ROWB, (unsigned)(T - 1) * KSTRIDE + ROWB);
        const u32x4 vr = make_srd(vp + grp * ROWB, (unsigned)(T - 1) * KSTRIDE + ROWB);
        const u32x4 qr = make_srd(qp + grp * 2 * ROWB, (unsigned)(T - 1) * QSTRIDE + 2 * ROWB);
        asm volatile("s_mov_b32 m0, %0" :: "s"(mscr) : "memory");
        const bool isv = rfl(threadIdx.x >> 7) != 0;
        const u32x4 r = isv ? vr : kr;
        for (unsigned base = 0; base < (unsigned)T * 3; base += 64 * 128) {
            const unsigned j3 = base + k * 128 + (threadIdx.x & 127), t3 = j3 / 3;
            const unsigned off = t3 * KSTRIDE + (j3 - 3 * t3) * 128;
            const unsigned j6 = 2 * base + k * 256 + threadIdx.x, t6 = j6 / 6;
            const unsigned offq = t6 * QSTRIDE + (j6 - 6 * t6) * 128;
            if (PFMODE == 2) asm volatile("buffer_load_dword %0, %1, 0 offen " PFPOL " lds" :: "v"(offq), "s"(qr) : "memory");
            asm volatile("buffer_load_dword %0, %1, 0 offen " PFPOL " lds" :: "v"(off), "s"(r) : "memory");
            if (PFMODE == 3) asm volatile("buffer_load_dword %0, %1, 0 offen " PFPOL " lds" :: "v"(offq), "s"(qr) : "memory");
        }
    }
    const int c0 = cu[min(lane, nseq)];
    const int c1 = cu[min(lane + 1, nseq)];
    const int l3 = (lane * 43) >> 7, r3 = lane - 3 * l3, st = l3 + 1;
    const int jx = 2 * st - 1 + r3;
    const bool okx = (r3 < 2) & (jx > BSPLIT) & (lane < 63), oky = (r3 == 2) & !(st & 1) & (st <= BSPLIT);
    const int thr = okx ? 32 * (jx - 1) : (oky ? 32 * (st - 2) : 0x7fffffff);
    const int len = c1 - c0;
    int cnt = 0;
#pragma unroll
    for (int s = 0; s < 8; ++s) cnt += (__builtin_amdgcn_readlane(len, s) > thr) ? 1 : 0;
    if (__builtin_expect(nseq > 8, 0)) {
        for (int s = 8; s < nseq; ++s) cnt += (__builtin_amdgcn_readlane(len, s & 63) > thr) ? 1 : 0;
    }
    const int incl = wave_incl_scan(cnt);
    const int N = __builtin_amdgcn_readlane(incl, 63);
    const int N2 = min(max(N - CUS_PER_XCD, 0), CUS_PER_XCD);
    const int rank = (k >= CUS_PER_XCD) ? k : ((k < N2) ? CUS_PER_XCD - 1 - k : k - N2);
    bool valid = rank < N;
    const int a = N - rank;
    const int lv = (__ffsll((long long)__ballot(incl >= a)) - 1) & 63;
    const int idx = __builtin_amdgcn_readlane(incl, lv) - a;
    int p0 = __builtin_amdgcn_readlane(thr, lv);
    int isx = __builtin_amdgcn_readlane(r3, lv) < 2;
    const bool has = len > p0;
    const unsigned long long mk = __ballot(has);
    const int below = __builtin_amdgcn_mbcnt_hi((unsigned)(mk >> 32), __builtin_amdgcn_mbcnt_lo((unsigned)mk, 0));
    const int sq = (__ffsll((long long)__ballot(has && below == idx)) - 1) & 63;
    int s0 = __builtin_amdgcn_readlane(c0, sq), L = __builtin_amdgcn_readlane(len, sq);
    if (__builtin_expect(nseq > 63 || __ballot(len > 32 * 42) != 0, 0)) valid = decode_slow(cu, nseq, k, s0, L, p0, isx);
    if (!valid) return;
    s0 = rfl(s0); L = rfl(L); p0 = rfl(p0); isx = rfl(isx);
    const unsigned lds0 = (unsigned)(size_t)(lds_char_ptr)smem_;
    if (isx) {
        const int n = (min(p0 + 32, L) + 31) >> 5;
        attn_item<2>(qp, kp, vp, op, grp, s0, L, p0, n, lds0 STPASS);
    } else {
        const int n = (min(p0 + 64, L) + 31) >> 5;
        attn_item<1>(qp, kp, vp, op, grp, s0, L, p0, n, lds0 STPASS);
    }
}

extern "C" void kernel_launch(void* const* d_in, const int* in_sizes, int n_in, void* d_out, int out_size, void* d_ws, size_t ws_size,
                              hipStream_t stream) {
    (void)n_in; (void)out_size; (void)d_ws; (void)ws_size;
    const int T = in_sizes[0] / (HQ * D);
    const int nseq = in_sizes[3] - 1;
    const int per_head = T / 32 + nseq + 1;
    attn_varlen_kernel<<<dim3(8 * per_head), dim3(256), 0, stream>>>((const char*)d_in[0], (const char*)d_in[1], (const char*)d_in[2],
                                                                    (const int*)d_in[3], (char*)d_out, nseq, T
                                                                    );
}
```

```cpp
#include <hip/hip_runtime.h>
#include <hip/hip_bf16.h>
#include <stdint.h>

typedef __bf16 bf16x8 __attribute__((ext_vector_type(8)));
typedef __bf16 bf16x2 __attribute__((ext_vector_type(2)));
typedef short s16x4 __attribute__((ext_vector_type(4)));
typedef short s16x8 __attribute__((ext_vector_type(8)));
typedef float f32x2 __attribute__((ext_vector_type(2)));
typedef float f32x4 __attribute__((ext_vector_type(4)));
typedef float f32x16 __attribute__((ext_vector_type(16)));
typedef unsigned u32x2 __attribute__((ext_vector_type(2)));
typedef unsigned u32x4 __attribute__((ext_vector_type(4)));
#define LDSP(T) __attribute__((address_space(3))) T*
typedef LDSP(char) lds_char_ptr;

#define DI __device__ __forceinline__
#define LDPOL ""
#define STAUX 16
#define PFPOL ""
#define PFMODE 0
#define CODEPF 26880
#define CODEWG 2
#define STAMP(i) do {} while (0)
#define STARG
#define STPASS

constexpr int HQ = 16, HKV = 8, D = 192;
constexpr unsigned QSTRIDE = HQ * D * 2;
constexpr unsigned KSTRIDE = HKV * D * 2;
constexpr unsigned ROWB = D * 2;
constexpr unsigned SLOT = 32 * ROWB;
constexpr unsigned ORS = 400;
constexpr unsigned LDS_BYTES = 77824;
constexpr float SCALE_LOG2E = 0.07216878364870322f * 1.4426950408889634f;
constexpr float THR_RAW = 8.0f / SCALE_LOG2E;
constexpr int CUS_PER_XCD = 32;
constexpr int BSPLIT = 8;

DI int rfl(int x) { return __builtin_amdgcn_readfirstlane(x); }
DI unsigned cvtpk(float lo, float hi) {
    f32x2 v = {lo, hi}; bf16x2 b = __builtin_convertvector(v, bf16x2); return __builtin_bit_cast(unsigned, b);
}
DI float max2f(float a, float b) { return __builtin_elementwise_maximum(a, b); }
DI float max3f(float a, float b, float c) { return max2f(max2f(a, b), c); }
DI float swap_max(float x) {
    auto r = __builtin_amdgcn_permlane32_swap(__float_as_uint(x), __float_as_uint(x), false, false);
    return max2f(__uint_as_float(r[0]), __uint_as_float(r[1]));
}
DI float swap_add(float x) {
    auto r = __builtin_amdgcn_permlane32_swap(__float_as_uint(x), __float_as_uint(x), false, false);
    return __uint_as_float(r[0]) + __uint_as_float(r[1]);
}
DI u32x4 make_srd(const void* base, unsigned nbytes) {
    unsigned long long a = (unsigned long long)base;
    u32x4 s; s[0] = rfl((unsigned)a); s[1] = rfl((unsigned)(a >> 32) & 0xffffu); s[2] = rfl(nbytes); s[3] = 0x00020000u;
    return s;
}
template <int IMM> DI void dma16(unsigned m0v, unsigned voff, u32x4 srd, unsigned soff) {
    asm volatile("s_mov_b32 m0, %0\n\ts_nop 0\n\tbuffer_load_dwordx4 %1, %2, %3 offen offset:%4 lds"
                 :: "s"(m0v), "v"(voff), "s"(srd), "s"(soff), "i"(IMM) : "memory");
}
template <int IMM, int NEXT> DI void dma16c(unsigned voff, u32x4 srd, unsigned soff, unsigned nbase) {
    asm volatile("buffer_load_dwordx4 %0, %1, %2 offen offset:%3 " LDPOL " lds\n\ts_add_u32 m0, %4, %5"
                 :: "v"(voff), "s"(srd), "s"(soff), "i"(IMM), "s"(nbase), "i"(NEXT) : "memory", "scc");
}
template <int IMM> DI void dma16e(unsigned voff, u32x4 srd, unsigned soff) {
    asm volatile("buffer_load_dwordx4 %0, %1, %2 offen offset:%3 " LDPOL " lds" :: "v"(voff), "s"(srd), "s"(soff), "i"(IMM) : "memory");
}
template <int N> DI void wait_vm() { asm volatile("s_waitcnt vmcnt(%0)" :: "i"(N) : "memory"); }
DI void barrier() { asm volatile("s_waitcnt lgkmcnt(0)\n\ts_barrier" ::: "memory"); }
#define SB() __builtin_amdgcn_sched_barrier(0)
#define MFMA(a, b, c) __builtin_amdgcn_mfma_f32_32x32x16_bf16((a), (b), (c), 0, 0, 0)

DI bf16x8 ldk(unsigned addr) { return *(LDSP(bf16x8))(size_t)addr; }
DI bf16x8 ldv(unsigned addr) {
    const s16x4 lo = __builtin_amdgcn_ds_read_tr16_b64_v4i16((LDSP(s16x4))(size_t)addr);
    const s16x4 hi = __builtin_amdgcn_ds_read_tr16_b64_v4i16((LDSP(s16x4))(size_t)(addr + 1024));
    return __builtin_bit_cast(bf16x8, __builtin_shufflevector(lo, hi, 0, 1, 2, 3, 4, 5, 6, 7));
}

template <int V> struct IC { static constexpr int value = V; };

template <int S>
DI void attn_item(const char* __restrict__ qp, const char* __restrict__ kp, const char* __restrict__ vp, char* __restrict__ op,
                  int grp, int s0, int L, int p0, int n, unsigned lds0 STARG) {
    constexpr int NS = (S == 1) ? 6 : 3;
    constexpr int PW = (S == 1) ? 3 : 6;
    constexpr unsigned TSTEP = 32 * S * KSTRIDE;
    const int tid = threadIdx.x, lane = tid & 63;
    const int w = rfl(tid >> 6);
    const int hh = w & 1, up = w >> 1;
    const int r32 = lane & 31, h = lane >> 5;
    const int sig = (S == 1) ? 0 : up;
    const int pw = (S == 1) ? p0 + 32 * up : p0;
    const int steps = (S == 1) ? n : (n + 1) >> 1;
    const int tdiag = pw >> 5;

    const int kend = min(L, 32 * n);
    const u32x4 ksrd = make_srd(kp + (size_t)s0 * KSTRIDE + grp * ROWB, (unsigned)(kend - 1) * KSTRIDE + ROWB);
    const u32x4 vsrd = make_srd(vp + (size_t)s0 * KSTRIDE + grp * ROWB, (unsigned)(kend - 1) * KSTRIDE + ROWB);
    const u32x4 qsrd = make_srd(qp + (size_t)s0 * QSTRIDE, (unsigned)L * QSTRIDE);

    const unsigned ring = lds0 + sig * NS * SLOT;
    const unsigned mW = ring + ((S == 1) ? w * 1024 : hh * 1024);
    const unsigned drow = (unsigned)(8 * hh + (lane >> 3) + ((S == 1) ? 16 * up : 0)) * KSTRIDE;
    const unsigned voffK = drow + 16u * ((lane & 7) ^ ((hh << 2) | (lane >> 4)));
    const unsigned voffV = drow + 16u * ((lane & 7) ^ (((lane >> 4) & 1) << 2));
    const unsigned voffK2 = voffK + 16 * KSTRIDE, voffV2 = voffV + 16 * KSTRIDE;
#define PIECE(ISV, j, so, mb, MO) do { const u32x4 sr_ = (ISV) ? vsrd : ksrd;                                                            \
        const unsigned v1_ = (ISV) ? voffV : voffK, v2_ = (ISV) ? voffV2 : voffK2;                                                       \
        if (j == 0) dma16c<0, (MO) + 4096 - 128>(v1_, sr_, so, mb);                                                                      \
        else if (j == 1) dma16c<128, (MO) + 8192 - 256>(v1_, sr_, so, mb);                                                               \
        else if (j == 2) { if (S == 1) dma16e<256>(v1_, sr_, so); else dma16c<256, (MO) + 2048>(v1_, sr_, so, mb); }                      \
        else if (j == 3) dma16c<0, (MO) + 2048 + 4096 - 128>(v2_, sr_, so, mb);                                                          \
        else if (j == 4) dma16c<128, (MO) + 2048 + 8192 - 256>(v2_, sr_, so, mb);                                                        \
        else dma16e<256>(v2_, sr_, so); } while (0)
#define SETM0(mb, MO) asm volatile("s_add_u32 m0, %0, %1\n\ts_nop 0" :: "s"(mb), "i"(MO) : "memory", "scc")
#define ELEM(ISV, so, mb, MO) do { SETM0(mb, MO); PIECE(ISV, 0, so, mb, MO); NOP0(); PIECE(ISV, 1, so, mb, MO); NOP0(); PIECE(ISV, 2, so, mb, MO);      \
        if (S == 2) { NOP0(); PIECE(ISV, 3, so, mb, MO); NOP0(); PIECE(ISV, 4, so, mb, MO); NOP0(); PIECE(ISV, 5, so, mb, MO); } } while (0)
#define NOP0() asm volatile("s_nop 0" ::: "memory")
    const unsigned so0 = (unsigned)(32 * sig) * KSTRIDE;

    STAMP(2);
    ELEM(false, so0, mW, 0);
    const unsigned qslot = (S == 1) ? lds0 + (2 + w) * SLOT : lds0 + (3 * hh + 2) * SLOT;
    {
        const unsigned qrow = (unsigned)(2 * grp + hh) * ROWB + (unsigned)pw * QSTRIDE;
        const unsigned l8 = (unsigned)(lane >> 3) * QSTRIDE;
        if (S == 1) {
            const unsigned vq0 = l8 + 16u * ((lane & 7) ^ (lane >> 4)), vq1 = l8 + 8 * QSTRIDE + 16u * ((lane & 7) ^ (4 | (lane >> 4)));
            const unsigned so2 = qrow + 16 * QSTRIDE;
            SETM0(qslot, 0);
            dma16c<0, 4096 - 128>(vq0, qsrd, qrow, qslot); NOP0(); dma16c<128, 8192 - 256>(vq0, qsrd, qrow, qslot); NOP0(); dma16c<256, 1024>(vq0, qsrd, qrow, qslot); NOP0();
            dma16c<0, 1024 + 4096 - 128>(vq1, qsrd, qrow, qslot); NOP0(); dma16c<128, 1024 + 8192 - 256>(vq1, qsrd, qrow, qslot); NOP0(); dma16c<256, 2048>(vq1, qsrd, qrow, qslot); NOP0();
            dma16c<0, 2048 + 4096 - 128>(vq0, qsrd, so2, qslot); NOP0(); dma16c<128, 2048 + 8192 - 256>(vq0, qsrd, so2, qslot); NOP0(); dma16c<256, 3072>(vq0, qsrd, so2, qslot); NOP0();
            dma16c<0, 3072 + 4096 - 128>(vq1, qsrd, so2, qslot); NOP0(); dma16c<128, 3072 + 8192 - 256>(vq1, qsrd, so2, qslot); NOP0(); dma16e<256>(vq1, qsrd, so2);
        } else {
            const unsigned vq = l8 + (unsigned)(8 * up) * QSTRIDE + 16u * ((lane & 7) ^ ((up << 2) | (lane >> 4))), vq2 = vq + 16 * QSTRIDE;
            const unsigned qm = qslot + up * 1024;
            SETM0(qm, 0);
            dma16c<0, 4096 - 128>(vq, qsrd, qrow, qm); NOP0(); dma16c<128, 8192 - 256>(vq, qsrd, qrow, qm); NOP0(); dma16c<256, 2048>(vq, qsrd, qrow, qm); NOP0();
            dma16c<0, 2048 + 4096 - 128>(vq2, qsrd, qrow, qm); NOP0(); dma16c<128, 2048 + 8192 - 256>(vq2, qsrd, qrow, qm); NOP0(); dma16e<256>(vq2, qsrd, qrow);
        }
    }
    ELEM(false, so0 + TSTEP, mW, SLOT);

    const int mysteps = (pw >= L) ? 0 : ((S == 1) ? tdiag + 1 : ((n - sig + 1) >> 1));
    const int itdiag = (S == 1) ? tdiag : (((tdiag - sig) & 1) ? -1 : (tdiag - sig) >> 1);
    const unsigned kb0 = r32 * 128 + 16 * (h ^ ((r32 >> 1) & 7));
    constexpr unsigned KB = (S == 1) ? SLOT : 0;
    unsigned kA = ring + KB + kb0, kBb = kA ^ 32, kC = kA ^ 64, kD = kA ^ 96;
    const int q4 = (lane & 15) >> 2, p4 = lane & 3, blk = (lane >> 4) & 1;
    unsigned vA = ring + (4 * h + q4) * 128 + 64 * (q4 >> 1) + 32 * blk + 8 * p4, vB = vA ^ 64;
    f32x16 o[6];
#pragma unroll
    for (int c = 0; c < 6; ++c) { o[c] = f32x16{}; asm volatile("" : "+v"(o[c])); }
    float m = -1e30f, l = 0.f, alpha = 1.f, mthr, mc;
    int pend = 0;
    int dq0 = pw + r32 - 4 * h;
    asm volatile("" : "+v"(kA), "+v"(kBb), "+v"(kC), "+v"(kD), "+v"(vA), "+v"(vB), "+v"(dq0));
    f32x16 s;
    float x[16];
#define KRD(i, OFF) ldk((((i) & 3) == 0 ? kA : ((i) & 3) == 1 ? kBb : ((i) & 3) == 2 ? kC : kD) + (OFF) + ((i) >> 2) * 4096)
#define VRD(i, OFF) ldv(((((i) % 6) & 1) ? vB : vA) + (OFF) + (((i) % 6) >> 1) * 4096 + ((i) / 6) * 2048)

    wait_vm<PW>();
    STAMP(4);
    barrier();
    bf16x8 q[12];
    {
        const unsigned a0 = qslot + kb0;
#pragma unroll
        for (int i = 0; i < 12; ++i) q[i] = ldk((a0 ^ (32 * (i & 3))) + (i >> 2) * 4096);
    }
    bf16x8 kf[12];
    {
        const unsigned a0 = ring + kb0;
#pragma unroll
        for (int i = 0; i < 6; ++i) kf[i] = ldk((a0 ^ (32 * (i & 3))) + (i >> 2) * 4096);
    }
    barrier();
    ELEM(true, so0, mW, 2 * SLOT);
    if (S == 1) {
        ELEM(false, so0 + 2 * TSTEP, mW, 3 * SLOT);
        ELEM(true, so0 + TSTEP, mW, 4 * SLOT);
        ELEM(false, so0 + 3 * TSTEP, mW, 5 * SLOT);
    }
    STAMP(5);

#define GATE(mb, MO) do { __builtin_amdgcn_s_waitcnt(((NS - 2) * PW) | 0x70);                                                            \
        asm volatile("s_barrier\n\ts_add_u32 m0, %0, %1" :: "s"(mb), "i"(MO) : "memory", "scc"); } while (0)
#define PIN1(a) asm volatile("" : "+v"(a))
#define PIN2(a, b) asm volatile("" : "+v"(a), "+v"(b))
#define PIN4(a, b, c, d) asm volatile("" : "+v"(a), "+v"(b), "+v"(c), "+v"(d))
    auto mask_diag = [&]() {
        int dq = dq0 - 32 * tdiag;
        asm volatile("" : "+v"(dq));
#pragma unroll
        for (int i = 0; i < 16; ++i) { const int c = (i & 3) + 8 * (i >> 2); s[i] = (c <= dq) ? s[i] : -__builtin_inff(); }
    };

    if (mysteps > 0) {
        const unsigned a0 = ring + kb0;
#pragma unroll
        for (int i = 6; i < 12; ++i) kf[i] = ldk((a0 ^ (32 * (i & 3))) + (i >> 2) * 4096);
        s = MFMA(kf[0], q[0], f32x16{});
#pragma unroll
        for (int i = 1; i < 12; ++i) s = MFMA(kf[i], q[i], s);
    }
    GATE(mW, 0);
    constexpr unsigned KS0 = (S == 1) ? 0 : SLOT;
    kf[0] = KRD(0, KS0);
    PIECE(S == 1, 0, so0 + 2 * TSTEP, mW, 0); kf[1] = KRD(1, KS0);
    PIECE(S == 1, 1, so0 + 2 * TSTEP, mW, 0); kf[2] = KRD(2, KS0);
    PIECE(S == 1, 2, so0 + 2 * TSTEP, mW, 0); kf[3] = KRD(3, KS0);
    if (S == 2) PIECE(false, 3, so0 + 2 * TSTEP, mW, 0);
    kf[4] = KRD(4, KS0);
    if (S == 2) { PIECE(false, 4, so0 + 2 * TSTEP, mW, 0); NOP0(); PIECE(false, 5, so0 + 2 * TSTEP, mW, 0); }
    if (mysteps > 0) {
        if (__builtin_expect(itdiag == 0, 0)) mask_diag();
        float a = max3f(s[0], s[1], s[2]), b = max3f(s[8], s[9], s[10]);
        a = max3f(a, s[3], s[4]); b = max3f(b, s[11], s[12]);
        a = max3f(a, s[5], s[6]); b = max3f(b, s[13], s[14]);
        a = max3f(a, s[7], s[15]);
        m = max2f(m, swap_max(max2f(a, b)));
    }
    mthr = m + THR_RAW; mc = -m * SCALE_LOG2E;
#pragma unroll
    for (int i = 0; i < 16; ++i) x[i] = fmaf(s[i], SCALE_LOG2E, mc);
    STAMP(6);

    int it = 0;
    unsigned soA = so0 + ((S == 1) ? 4 : 1) * TSTEP, soB = so0 + 3 * TSTEP;
    u32x4 pa, pb;
#define EXP2X(i, j) do { x[i] = __builtin_amdgcn_exp2f(x[i]); x[j] = __builtin_amdgcn_exp2f(x[j]); PIN2(x[i], x[j]); } while (0)
#define FMAX2(i, j) do { x[i] = fmaf(s[i], SCALE_LOG2E, mc); x[j] = fmaf(s[j], SCALE_LOG2E, mc); PIN2(x[i], x[j]); } while (0)
    auto iter = [&](auto PHC) {
        constexpr int PH = decltype(PHC)::value, PN = (PH + 1) % 3;
        constexpr unsigned SK = ((S == 1) ? 1 + 2 * PH : (PH == 0 ? 1 : PH == 1 ? 0 : 2)) * SLOT;
        constexpr unsigned SV = ((S == 1) ? (2 + 2 * PH) % 6 : 2 - PH) * SLOT;
        constexpr unsigned SKN = ((S == 1) ? 1 + 2 * PN : (PN == 0 ? 1 : PN == 1 ? 0 : 2)) * SLOT;
        constexpr unsigned RK = SK - KB, RKN = SKN - KB;
        float ls0, ls1;
        PIN2(kf[0], kf[1]);
        kf[5] = KRD(5, RK);
        s = MFMA(kf[0], q[0], f32x16{}); EXP2X(0, 1); SB();
        kf[6] = KRD(6, RK);
        s = MFMA(kf[1], q[1], s); EXP2X(2, 3); SB();
        PIN2(kf[2], kf[3]);
        kf[7] = KRD(7, RK);
        s = MFMA(kf[2], q[2], s); EXP2X(4, 5); SB();
        kf[8] = KRD(8, RK);
        s = MFMA(kf[3], q[3], s); EXP2X(6, 7); SB();
        PIN2(kf[4], kf[5]);
        kf[9] = KRD(9, RK);
        s = MFMA(kf[4], q[4], s); EXP2X(8, 9); ls0 = x[0] + x[2]; ls1 = x[1] + x[3]; PIN2(ls0, ls1); SB();
        kf[10] = KRD(10, RK); kf[11] = KRD(11, RK);
        s = MFMA(kf[5], q[5], s); EXP2X(10, 11); ls0 += x[4]; ls1 += x[5]; PIN2(ls0, ls1); SB();
        PIN2(kf[6], kf[7]);
        s = MFMA(kf[6], q[6], s); EXP2X(12, 13); pa[0] = cvtpk(x[0], x[1]); pa[1] = cvtpk(x[2], x[3]); PIN2(pa[0], pa[1]); SB();
        s = MFMA(kf[7], q[7], s); EXP2X(14, 15); pa[2] = cvtpk(x[4], x[5]); pa[3] = cvtpk(x[6], x[7]); PIN2(pa[2], pa[3]); SB();
        GATE(mW, SK);
        bf16x8 vf[12];
        vf[0] = VRD(0, SV); SB();
        s = MFMA(kf[8], q[8], s); vf[1] = VRD(1, SV); ls0 += x[6]; ls1 += x[7]; PIN2(ls0, ls1); SB();
        s = MFMA(kf[9], q[9], s); vf[2] = VRD(2, SV); PIECE(S == 2, 0, soA, mW, SK); ls0 += x[8]; ls1 += x[9]; PIN2(ls0, ls1); SB();
        s = MFMA(kf[10], q[10], s); vf[3] = VRD(3, SV); PIECE(S == 2, 1, soA, mW, SK); ls0 += x[10]; ls1 += x[11]; PIN2(ls0, ls1); SB();
        s = MFMA(kf[11], q[11], s); vf[4] = VRD(4, SV); PIECE(S == 2, 2, soA, mW, SK); ls0 += x[12]; ls1 += x[13]; PIN2(ls0, ls1); SB();
        const bf16x8 p0f = __builtin_bit_cast(bf16x8, pa);
        PIN2(vf[0], vf[1]);
        vf[5] = VRD(5, SV);
        o[0] = MFMA(vf[0], p0f, o[0]); if (S == 2) PIECE(true, 3, soA, mW, SK);
        pb[0] = cvtpk(x[8], x[9]); pb[1] = cvtpk(x[10], x[11]); PIN2(pb[0], pb[1]); ls0 += x[14]; ls1 += x[15]; PIN2(ls0, ls1); SB();
        vf[6] = VRD(6, SV);
        o[1] = MFMA(vf[1], p0f, o[1]); if (S == 2) PIECE(true, 4, soA, mW, SK);
        pb[2] = cvtpk(x[12], x[13]); pb[3] = cvtpk(x[14], x[15]); PIN2(pb[2], pb[3]); l += ls0; PIN1(l); SB();
        PIN2(vf[2], vf[3]);
        vf[7] = VRD(7, SV);
        o[2] = MFMA(vf[2], p0f, o[2]); if (S == 2) PIECE(true, 5, soA, mW, SK);
        l += ls1; PIN1(l); SB();
        const bf16x8 p1f = __builtin_bit_cast(bf16x8, pb);
        vf[8] = VRD(8, SV);
        o[3] = MFMA(vf[3], p0f, o[3]); SB();
        if (__builtin_expect(it + 1 == itdiag, 0)) mask_diag();
        float ma = max3f(s[0], s[1], s[2]), mb = max3f(s[8], s[9], s[10]);
        ma = max3f(ma, s[3], s[4]); mb = max3f(mb, s[11], s[12]);
        ma = max3f(ma, s[5], s[6]); mb = max3f(mb, s[13], s[14]); PIN2(ma, mb); SB();
        PIN2(vf[4], vf[5]);
        vf[9] = VRD(9, SV);
        o[4] = MFMA(vf[4], p0f, o[4]);
        ma = max3f(ma, s[7], s[15]);
        float pm = swap_max(max2f(ma, mb)); PIN1(pm); SB();
        vf[10] = VRD(10, SV); vf[11] = VRD(11, SV);
        o[5] = MFMA(vf[5], p0f, o[5]); SB();
        if (__builtin_expect(__any(pm > mthr), 0)) {
            const float mn = max2f(m, pm);
            alpha = __builtin_amdgcn_exp2f((m - mn) * SCALE_LOG2E);
            m = mn; mthr = m + THR_RAW; mc = -m * SCALE_LOG2E; pend = 1;
        }
        PIN2(vf[6], vf[7]);
        o[0] = MFMA(vf[6], p1f, o[0]); FMAX2(0, 1); FMAX2(2, 3); FMAX2(4, 5); SB();
        o[1] = MFMA(vf[7], p1f, o[1]); FMAX2(6, 7); FMAX2(8, 9); FMAX2(10, 11); SB();
        GATE(mW, SV);
        kf[0] = KRD(0, RKN); SB();
        o[2] = MFMA(vf[8], p1f, o[2]); kf[1] = KRD(1, RKN); PIECE(S == 1, 0, soB, mW, SV); FMAX2(12, 13); SB();
        o[3] = MFMA(vf[9], p1f, o[3]); kf[2] = KRD(2, RKN); PIECE(S == 1, 1, soB, mW, SV); FMAX2(14, 15); SB();
        o[4] = MFMA(vf[10], p1f, o[4]); PIECE(S == 1, 2, soB, mW, SV); kf[3] = KRD(3, RKN); if (S == 2) PIECE(false, 3, soB, mW, SV); SB();
        o[5] = MFMA(vf[11], p1f, o[5]); if (S == 2) PIECE(false, 4, soB, mW, SV); kf[4] = KRD(4, RKN); if (S == 2) PIECE(false, 5, soB, mW, SV); SB();
        soA += TSTEP; soB += TSTEP;
        if (__builtin_expect(pend, 0)) {
            l *= alpha;
#pragma unroll
            for (int c = 0; c < 6; ++c) o[c] *= alpha;
            pend = 0;
        }
        ++it;
    };
    if (it + 1 < mysteps) {
        for (;;) {
            iter(IC<0>{}); if (it + 1 >= mysteps) break;
            iter(IC<1>{}); if (it + 1 >= mysteps) break;
            iter(IC<2>{}); if (it + 1 >= mysteps) break;
        }
    }
    STAMP(7);
    const int ph = it % 3;
    unsigned tk = ((S == 1) ? 1 + 2 * ph : (ph == 0 ? 1 : ph == 1 ? 0 : 2)) * SLOT;
    unsigned tv = ((S == 1) ? (ph == 2 ? 0 : 2 + 2 * ph) : 2 - ph) * SLOT;
    auto adv2 = [&](unsigned a) { const unsigned t = a + 2 * SLOT; return t >= NS * SLOT ? t - NS * SLOT : t; };
    if (mysteps > 0) {
        const unsigned mk = mW + tk, mv = mW + tv;
        GATE(mk, 0);
        const unsigned va = vA + tv, vb = vB + tv;
        bf16x8 vf[12];
#define VRT(i) ldv(((((i) % 6) & 1) ? vb : va) + (((i) % 6) >> 1) * 4096 + ((i) / 6) * 2048)
        vf[0] = VRT(0); PIECE(S == 2, 0, soA, mk, 0); vf[1] = VRT(1); PIECE(S == 2, 1, soA, mk, 0); vf[2] = VRT(2); PIECE(S == 2, 2, soA, mk, 0);
        vf[3] = VRT(3); if (S == 2) PIECE(true, 3, soA, mk, 0); vf[4] = VRT(4); if (S == 2) PIECE(true, 4, soA, mk, 0); vf[5] = VRT(5); if (S == 2) PIECE(true, 5, soA, mk, 0);
        float ls0 = 0.f, ls1 = 0.f;
#pragma unroll
        for (int i = 0; i < 16; i += 2) { x[i] = __builtin_amdgcn_exp2f(x[i]); x[i + 1] = __builtin_amdgcn_exp2f(x[i + 1]); ls0 += x[i]; ls1 += x[i + 1]; }
        l += ls0 + ls1;
#pragma unroll
        for (int i = 0; i < 4; ++i) { pa[i] = cvtpk(x[2 * i], x[2 * i + 1]); pb[i] = cvtpk(x[8 + 2 * i], x[9 + 2 * i]); }
        PIN4(pa[0], pa[1], pa[2], pa[3]); PIN4(pb[0], pb[1], pb[2], pb[3]);
        const bf16x8 p0f = __builtin_bit_cast(bf16x8, pa), p1f = __builtin_bit_cast(bf16x8, pb);
        SB();
        vf[6] = VRT(6); vf[7] = VRT(7);
        o[0] = MFMA(vf[0], p0f, o[0]); SB();
        vf[8] = VRT(8); vf[9] = VRT(9);
        o[1] = MFMA(vf[1], p0f, o[1]); SB();
        vf[10] = VRT(10); vf[11] = VRT(11);
        o[2] = MFMA(vf[2], p0f, o[2]); SB();
        o[3] = MFMA(vf[3], p0f, o[3]); o[4] = MFMA(vf[4], p0f, o[4]); o[5] = MFMA(vf[5], p0f, o[5]);
        o[0] = MFMA(vf[6], p1f, o[0]); o[1] = MFMA(vf[7], p1f, o[1]); SB();
        if (it + 1 < steps) {
            GATE(mv, 0);
            NOP0();
            PIECE(S == 1, 0, soB, mv, 0); NOP0(); PIECE(S == 1, 1, soB, mv, 0); NOP0(); PIECE(S == 1, 2, soB, mv, 0);
            if (S == 2) { NOP0(); PIECE(false, 3, soB, mv, 0); NOP0(); PIECE(false, 4, soB, mv, 0); NOP0(); PIECE(false, 5, soB, mv, 0); }
        }
        o[2] = MFMA(vf[8], p1f, o[2]); o[3] = MFMA(vf[9], p1f, o[3]); o[4] = MFMA(vf[10], p1f, o[4]); o[5] = MFMA(vf[11], p1f, o[5]);
        tk = adv2(tk); tv = adv2(tv); soA += TSTEP; soB += TSTEP;
        ++it;
    }
    STAMP(8);
    for (; it < steps; ++it) {
        const unsigned mk = mW + tk, mv = mW + tv;
        GATE(mk, 0);
        NOP0();
        PIECE(S == 2, 0, soA, mk, 0); NOP0(); PIECE(S == 2, 1, soA, mk, 0); NOP0(); PIECE(S == 2, 2, soA, mk, 0);
        if (S == 2) { NOP0(); PIECE(true, 3, soA, mk, 0); NOP0(); PIECE(true, 4, soA, mk, 0); NOP0(); PIECE(true, 5, soA, mk, 0); }
        if (it + 1 < steps) {
            GATE(mv, 0);
            NOP0();
            PIECE(S == 1, 0, soB, mv, 0); NOP0(); PIECE(S == 1, 1, soB, mv, 0); NOP0(); PIECE(S == 1, 2, soB, mv, 0);
            if (S == 2) { NOP0(); PIECE(false, 3, soB, mv, 0); NOP0(); PIECE(false, 4, soB, mv, 0); NOP0(); PIECE(false, 5, soB, mv, 0); }
        }
        tk = adv2(tk); tv = adv2(tv); soA += TSTEP; soB += TSTEP;
    }

    STAMP(10);
    l = swap_add(l);
    const __amdgpu_buffer_rsrc_t orsrc = __builtin_amdgcn_make_buffer_rsrc((void*)(op + (size_t)s0 * QSTRIDE), 0, (unsigned)L * QSTRIDE, 0x00020000);
    const unsigned stw = r32 * ORS + 8 * h;
    auto stage_block = [&](unsigned base, int c, const f32x16& z) {
#pragma unroll
        for (int g = 0; g < 4; ++g) {
            u32x2 v = {cvtpk(z[4 * g], z[4 * g + 1]), cvtpk(z[4 * g + 2], z[4 * g + 3])};
            *(LDSP(u32x2))(size_t)(base + (32 * c + 8 * g) * 2) = v;
        }
    };
    unsigned lrd[3], gof[3];
#pragma unroll
    for (int j = 0; j < 3; ++j) { const unsigned idx = 64 * j + lane, row = (idx * 2731) >> 16, ch = idx - 24 * row; lrd[j] = row * ORS + ch * 16; gof[j] = row * QSTRIDE + ch * 16; }
    wait_vm<0>();
    barrier();
    STAMP(11);
    if (S == 1) {
        if (mysteps > 0) {
            const unsigned stg = lds0 + w * (32 * ORS);
            const float inv = __builtin_amdgcn_rcpf(l);
#pragma unroll
            for (int c = 0; c < 6; ++c) { const f32x16 z = o[c] * inv; stage_block(stg + stw, c, z); }
            STAMP(15);
            const unsigned obase = (unsigned)pw * QSTRIDE + (unsigned)(2 * grp + hh) * ROWB;
#pragma unroll
            for (int k = 0; k < 4; ++k)
#pragma unroll
                for (int j = 0; j < 3; ++j) {
                    const u32x4 v = *(LDSP(u32x4))(size_t)(stg + lrd[j] + 8 * k * ORS);
                    __builtin_amdgcn_raw_buffer_store_b128(v, orsrc, gof[j], obase + 8 * k * QSTRIDE, STAUX);
                }
        }
    } else {
        const unsigned mine = lds0 + w * SLOT + lane * 16, other = lds0 + (w ^ 2) * SLOT + lane * 16;
        {
            f32x2 ml = {m, l};
            *(LDSP(f32x2))(size_t)(lds0 + 4 * SLOT + w * 512 + lane * 8) = ml;
        }
        auto send = [&](const f32x16& z, int c) {
#pragma unroll
            for (int g = 0; g < 4; ++g) { f32x4 y = {z[4 * g], z[4 * g + 1], z[4 * g + 2], z[4 * g + 3]}; *(LDSP(f32x4))(size_t)(mine + (c * 4 + g) * 1024) = y; }
        };
        if (sig) { send(o[0], 0); send(o[1], 1); send(o[2], 2); } else { send(o[3], 0); send(o[4], 1); send(o[5], 2); }
        STAMP(12);
        barrier();
        STAMP(13);
        const f32x2 mlo = *(LDSP(f32x2))(size_t)(lds0 + 4 * SLOT + (w ^ 2) * 512 + lane * 8);
        const float mx = max2f(m, mlo[0]);
        const float a0 = __builtin_amdgcn_exp2f((m - mx) * SCALE_LOG2E), a1 = __builtin_amdgcn_exp2f((mlo[0] - mx) * SCALE_LOG2E);
        const float inv = __builtin_amdgcn_rcpf(l * a0 + mlo[1] * a1);
        const float f0 = a0 * inv, f1 = a1 * inv;
        const unsigned stg = lds0 + 51200 + hh * (32 * ORS);
        auto merge = [&](const f32x16& xo, int c, int cd) {
            f32x16 z;
#pragma unroll
            for (int g = 0; g < 4; ++g) {
                const f32x4 y = *(LDSP(f32x4))(size_t)(other + (c * 4 + g) * 1024);
                const f32x4 t = {xo[4 * g], xo[4 * g + 1], xo[4 * g + 2], xo[4 * g + 3]};
                const f32x4 u = t * f0 + y * f1;
                z[4 * g] = u[0]; z[4 * g + 1] = u[1]; z[4 * g + 2] = u[2]; z[4 * g + 3] = u[3];
            }
            stage_block(stg + stw, cd, z);
        };
        if (sig) { merge(o[3], 0, 3); merge(o[4], 1, 4); merge(o[5], 2, 5); } else { merge(o[0], 0, 0); merge(o[1], 1, 1); merge(o[2], 2, 2); }
        STAMP(14);
        barrier();
        STAMP(15);
        const unsigned obase = (unsigned)(pw + 16 * sig) * QSTRIDE + (unsigned)(2 * grp + hh) * ROWB;
        const unsigned stgr = stg + 16 * sig * ORS;
#pragma unroll
        for (int k = 0; k < 2; ++k)
#pragma unroll
            for (int j = 0; j < 3; ++j) {
                const u32x4 v = *(LDSP(u32x4))(size_t)(stgr + lrd[j] + 8 * k * ORS);
                __builtin_amdgcn_raw_buffer_store_b128(v, orsrc, gof[j], obase + 8 * k * QSTRIDE, STAUX);
            }
    }
    STAMP(16);
}

template <int CTRL> DI int dpp_mov(int old, int x) { return __builtin_amdgcn_update_dpp(old, x, CTRL, 0xf, 0xf, false); }
DI int wave_incl_scan(int x) {
    x += dpp_mov<0x111>(0, x);
    x += dpp_mov<0x112>(0, x);
    x += dpp_mov<0x114>(0, x);
    x += dpp_mov<0x118>(0, x);
    x += __builtin_amdgcn_update_dpp(0, x, 0x142, 0xa, 0xf, false);
    x += __builtin_amdgcn_update_dpp(0, x, 0x143, 0xc, 0xf, false);
    return x;
}
DI int wave_max_nonneg(int x) {
    x = max(x, dpp_mov<0x111>(0, x));
    x = max(x, dpp_mov<0x112>(0, x));
    x = max(x, dpp_mov<0x114>(0, x));
    x = max(x, dpp_mov<0x118>(0, x));
    x = max(x, __builtin_amdgcn_update_dpp(0, x, 0x142, 0xa, 0xf, false));
    x = max(x, __builtin_amdgcn_update_dpp(0, x, 0x143, 0xc, 0xf, false));
    return __builtin_amdgcn_readlane(x, 63);
}

DI bool decode_slow(const int* __restrict__ cu, int nseq, int k, int& s0, int& L, int& p0, int& isx) {
    int nmax = 0;
    for (int s = 0; s < nseq; ++s) nmax = max(nmax, (cu[s + 1] - cu[s] + 31) >> 5);
    const int B = BSPLIT;
    int N = 0;
    for (int s = 0; s < nseq; ++s) { const int n = (cu[s + 1] - cu[s] + 31) >> 5; N += max(n - B, 0) + min(B >> 1, (n + 1) >> 1); }
    int rank = k;
    if (k < CUS_PER_XCD) { const int N2 = min(max(N - CUS_PER_XCD, 0), CUS_PER_XCD); rank = (k < N2) ? CUS_PER_XCD - 1 - k : k - N2; }
    if (rank >= N) return false;
    int jx = nmax, my = min(B >> 1, (nmax + 1) >> 1);
    for (;;) {
        const int x = (jx > B) && (((jx + 1) >> 1) >= 2 * my);
        const int thr = x ? 32 * (jx - 1) : 64 * (my - 1);
        for (int s = 0; s < nseq; ++s) {
            const int len = cu[s + 1] - cu[s];
            if (len > thr) { if (rank == 0) { s0 = cu[s]; L = len; p0 = thr; isx = x; return true; } --rank; }
        }
        if (x) --jx; else --my;
    }
}

__global__ __launch_bounds__(256, 2) void attn_varlen_kernel(const char* __restrict__ qp, const char* __restrict__ kp, const char* __restrict__ vp,
                                                             const int* __restrict__ cu, char* __restrict__ op, int nseq, int T
                                                             ) {
    __shared__ __attribute__((aligned(16))) char smem_[LDS_BYTES];
    const int grp = blockIdx.x & 7;
    const int k = blockIdx.x >> 3;
    const int lane = threadIdx.x & 63;

    if (CODEPF > 0 && k < CODEWG) {
        unsigned long long pc; asm volatile("s_getpc_b64 %0" : "=s"(pc));
        const char* cb = (const char*)(pc & ~127ull);
        const unsigned mscr0 = rfl((unsigned)(size_t)(lds_char_ptr)smem_ + 73728 + 1024 + (threadIdx.x >> 6) * 256);
        asm volatile("s_mov_b32 m0, %0" :: "s"(mscr0) : "memory");
        for (unsigned o = threadIdx.x * 128; o < CODEPF; o += 256 * 128)
            asm volatile("global_load_lds_dword %0, off" :: "v"(cb + o) : "memory");
    }
    if (PFMODE != 0 && k < 64) {
        const unsigned scr = (unsigned)(size_t)(lds_char_ptr)smem_ + 73728 + (threadIdx.x >> 6) * 256;
        const unsigned mscr = rfl(scr);
        const u32x4 kr = make_srd(kp + grp * ROWB, (unsigned)(T - 1) * KSTRIDE + ROWB);
        const u32x4 vr = make_srd(vp + grp * ROWB, (unsigned)(T - 1) * KSTRIDE + ROWB);
        const u32x4 qr = make_srd(qp + grp * 2 * ROWB, (unsigned)(T - 1) * QSTRIDE + 2 * ROWB);
        asm volatile("s_mov_b32 m0, %0" :: "s"(mscr) : "memory");
        const bool isv = rfl(threadIdx.x >> 7) != 0;
        const u32x4 r = isv ? vr : kr;
        for (unsigned base = 0; base < (unsigned)T * 3; base += 64 * 128) {
            const unsigned j3 = base + k * 128 + (threadIdx.x & 127), t3 = j3 / 3;
            const unsigned off = t3 * KSTRIDE + (j3 - 3 * t3) * 128;
            const unsigned j6 = 2 * base + k * 256 + threadIdx.x, t6 = j6 / 6;
            const unsigned offq = t6 * QSTRIDE + (j6 - 6 * t6) * 128;
            if (PFMODE == 2) asm volatile("buffer_load_dword %0, %1, 0 offen " PFPOL " lds" :: "v"(offq), "s"(qr) : "memory");
            asm volatile("buffer_load_dword %0, %1, 0 offen " PFPOL " lds" :: "v"(off), "s"(r) : "memory");
            if (PFMODE == 3) asm volatile("buffer_load_dword %0, %1, 0 offen " PFPOL " lds" :: "v"(offq), "s"(qr) : "memory");
        }
    }
    const int c0 = cu[min(lane, nseq)];
    const int c1 = cu[min(lane + 1, nseq)];
    const int l3 = (lane * 43) >> 7, r3 = lane - 3 * l3, st = l3 + 1;
    const int jx = 2 * st - 1 + r3;
    const bool okx = (r3 < 2) & (jx > BSPLIT) & (lane < 63), oky = (r3 == 2) & !(st & 1) & (st <= BSPLIT);
    const int thr = okx ? 32 * (jx - 1) : (oky ? 32 * (st - 2) : 0x7fffffff);
    const int len = c1 - c0;
    int cnt = 0;
#pragma unroll
    for (int s = 0; s < 8; ++s) cnt += (__builtin_amdgcn_readlane(len, s) > thr) ? 1 : 0;
    if (__builtin_expect(nseq > 8, 0)) {
        for (int s = 8; s < nseq; ++s) cnt += (__builtin_amdgcn_readlane(len, s & 63) > thr) ? 1 : 0;
    }
    const int incl = wave_incl_scan(cnt);
    const int N = __builtin_amdgcn_readlane(incl, 63);
    const int N2 = min(max(N - CUS_PER_XCD, 0), CUS_PER_XCD);
    const int rank = (k >= CUS_PER_XCD) ? k : ((k < N2) ? CUS_PER_XCD - 1 - k : k - N2);
    bool valid = rank < N;
    const int a = N - rank;
    const int lv = (__ffsll((long long)__ballot(incl >= a)) - 1) & 63;
    const int idx = __builtin_amdgcn_readlane(incl, lv) - a;
    int p0 = __builtin_amdgcn_readlane(thr, lv);
    int isx = __builtin_amdgcn_readlane(r3, lv) < 2;
    const bool has = len > p0;
    const unsigned long long mk = __ballot(has);
    const int below = __builtin_amdgcn_mbcnt_hi((unsigned)(mk >> 32), __builtin_amdgcn_mbcnt_lo((unsigned)mk, 0));
    const int sq = (__ffsll((long long)__ballot(has && below == idx)) - 1) & 63;
    int s0 = __builtin_amdgcn_readlane(c0, sq), L = __builtin_amdgcn_readlane(len, sq);
    if (__builtin_expect(nseq > 63 || __ballot(len > 32 * 42) != 0, 0)) valid = decode_slow(cu, nseq, k, s0, L, p0, isx);
    if (!valid) return;
    s0 = rfl(s0); L = rfl(L); p0 = rfl(p0); isx = rfl(isx);
    const unsigned lds0 = (unsigned)(size_t)(lds_char_ptr)smem_;
    if (isx) {
        const int n = (min(p0 + 32, L) + 31) >> 5;
        attn_item<2>(qp, kp, vp, op, grp, s0, L, p0, n, lds0 STPASS);
    } else {
        const int n = (min(p0 + 64, L) + 31) >> 5;
        attn_item<1>(qp, kp, vp, op, grp, s0, L, p0, n, lds0 STPASS);
    }
}

extern "C" void kernel_launch(void* const* d_in, const int* in_sizes, int n_in, void* d_out, int out_size, void* d_ws, size_t ws_size,
                              hipStream_t stream) {
    (void)n_in; (void)out_size; (void)d_ws; (void)ws_size;
    const int T = in_sizes[0] / (HQ * D);
    const int nseq = in_sizes[3] - 1;
    const int per_head = T / 32 + nseq + 1;
    attn_varlen_kernel<<<dim3(8 * per_head), dim3(256), 0, stream>>>((const char*)d_in[0], (const char*)d_in[1], (const char*)d_in[2],
                                                                    (const int*)d_in[3], (char*)d_out, nseq, T
                                                                    );
}
```

```cpp
#include <hip/hip_runtime.h>
#include <hip/hip_bf16.h>
#include <stdint.h>

typedef __bf16 bf16x8 __attribute__((ext_vector_type(8)));
typedef __bf16 bf16x2 __attribute__((ext_vector_type(2)));
typedef short s16x4 __attribute__((ext_vector_type(4)));
typedef short s16x8 __attribute__((ext_vector_type(8)));
typedef float f32x2 __attribute__((ext_vector_type(2)));
typedef float f32x4 __attribute__((ext_vector_type(4)));
typedef float f32x16 __attribute__((ext_vector_type(16)));
typedef unsigned u32x2 __attribute__((ext_vector_type(2)));
typedef unsigned u32x4 __attribute__((ext_vector_type(4)));
#define LDSP(T) __attribute__((address_space(3))) T*
typedef LDSP(char) lds_char_ptr;

#define DI __device__ __forceinline__
#define LDPOL ""
#define FTPOL ""
#define PAIRMODE 1
#define PRIO 2
#define QPOL ""
#define STAUX 16
#define PFPOL ""
#define PFMODE 0
#define CODEPF 0
#define CODEWG 2
#define STAMP(i) do {} while (0)
#define STARG
#define STPASS

constexpr int HQ = 16, HKV = 8, D = 192;
constexpr unsigned QSTRIDE = HQ * D * 2;
constexpr unsigned KSTRIDE = HKV * D * 2;
constexpr unsigned ROWB = D * 2;
constexpr unsigned SLOT = 32 * ROWB;
constexpr unsigned ORS = 400;
constexpr unsigned LDS_BYTES = 77824;
constexpr float SCALE_LOG2E = 0.07216878364870322f * 1.4426950408889634f;
constexpr float THR_RAW = 8.0f / SCALE_LOG2E;
constexpr int CUS_PER_XCD = 32;
constexpr int PF0 = 56;
constexpr int BSPLIT = 8;

DI int rfl(int x) { return __builtin_amdgcn_readfirstlane(x); }
DI unsigned cvtpk(float lo, float hi) {
    f32x2 v = {lo, hi}; bf16x2 b = __builtin_convertvector(v, bf16x2); return __builtin_bit_cast(unsigned, b);
}
DI float max2f(float a, float b) { return __builtin_elementwise_maximum(a, b); }
DI float max3f(float a, float b, float c) { return max2f(max2f(a, b), c); }
DI float swap_max(float x) {
    auto r = __builtin_amdgcn_permlane32_swap(__float_as_uint(x), __float_as_uint(x), false, false);
    return max2f(__uint_as_float(r[0]), __uint_as_float(r[1]));
}
DI float swap_add(float x) {
    auto r = __builtin_amdgcn_permlane32_swap(__float_as_uint(x), __float_as_uint(x), false, false);
    return __uint_as_float(r[0]) + __uint_as_float(r[1]);
}
DI u32x4 make_srd(const void* base, unsigned nbytes) {
    unsigned long long a = (unsigned long long)base;
    u32x4 s; s[0] = rfl((unsigned)a); s[1] = rfl((unsigned)(a >> 32) & 0xffffu); s[2] = rfl(nbytes); s[3] = 0x00020000u;
    return s;
}
template <int IMM> DI void dma16(unsigned m0v, unsigned voff, u32x4 srd, unsigned soff) {
    asm volatile("s_mov_b32 m0, %0\n\ts_nop 0\n\tbuffer_load_dwordx4 %1, %2, %3 offen offset:%4 lds"
                 :: "s"(m0v), "v"(voff), "s"(srd), "s"(soff), "i"(IMM) : "memory");
}
template <int IMM, int NEXT> DI void dma16c_nt(unsigned voff, u32x4 srd, unsigned soff, unsigned nbase) {
    asm volatile("buffer_load_dwordx4 %0, %1, %2 offen offset:%3 " FTPOL " lds\n\ts_add_u32 m0, %4, %5"
                 :: "v"(voff), "s"(srd), "s"(soff), "i"(IMM), "s"(nbase), "i"(NEXT) : "memory", "scc");
}
template <int IMM> DI void dma16e_nt(unsigned voff, u32x4 srd, unsigned soff) {
    asm volatile("buffer_load_dwordx4 %0, %1, %2 offen offset:%3 " FTPOL " lds" :: "v"(voff), "s"(srd), "s"(soff), "i"(IMM) : "memory");
}
template <int IMM, int NEXT> DI void dma16c(unsigned voff, u32x4 srd, unsigned soff, unsigned nbase) {
    asm volatile("buffer_load_dwordx4 %0, %1, %2 offen offset:%3 " LDPOL " lds\n\ts_add_u32 m0, %4, %5"
                 :: "v"(voff), "s"(srd), "s"(soff), "i"(IMM), "s"(nbase), "i"(NEXT) : "memory", "scc");
}
template <int IMM, int NEXT> DI void dmaqc(unsigned voff, u32x4 srd, unsigned soff, unsigned nbase) {
    asm volatile("buffer_load_dwordx4 %0, %1, %2 offen offset:%3 " QPOL " lds\n\ts_add_u32 m0, %4, %5"
                 :: "v"(voff), "s"(srd), "s"(soff), "i"(IMM), "s"(nbase), "i"(NEXT) : "memory", "scc");
}
template <int IMM> DI void dmaqe(unsigned voff, u32x4 srd, unsigned soff) {
    asm volatile("buffer_load_dwordx4 %0, %1, %2 offen offset:%3 " QPOL " lds" :: "v"(voff), "s"(srd), "s"(soff), "i"(IMM) : "memory");
}
template <int IMM> DI void dma16e(unsigned voff, u32x4 srd, unsigned soff) {
    asm volatile("buffer_load_dwordx4 %0, %1, %2 offen offset:%3 " LDPOL " lds" :: "v"(voff), "s"(srd), "s"(soff), "i"(IMM) : "memory");
}
template <int N> DI void wait_vm() { asm volatile("s_waitcnt vmcnt(%0)" :: "i"(N) : "memory"); }
DI void barrier() { asm volatile("s_waitcnt lgkmcnt(0)\n\ts_barrier" ::: "memory"); }
#define SB() __builtin_amdgcn_sched_barrier(0)
#define MFMA(a, b, c) __builtin_amdgcn_mfma_f32_32x32x16_bf16((a), (b), (c), 0, 0, 0)

DI bf16x8 ldk(unsigned addr) { return *(LDSP(bf16x8))(size_t)addr; }
DI bf16x8 ldv(unsigned addr) {
    const s16x4 lo = __builtin_amdgcn_ds_read_tr16_b64_v4i16((LDSP(s16x4))(size_t)addr);
    const s16x4 hi = __builtin_amdgcn_ds_read_tr16_b64_v4i16((LDSP(s16x4))(size_t)(addr + 1024));
    return __builtin_bit_cast(bf16x8, __builtin_shufflevector(lo, hi, 0, 1, 2, 3, 4, 5, 6, 7));
}

template <int V> struct IC { static constexpr int value = V; };

template <int S>
DI void attn_item(const char* __restrict__ qp, const char* __restrict__ kp, const char* __restrict__ vp, char* __restrict__ op,
                  int grp, int s0, int L, int p0, int n, unsigned lds0 STARG) {
    constexpr int NS = (S == 1) ? 6 : 3;
    constexpr int PW = (S == 1) ? 3 : 6;
    constexpr unsigned TSTEP = 32 * S * KSTRIDE;
    const int tid = threadIdx.x, lane = tid & 63;
    const int w = rfl(tid >> 6);
    const int hh = w & 1, up = w >> 1;
    const int r32 = lane & 31, h = lane >> 5;
    const int sig = (S == 1) ? 0 : up;
    const int pw = (S == 1) ? p0 + 32 * up : p0;
    const int steps = (S == 1) ? n : (n + 1) >> 1;
    const int tdiag = pw >> 5;

    const int kend = min(L, 32 * n);
    const u32x4 ksrd = make_srd(kp + (size_t)s0 * KSTRIDE + grp * ROWB, (unsigned)(kend - 1) * KSTRIDE + ROWB);
    const u32x4 vsrd = make_srd(vp + (size_t)s0 * KSTRIDE + grp * ROWB, (unsigned)(kend - 1) * KSTRIDE + ROWB);
    const u32x4 qsrd = make_srd(qp + (size_t)s0 * QSTRIDE, (unsigned)L * QSTRIDE);

    const unsigned ring = lds0 + sig * NS * SLOT;
    const unsigned mW = ring + ((S == 1) ? w * 1024 : hh * 1024);
    const unsigned drow = (unsigned)(8 * hh + (lane >> 3) + ((S == 1) ? 16 * up : 0)) * KSTRIDE;
    const unsigned voffK = drow + 16u * ((lane & 7) ^ ((hh << 2) | (lane >> 4)));
    const unsigned voffV = drow + 16u * ((lane & 7) ^ (((lane >> 4) & 1) << 2));
    const unsigned voffK2 = voffK + 16 * KSTRIDE, voffV2 = voffV + 16 * KSTRIDE;
#define PIECE(ISV, j, so, mb, MO) do { const u32x4 sr_ = (ISV) ? vsrd : ksrd;                                                            \
        const unsigned v1_ = (ISV) ? voffV : voffK, v2_ = (ISV) ? voffV2 : voffK2;                                                       \
        if (j == 0) dma16c<0, (MO) + 4096 - 128>(v1_, sr_, so, mb);                                                                      \
        else if (j == 1) dma16c<128, (MO) + 8192 - 256>(v1_, sr_, so, mb);                                                               \
        else if (j == 2) { if (S == 1) dma16e<256>(v1_, sr_, so); else dma16c<256, (MO) + 2048>(v1_, sr_, so, mb); }                      \
        else if (j == 3) dma16c<0, (MO) + 2048 + 4096 - 128>(v2_, sr_, so, mb);                                                          \
        else if (j == 4) dma16c<128, (MO) + 2048 + 8192 - 256>(v2_, sr_, so, mb);                                                        \
        else dma16e<256>(v2_, sr_, so); } while (0)
#define PIECEN(ISV, j, so, mb, MO) do { const u32x4 sr_ = (ISV) ? vsrd : ksrd;                                                           \
        const unsigned v1_ = (ISV) ? voffV : voffK, v2_ = (ISV) ? voffV2 : voffK2;                                                       \
        if (j == 0) dma16c_nt<0, (MO) + 4096 - 128>(v1_, sr_, so, mb);                                                                   \
        else if (j == 1) dma16c_nt<128, (MO) + 8192 - 256>(v1_, sr_, so, mb);                                                            \
        else if (j == 2) { if (S == 1) dma16e_nt<256>(v1_, sr_, so); else dma16c_nt<256, (MO) + 2048>(v1_, sr_, so, mb); }                \
        else if (j == 3) dma16c_nt<0, (MO) + 2048 + 4096 - 128>(v2_, sr_, so, mb);                                                       \
        else if (j == 4) dma16c_nt<128, (MO) + 2048 + 8192 - 256>(v2_, sr_, so, mb);                                                     \
        else dma16e_nt<256>(v2_, sr_, so); } while (0)
#define ELEMN(ISV, so, mb, MO) do { SETM0(mb, MO); PIECEN(ISV, 0, so, mb, MO); NOP0(); PIECEN(ISV, 1, so, mb, MO); NOP0(); PIECEN(ISV, 2, so, mb, MO);  \
        if (S == 2) { NOP0(); PIECEN(ISV, 3, so, mb, MO); NOP0(); PIECEN(ISV, 4, so, mb, MO); NOP0(); PIECEN(ISV, 5, so, mb, MO); } } while (0)
#define SETM0(mb, MO) asm volatile("s_add_u32 m0, %0, %1\n\ts_nop 0" :: "s"(mb), "i"(MO) : "memory", "scc")
#define ELEM(ISV, so, mb, MO) do { SETM0(mb, MO); PIECE(ISV, 0, so, mb, MO); NOP0(); PIECE(ISV, 1, so, mb, MO); NOP0(); PIECE(ISV, 2, so, mb, MO);      \
        if (S == 2) { NOP0(); PIECE(ISV, 3, so, mb, MO); NOP0(); PIECE(ISV, 4, so, mb, MO); NOP0(); PIECE(ISV, 5, so, mb, MO); } } while (0)
#define NOP0() asm volatile("s_nop 0" ::: "memory")
    const unsigned so0 = (unsigned)(32 * (n - 1 - sig)) * KSTRIDE;

    STAMP(2);
    ELEMN(false, so0, mW, 0);
    const unsigned qslot = (S == 1) ? lds0 + (2 + w) * SLOT : lds0 + (3 * hh + 2) * SLOT;
    {
        const unsigned qrow = (unsigned)(2 * grp + hh) * ROWB + (unsigned)pw * QSTRIDE;
        const unsigned l8 = (unsigned)(lane >> 3) * QSTRIDE;
        if (S == 1) {
            const unsigned vq0 = l8 + 16u * ((lane & 7) ^ (lane >> 4)), vq1 = l8 + 8 * QSTRIDE + 16u * ((lane & 7) ^ (4 | (lane >> 4)));
            const unsigned so2 = qrow + 16 * QSTRIDE;
            SETM0(qslot, 0);
            dmaqc<0, 4096 - 128>(vq0, qsrd, qrow, qslot); NOP0(); dmaqc<128, 8192 - 256>(vq0, qsrd, qrow, qslot); NOP0(); dmaqc<256, 1024>(vq0, qsrd, qrow, qslot); NOP0();
            dmaqc<0, 1024 + 4096 - 128>(vq1, qsrd, qrow, qslot); NOP0(); dmaqc<128, 1024 + 8192 - 256>(vq1, qsrd, qrow, qslot); NOP0(); dmaqc<256, 2048>(vq1, qsrd, qrow, qslot); NOP0();
            dmaqc<0, 2048 + 4096 - 128>(vq0, qsrd, so2, qslot); NOP0(); dmaqc<128, 2048 + 8192 - 256>(vq0, qsrd, so2, qslot); NOP0(); dmaqc<256, 3072>(vq0, qsrd, so2, qslot); NOP0();
            dmaqc<0, 3072 + 4096 - 128>(vq1, qsrd, so2, qslot); NOP0(); dmaqc<128, 3072 + 8192 - 256>(vq1, qsrd, so2, qslot); NOP0(); dmaqe<256>(vq1, qsrd, so2);
        } else {
            const unsigned vq = l8 + (unsigned)(8 * up) * QSTRIDE + 16u * ((lane & 7) ^ ((up << 2) | (lane >> 4))), vq2 = vq + 16 * QSTRIDE;
            const unsigned qm = qslot + up * 1024;
            SETM0(qm, 0);
            dmaqc<0, 4096 - 128>(vq, qsrd, qrow, qm); NOP0(); dmaqc<128, 8192 - 256>(vq, qsrd, qrow, qm); NOP0(); dmaqc<256, 2048>(vq, qsrd, qrow, qm); NOP0();
            dmaqc<0, 2048 + 4096 - 128>(vq2, qsrd, qrow, qm); NOP0(); dmaqc<128, 2048 + 8192 - 256>(vq2, qsrd, qrow, qm); NOP0(); dmaqe<256>(vq2, qsrd, qrow);
        }
    }
    if (S == 1) ELEMN(false, so0 - TSTEP, mW, SLOT); else ELEM(false, so0 - TSTEP, mW, SLOT);

    const bool act = (S == 2) || pw < L;
    const bool late = (S == 1) && act && tdiag != n - 1;
    const int itend = (S == 1) ? n - 1 : ((n - sig + 1) >> 1) - 1;
    const unsigned kb0 = r32 * 128 + 16 * (h ^ ((r32 >> 1) & 7));
    constexpr unsigned KB = (S == 1) ? SLOT : 0;
    unsigned kA = ring + KB + kb0, kBb = kA ^ 32, kC = kA ^ 64, kD = kA ^ 96;
    const int q4 = (lane & 15) >> 2, p4 = lane & 3, blk = (lane >> 4) & 1;
    unsigned vA = ring + (4 * h + q4) * 128 + 64 * (q4 >> 1) + 32 * blk + 8 * p4, vB = vA ^ 64;
    f32x16 o[6];
#pragma unroll
    for (int c = 0; c < 6; ++c) { o[c] = f32x16{}; asm volatile("" : "+v"(o[c])); }
    float m = -1e30f, l = 0.f, alpha = 1.f, mthr, mc;
    int pend = 0;
    int dq0 = pw + r32 - 4 * h;
    asm volatile("" : "+v"(kA), "+v"(kBb), "+v"(kC), "+v"(kD), "+v"(vA), "+v"(vB), "+v"(dq0));
    f32x16 s;
    float x[16];
#define KRD(i, OFF) ldk((((i) & 3) == 0 ? kA : ((i) & 3) == 1 ? kBb : ((i) & 3) == 2 ? kC : kD) + (OFF) + ((i) >> 2) * 4096)
#define VRD(i, OFF) ldv(((((i) % 6) & 1) ? vB : vA) + (OFF) + (((i) % 6) >> 1) * 4096 + ((i) / 6) * 2048)

    wait_vm<PW>();
    STAMP(4);
    barrier();
    bf16x8 q[12];
    {
        const unsigned a0 = qslot + kb0;
#pragma unroll
        for (int i = 0; i < 12; ++i) q[i] = ldk((a0 ^ (32 * (i & 3))) + (i >> 2) * 4096);
    }
    bf16x8 kf[12];
    {
        const unsigned a0 = ring + kb0;
#pragma unroll
        for (int i = 0; i < 6; ++i) kf[i] = ldk((a0 ^ (32 * (i & 3))) + (i >> 2) * 4096);
    }
    barrier();
    ELEMN(true, so0, mW, 2 * SLOT);
    if (S == 1) {
        ELEM(false, so0 - 2 * TSTEP, mW, 3 * SLOT);
        ELEMN(true, so0 - TSTEP, mW, 4 * SLOT);
        ELEM(false, so0 - 3 * TSTEP, mW, 5 * SLOT);
    }
    STAMP(5);

#define GATE(mb, MO) do { __builtin_amdgcn_s_waitcnt(((NS - 2) * PW) | 0x70);                                                            \
        asm volatile("s_barrier\n\ts_add_u32 m0, %0, %1" :: "s"(mb), "i"(MO) : "memory", "scc"); } while (0)
#define PIN1(a) asm volatile("" : "+v"(a))
#define PIN2(a, b) asm volatile("" : "+v"(a), "+v"(b))
#define PIN4(a, b, c, d) asm volatile("" : "+v"(a), "+v"(b), "+v"(c), "+v"(d))
    auto mask_diag = [&]() {
        int dq = dq0 - 32 * tdiag;
        asm volatile("" : "+v"(dq));
#pragma unroll
        for (int i = 0; i < 16; ++i) { const int c = (i & 3) + 8 * (i >> 2); s[i] = (c <= dq) ? s[i] : -__builtin_inff(); }
    };

    if (act && !late) {
        const unsigned a0 = ring + kb0;
#pragma unroll
        for (int i = 6; i < 12; ++i) kf[i] = ldk((a0 ^ (32 * (i & 3))) + (i >> 2) * 4096);
        s = MFMA(kf[0], q[0], f32x16{});
#pragma unroll
        for (int i = 1; i < 12; ++i) s = MFMA(kf[i], q[i], s);
    }
    GATE(mW, 0);
    constexpr unsigned KS0 = (S == 1) ? 0 : SLOT;
    kf[0] = KRD(0, KS0);
    PIECE(S == 1, 0, so0 - 2 * TSTEP, mW, 0); kf[1] = KRD(1, KS0);
    PIECE(S == 1, 1, so0 - 2 * TSTEP, mW, 0); kf[2] = KRD(2, KS0);
    PIECE(S == 1, 2, so0 - 2 * TSTEP, mW, 0); kf[3] = KRD(3, KS0);
    if (S == 2) PIECE(false, 3, so0 - 2 * TSTEP, mW, 0);
    kf[4] = KRD(4, KS0);
    if (S == 2) { PIECE(false, 4, so0 - 2 * TSTEP, mW, 0); NOP0(); PIECE(false, 5, so0 - 2 * TSTEP, mW, 0); }
    auto first_max = [&]() {
        float a = max3f(s[0], s[1], s[2]), b = max3f(s[8], s[9], s[10]);
        a = max3f(a, s[3], s[4]); b = max3f(b, s[11], s[12]);
        a = max3f(a, s[5], s[6]); b = max3f(b, s[13], s[14]);
        a = max3f(a, s[7], s[15]);
        m = max2f(m, swap_max(max2f(a, b)));
        mthr = m + THR_RAW; mc = -m * SCALE_LOG2E;
#pragma unroll
        for (int i = 0; i < 16; ++i) x[i] = fmaf(s[i], SCALE_LOG2E, mc);
    };
    mthr = m; mc = 0.f;
    if (act && !late) {
        if (S == 1 || sig == 0) mask_diag();
        first_max();
    }
    STAMP(6);

    int it = 0;
    unsigned soA = so0 - ((S == 1) ? 4 : 1) * TSTEP, soB = so0 - 3 * TSTEP;
    u32x4 pa, pb;
#define EXP2X(i, j) do { x[i] = __builtin_amdgcn_exp2f(x[i]); x[j] = __builtin_amdgcn_exp2f(x[j]); PIN2(x[i], x[j]); } while (0)
#define FMAX2(i, j) do { x[i] = fmaf(s[i], SCALE_LOG2E, mc); x[j] = fmaf(s[j], SCALE_LOG2E, mc); PIN2(x[i], x[j]); } while (0)
    auto iter = [&](auto PHC) {
        constexpr int PH = decltype(PHC)::value, PN = (PH + 1) % 3;
        constexpr unsigned SK = ((S == 1) ? 1 + 2 * PH : (PH == 0 ? 1 : PH == 1 ? 0 : 2)) * SLOT;
        constexpr unsigned SV = ((S == 1) ? (2 + 2 * PH) % 6 : 2 - PH) * SLOT;
        constexpr unsigned SKN = ((S == 1) ? 1 + 2 * PN : (PN == 0 ? 1 : PN == 1 ? 0 : 2)) * SLOT;
        constexpr unsigned RK = SK - KB, RKN = SKN - KB;
        float ls0, ls1;
        PIN2(kf[0], kf[1]);
        kf[5] = KRD(5, RK);
        s = MFMA(kf[0], q[0], f32x16{}); EXP2X(0, 1); SB();
        kf[6] = KRD(6, RK);
        s = MFMA(kf[1], q[1], s); EXP2X(2, 3); SB();
        PIN2(kf[2], kf[3]);
        kf[7] = KRD(7, RK);
        s = MFMA(kf[2], q[2], s); EXP2X(4, 5); SB();
        kf[8] = KRD(8, RK);
        s = MFMA(kf[3], q[3], s); EXP2X(6, 7); SB();
        PIN2(kf[4], kf[5]);
        kf[9] = KRD(9, RK);
        s = MFMA(kf[4], q[4], s); EXP2X(8, 9); ls0 = x[0] + x[2]; ls1 = x[1] + x[3]; PIN2(ls0, ls1); SB();
        kf[10] = KRD(10, RK); kf[11] = KRD(11, RK);
        s = MFMA(kf[5], q[5], s); EXP2X(10, 11); ls0 += x[4]; ls1 += x[5]; PIN2(ls0, ls1); SB();
        PIN2(kf[6], kf[7]);
        s = MFMA(kf[6], q[6], s); EXP2X(12, 13); pa[0] = cvtpk(x[0], x[1]); pa[1] = cvtpk(x[2], x[3]); PIN2(pa[0], pa[1]); SB();
        s = MFMA(kf[7], q[7], s); EXP2X(14, 15); pa[2] = cvtpk(x[4], x[5]); pa[3] = cvtpk(x[6], x[7]); PIN2(pa[2], pa[3]); SB();
        GATE(mW, SK);
        bf16x8 vf[12];
        vf[0] = VRD(0, SV); SB();
        s = MFMA(kf[8], q[8], s); vf[1] = VRD(1, SV); ls0 += x[6]; ls1 += x[7]; PIN2(ls0, ls1); SB();
        s = MFMA(kf[9], q[9], s); vf[2] = VRD(2, SV); PIECE(S == 2, 0, soA, mW, SK); ls0 += x[8]; ls1 += x[9]; PIN2(ls0, ls1); SB();
        s = MFMA(kf[10], q[10], s); vf[3] = VRD(3, SV); PIECE(S == 2, 1, soA, mW, SK); ls0 += x[10]; ls1 += x[11]; PIN2(ls0, ls1); SB();
        s = MFMA(kf[11], q[11], s); vf[4] = VRD(4, SV); PIECE(S == 2, 2, soA, mW, SK); ls0 += x[12]; ls1 += x[13]; PIN2(ls0, ls1); SB();
        const bf16x8 p0f = __builtin_bit_cast(bf16x8, pa);
        PIN2(vf[0], vf[1]);
        vf[5] = VRD(5, SV);
        o[0] = MFMA(vf[0], p0f, o[0]); if (S == 2) PIECE(true, 3, soA, mW, SK);
        pb[0] = cvtpk(x[8], x[9]); pb[1] = cvtpk(x[10], x[11]); PIN2(pb[0], pb[1]); ls0 += x[14]; ls1 += x[15]; PIN2(ls0, ls1); SB();
        vf[6] = VRD(6, SV);
        o[1] = MFMA(vf[1], p0f, o[1]); if (S == 2) PIECE(true, 4, soA, mW, SK);
        pb[2] = cvtpk(x[12], x[13]); pb[3] = cvtpk(x[14], x[15]); PIN2(pb[2], pb[3]); l += ls0; PIN1(l); SB();
        PIN2(vf[2], vf[3]);
        vf[7] = VRD(7, SV);
        o[2] = MFMA(vf[2], p0f, o[2]); if (S == 2) PIECE(true, 5, soA, mW, SK);
        l += ls1; PIN1(l); SB();
        const bf16x8 p1f = __builtin_bit_cast(bf16x8, pb);
        vf[8] = VRD(8, SV);
        o[3] = MFMA(vf[3], p0f, o[3]); SB();
        float ma = max3f(s[0], s[1], s[2]), mb = max3f(s[8], s[9], s[10]);
        ma = max3f(ma, s[3], s[4]); mb = max3f(mb, s[11], s[12]);
        ma = max3f(ma, s[5], s[6]); mb = max3f(mb, s[13], s[14]); PIN2(ma, mb); SB();
        PIN2(vf[4], vf[5]);
        vf[9] = VRD(9, SV);
        o[4] = MFMA(vf[4], p0f, o[4]);
        ma = max3f(ma, s[7], s[15]);
        float pm = swap_max(max2f(ma, mb)); PIN1(pm); SB();
        vf[10] = VRD(10, SV); vf[11] = VRD(11, SV);
        o[5] = MFMA(vf[5], p0f, o[5]); SB();
        if (__builtin_expect(__any(pm > mthr), 0)) {
            const float mn = max2f(m, pm);
            alpha = __builtin_amdgcn_exp2f((m - mn) * SCALE_LOG2E);
            m = mn; mthr = m + THR_RAW; mc = -m * SCALE_LOG2E; pend = 1;
        }
        PIN2(vf[6], vf[7]);
        o[0] = MFMA(vf[6], p1f, o[0]); FMAX2(0, 1); FMAX2(2, 3); FMAX2(4, 5); SB();
        o[1] = MFMA(vf[7], p1f, o[1]); FMAX2(6, 7); FMAX2(8, 9); FMAX2(10, 11); SB();
        GATE(mW, SV);
        kf[0] = KRD(0, RKN); SB();
        o[2] = MFMA(vf[8], p1f, o[2]); kf[1] = KRD(1, RKN); PIECE(S == 1, 0, soB, mW, SV); FMAX2(12, 13); SB();
        o[3] = MFMA(vf[9], p1f, o[3]); kf[2] = KRD(2, RKN); PIECE(S == 1, 1, soB, mW, SV); FMAX2(14, 15); SB();
        o[4] = MFMA(vf[10], p1f, o[4]); PIECE(S == 1, 2, soB, mW, SV); kf[3] = KRD(3, RKN); if (S == 2) PIECE(false, 3, soB, mW, SV); SB();
        o[5] = MFMA(vf[11], p1f, o[5]); if (S == 2) PIECE(false, 4, soB, mW, SV); kf[4] = KRD(4, RKN); if (S == 2) PIECE(false, 5, soB, mW, SV); SB();
        soA -= TSTEP; soB -= TSTEP;
        if (__builtin_expect(pend, 0)) {
            l *= alpha;
#pragma unroll
            for (int c = 0; c < 6; ++c) o[c] *= alpha;
            pend = 0;
        }
        ++it;
    };
    bool skip0 = false;
    if (S == 1 && late) {
        constexpr unsigned SK = SLOT, SV = 2 * SLOT, RKN = 3 * SLOT - KB;
        kf[5] = KRD(5, 0); kf[6] = KRD(6, 0); kf[7] = KRD(7, 0); kf[8] = KRD(8, 0);
        s = MFMA(kf[0], q[0], f32x16{});
        kf[9] = KRD(9, 0); kf[10] = KRD(10, 0); kf[11] = KRD(11, 0);
#pragma unroll
        for (int i = 1; i < 8; ++i) s = MFMA(kf[i], q[i], s);
        GATE(mW, SK);
        s = MFMA(kf[8], q[8], s); SB();
        PIECE(false, 0, soA, mW, SK); s = MFMA(kf[9], q[9], s); SB();
        PIECE(false, 1, soA, mW, SK); s = MFMA(kf[10], q[10], s); SB();
        PIECE(false, 2, soA, mW, SK); s = MFMA(kf[11], q[11], s); SB();
        GATE(mW, SV);
        kf[0] = KRD(0, RKN); PIECE(true, 0, soB, mW, SV); kf[1] = KRD(1, RKN); PIECE(true, 1, soB, mW, SV); kf[2] = KRD(2, RKN); PIECE(true, 2, soB, mW, SV);
        kf[3] = KRD(3, RKN); kf[4] = KRD(4, RKN);
        mask_diag();
        first_max();
        soA -= TSTEP; soB -= TSTEP;
        it = 1; skip0 = true;
    }
    if (act && it < itend) {
        for (;;) {
            if (!skip0) { iter(IC<0>{}); if (it >= itend) break; }
            skip0 = false;
            iter(IC<1>{}); if (it >= itend) break;
            iter(IC<2>{}); if (it >= itend) break;
        }
    }
    STAMP(7);
    const int ph = it % 3;
    unsigned tk = ((S == 1) ? 1 + 2 * ph : (ph == 0 ? 1 : ph == 1 ? 0 : 2)) * SLOT;
    unsigned tv = ((S == 1) ? (ph == 2 ? 0 : 2 + 2 * ph) : 2 - ph) * SLOT;
    auto adv2 = [&](unsigned a) { const unsigned t = a + 2 * SLOT; return t >= NS * SLOT ? t - NS * SLOT : t; };
    if (act) {
        const unsigned mk = mW + tk, mv = mW + tv;
        GATE(mk, 0);
        const unsigned va = vA + tv, vb = vB + tv;
        bf16x8 vf[12];
#define VRT(i) ldv(((((i) % 6) & 1) ? vb : va) + (((i) % 6) >> 1) * 4096 + ((i) / 6) * 2048)
        vf[0] = VRT(0); PIECE(S == 2, 0, soA, mk, 0); vf[1] = VRT(1); PIECE(S == 2, 1, soA, mk, 0); vf[2] = VRT(2); PIECE(S == 2, 2, soA, mk, 0);
        vf[3] = VRT(3); if (S == 2) PIECE(true, 3, soA, mk, 0); vf[4] = VRT(4); if (S == 2) PIECE(true, 4, soA, mk, 0); vf[5] = VRT(5); if (S == 2) PIECE(true, 5, soA, mk, 0);
        float ls0 = 0.f, ls1 = 0.f;
#pragma unroll
        for (int i = 0; i < 16; i += 2) { x[i] = __builtin_amdgcn_exp2f(x[i]); x[i + 1] = __builtin_amdgcn_exp2f(x[i + 1]); ls0 += x[i]; ls1 += x[i + 1]; }
        l += ls0 + ls1;
#pragma unroll
        for (int i = 0; i < 4; ++i) { pa[i] = cvtpk(x[2 * i], x[2 * i + 1]); pb[i] = cvtpk(x[8 + 2 * i], x[9 + 2 * i]); }
        PIN4(pa[0], pa[1], pa[2], pa[3]); PIN4(pb[0], pb[1], pb[2], pb[3]);
        const bf16x8 p0f = __builtin_bit_cast(bf16x8, pa), p1f = __builtin_bit_cast(bf16x8, pb);
        SB();
        vf[6] = VRT(6); vf[7] = VRT(7);
        o[0] = MFMA(vf[0], p0f, o[0]); SB();
        vf[8] = VRT(8); vf[9] = VRT(9);
        o[1] = MFMA(vf[1], p0f, o[1]); SB();
        vf[10] = VRT(10); vf[11] = VRT(11);
        o[2] = MFMA(vf[2], p0f, o[2]); SB();
        o[3] = MFMA(vf[3], p0f, o[3]); o[4] = MFMA(vf[4], p0f, o[4]); o[5] = MFMA(vf[5], p0f, o[5]);
        o[0] = MFMA(vf[6], p1f, o[0]); o[1] = MFMA(vf[7], p1f, o[1]); SB();
        if (it + 1 < steps) {
            GATE(mv, 0);
            NOP0();
            PIECE(S == 1, 0, soB, mv, 0); NOP0(); PIECE(S == 1, 1, soB, mv, 0); NOP0(); PIECE(S == 1, 2, soB, mv, 0);
            if (S == 2) { NOP0(); PIECE(false, 3, soB, mv, 0); NOP0(); PIECE(false, 4, soB, mv, 0); NOP0(); PIECE(false, 5, soB, mv, 0); }
        }
        o[2] = MFMA(vf[8], p1f, o[2]); o[3] = MFMA(vf[9], p1f, o[3]); o[4] = MFMA(vf[10], p1f, o[4]); o[5] = MFMA(vf[11], p1f, o[5]);
        tk = adv2(tk); tv = adv2(tv); soA -= TSTEP; soB -= TSTEP;
        ++it;
    }
    STAMP(8);
    for (; it < steps; ++it) {
        const unsigned mk = mW + tk, mv = mW + tv;
        GATE(mk, 0);
        NOP0();
        PIECE(S == 2, 0, soA, mk, 0); NOP0(); PIECE(S == 2, 1, soA, mk, 0); NOP0(); PIECE(S == 2, 2, soA, mk, 0);
        if (S == 2) { NOP0(); PIECE(true, 3, soA, mk, 0); NOP0(); PIECE(true, 4, soA, mk, 0); NOP0(); PIECE(true, 5, soA, mk, 0); }
        if (it + 1 < steps) {
            GATE(mv, 0);
            NOP0();
            PIECE(S == 1, 0, soB, mv, 0); NOP0(); PIECE(S == 1, 1, soB, mv, 0); NOP0(); PIECE(S == 1, 2, soB, mv, 0);
            if (S == 2) { NOP0(); PIECE(false, 3, soB, mv, 0); NOP0(); PIECE(false, 4, soB, mv, 0); NOP0(); PIECE(false, 5, soB, mv, 0); }
        }
        tk = adv2(tk); tv = adv2(tv); soA -= TSTEP; soB -= TSTEP;
    }

    STAMP(10);
    l = swap_add(l);
    const __amdgpu_buffer_rsrc_t orsrc = __builtin_amdgcn_make_buffer_rsrc((void*)(op + (size_t)s0 * QSTRIDE), 0, (unsigned)L * QSTRIDE, 0x00020000);
    const unsigned stw = r32 * ORS + 8 * h;
    auto stage_block = [&](unsigned base, int c, const f32x16& z) {
#pragma unroll
        for (int g = 0; g < 4; ++g) {
            u32x2 v = {cvtpk(z[4 * g], z[4 * g + 1]), cvtpk(z[4 * g + 2], z[4 * g + 3])};
            *(LDSP(u32x2))(size_t)(base + (32 * c + 8 * g) * 2) = v;
        }
    };
    unsigned lrd[3], gof[3];
#pragma unroll
    for (int j = 0; j < 3; ++j) { const unsigned idx = 64 * j + lane, row = (idx * 2731) >> 16, ch = idx - 24 * row; lrd[j] = row * ORS + ch * 16; gof[j] = row * QSTRIDE + ch * 16; }
    wait_vm<0>();
    barrier();
    STAMP(11);
    if (S == 1) {
        if (act) {
            const unsigned stg = lds0 + w * (32 * ORS);
            const float inv = __builtin_amdgcn_rcpf(l);
#pragma unroll
            for (int c = 0; c < 6; ++c) { const f32x16 z = o[c] * inv; stage_block(stg + stw, c, z); }
            STAMP(15);
            const unsigned obase = (unsigned)pw * QSTRIDE + (unsigned)(2 * grp + hh) * ROWB;
#pragma unroll
            for (int k = 0; k < 4; ++k)
#pragma unroll
                for (int j = 0; j < 3; ++j) {
                    const u32x4 v = *(LDSP(u32x4))(size_t)(stg + lrd[j] + 8 * k * ORS);
                    __builtin_amdgcn_raw_buffer_store_b128(v, orsrc, gof[j], obase + 8 * k * QSTRIDE, STAUX);
                }
        }
    } else {
        const unsigned mine = lds0 + w * SLOT + lane * 16, other = lds0 + (w ^ 2) * SLOT + lane * 16;
        {
            f32x2 ml = {m, l};
            *(LDSP(f32x2))(size_t)(lds0 + 4 * SLOT + w * 512 + lane * 8) = ml;
        }
        auto send = [&](const f32x16& z, int c) {
#pragma unroll
            for (int g = 0; g < 4; ++g) { f32x4 y = {z[4 * g], z[4 * g + 1], z[4 * g + 2], z[4 * g + 3]}; *(LDSP(f32x4))(size_t)(mine + (c * 4 + g) * 1024) = y; }
        };
        if (sig) { send(o[0], 0); send(o[1], 1); send(o[2], 2); } else { send(o[3], 0); send(o[4], 1); send(o[5], 2); }
        STAMP(12);
        barrier();
        STAMP(13);
        const f32x2 mlo = *(LDSP(f32x2))(size_t)(lds0 + 4 * SLOT + (w ^ 2) * 512 + lane * 8);
        const float mx = max2f(m, mlo[0]);
        const float a0 = __builtin_amdgcn_exp2f((m - mx) * SCALE_LOG2E), a1 = __builtin_amdgcn_exp2f((mlo[0] - mx) * SCALE_LOG2E);
        const float inv = __builtin_amdgcn_rcpf(l * a0 + mlo[1] * a1);
        const float f0 = a0 * inv, f1 = a1 * inv;
        const unsigned stg = lds0 + 51200 + hh * (32 * ORS);
        auto merge = [&](const f32x16& xo, int c, int cd) {
            f32x16 z;
#pragma unroll
            for (int g = 0; g < 4; ++g) {
                const f32x4 y = *(LDSP(f32x4))(size_t)(other + (c * 4 + g) * 1024);
                const f32x4 t = {xo[4 * g], xo[4 * g + 1], xo[4 * g + 2], xo[4 * g + 3]};
                const f32x4 u = t * f0 + y * f1;
                z[4 * g] = u[0]; z[4 * g + 1] = u[1]; z[4 * g + 2] = u[2]; z[4 * g + 3] = u[3];
            }
            stage_block(stg + stw, cd, z);
        };
        if (sig) { merge(o[3], 0, 3); merge(o[4], 1, 4); merge(o[5], 2, 5); } else { merge(o[0], 0, 0); merge(o[1], 1, 1); merge(o[2], 2, 2); }
        STAMP(14);
        barrier();
        STAMP(15);
        const unsigned obase = (unsigned)(pw + 16 * sig) * QSTRIDE + (unsigned)(2 * grp + hh) * ROWB;
        const unsigned stgr = stg + 16 * sig * ORS;
#pragma unroll
        for (int k = 0; k < 2; ++k)
#pragma unroll
            for (int j = 0; j < 3; ++j) {
                const u32x4 v = *(LDSP(u32x4))(size_t)(stgr + lrd[j] + 8 * k * ORS);
                __builtin_amdgcn_raw_buffer_store_b128(v, orsrc, gof[j], obase + 8 * k * QSTRIDE, STAUX);
            }
    }
    STAMP(16);
}

template <int CTRL> DI int dpp_mov(int old, int x) { return __builtin_amdgcn_update_dpp(old, x, CTRL, 0xf, 0xf, false); }
DI int wave_incl_scan(int x) {
    x += dpp_mov<0x111>(0, x);
    x += dpp_mov<0x112>(0, x);
    x += dpp_mov<0x114>(0, x);
    x += dpp_mov<0x118>(0, x);
    x += __builtin_amdgcn_update_dpp(0, x, 0x142, 0xa, 0xf, false);
    x += __builtin_amdgcn_update_dpp(0, x, 0x143, 0xc, 0xf, false);
    return x;
}
DI int wave_max_nonneg(int x) {
    x = max(x, dpp_mov<0x111>(0, x));
    x = max(x, dpp_mov<0x112>(0, x));
    x = max(x, dpp_mov<0x114>(0, x));
    x = max(x, dpp_mov<0x118>(0, x));
    x = max(x, __builtin_amdgcn_update_dpp(0, x, 0x142, 0xa, 0xf, false));
    x = max(x, __builtin_amdgcn_update_dpp(0, x, 0x143, 0xc, 0xf, false));
    return __builtin_amdgcn_readlane(x, 63);
}

DI bool decode_slow(const int* __restrict__ cu, int nseq, int k, int& s0, int& L, int& p0, int& isx) {
    int nmax = 0;
    for (int s = 0; s < nseq; ++s) nmax = max(nmax, (cu[s + 1] - cu[s] + 31) >> 5);
    const int B = BSPLIT;
    int N = 0;
    for (int s = 0; s < nseq; ++s) { const int n = (cu[s + 1] - cu[s] + 31) >> 5; N += max(n - B, 0) + min(B >> 1, (n + 1) >> 1); }
    int rank = k;
    if (k < CUS_PER_XCD) { const int N2 = min(max(N - CUS_PER_XCD, 0), (PFMODE != 0) ? PF0 - CUS_PER_XCD : CUS_PER_XCD); rank = (k < N2) ? CUS_PER_XCD - 1 - k : k - N2; }
    if (rank >= N) return false;
    int jx = nmax, my = min(B >> 1, (nmax + 1) >> 1);
    for (;;) {
        const int x = (jx > B) && (((jx + 1) >> 1) >= 2 * my);
        const int thr = x ? 32 * (jx - 1) : 64 * (my - 1);
        for (int s = 0; s < nseq; ++s) {
            const int len = cu[s + 1] - cu[s];
            if (len > thr) { if (rank == 0) { s0 = cu[s]; L = len; p0 = thr; isx = x; return true; } --rank; }
        }
        if (x) --jx; else --my;
    }
}

__global__ __launch_bounds__(256, 2) void attn_varlen_kernel(const char* __restrict__ qp, const char* __restrict__ kp, const char* __restrict__ vp,
                                                             const int* __restrict__ cu, char* __restrict__ op, int nseq, int T
                                                             ) {
    __shared__ __attribute__((aligned(16))) char smem_[LDS_BYTES];
    const int grp = blockIdx.x & 7;
    const int kraw = blockIdx.x >> 3;
    const int lane = threadIdx.x & 63;

    asm volatile("" :: "s"(qp), "s"(kp), "s"(vp), "s"(op), "s"(cu), "s"(nseq));
    if (CODEPF > 0 && kraw < CODEWG) {
        unsigned long long pc; asm volatile("s_getpc_b64 %0" : "=s"(pc));
        const char* cb = (const char*)(pc & ~127ull);
        const unsigned mscr0 = rfl((unsigned)(size_t)(lds_char_ptr)smem_ + 73728 + 1024 + (threadIdx.x >> 6) * 256);
        asm volatile("s_mov_b32 m0, %0" :: "s"(mscr0) : "memory");
        for (unsigned o = threadIdx.x * 128; o < CODEPF; o += 256 * 128)
            asm volatile("global_load_lds_dword %0, off" :: "v"(cb + o) : "memory");
    }
    if (PFMODE != 0 && kraw >= PF0 && kraw < PF0 + 8) {
        const int wv = rfl(threadIdx.x >> 6);
        const unsigned mscr = rfl((unsigned)(size_t)(lds_char_ptr)smem_ + wv * 256);
        asm volatile("s_mov_b32 m0, %0" :: "s"(mscr) : "memory");
        const char* base = (wv >> 1) ? vp : kp;
        for (int s = kraw - PF0; s < nseq; s += 8) {
            const int a = rfl(cu[s]), b = rfl(cu[s + 1]);
            if (b <= a) continue;
            const u32x4 r = make_srd(base + (size_t)a * KSTRIDE + grp * ROWB, (unsigned)(b - a - 1) * KSTRIDE + ROWB);
            const unsigned nl = 3u * (b - a);
            for (unsigned j0 = 0; j0 < nl; j0 += 128) {
                const unsigned j3 = j0 + (threadIdx.x & 127), t3 = j3 / 3;
                const unsigned off = t3 * KSTRIDE + (j3 - 3 * t3) * 128;
                asm volatile("buffer_load_dword %0, %1, 0 offen " PFPOL " lds" :: "v"(off), "s"(r) : "memory");
            }
        }
        return;
    }
    const int k = (PFMODE != 0 && kraw >= PF0 + 8) ? kraw - 8 : kraw;
    typedef unsigned u32x16 __attribute__((ext_vector_type(16)));
    u32x16 cus;
    {
        const u32x4 cr = make_srd(cu, (unsigned)(nseq + 1) * 4);
        asm volatile("s_buffer_load_dwordx16 %0, %1, 0" : "=s"(cus) : "s"(cr) : "memory");
    }
    const int l3 = (lane * 43) >> 7, r3 = lane - 3 * l3, st = l3 + 1;
    const int jx = 2 * st - 1 + r3;
    const bool okx = (r3 < 2) & (jx > BSPLIT) & (lane < 63), oky = (r3 == 2) & !(st & 1) & (st <= BSPLIT);
    int thr = oky ? 32 * (st - 2) : 0x7fffffff;
    thr = okx ? 32 * (jx - 1) : thr;
    asm volatile("s_waitcnt lgkmcnt(0)" : "+s"(cus) :: "memory");
    int len = 0, c0 = 0, cnt = 0;
#define SEQ(s) do { const int ls = (int)cus[(s) + 1] - (int)cus[s];                         \
        cnt += (ls > thr) ? 1 : 0;                                                                                                      \
        asm("v_writelane_b32 %0, %1, %2" : "+v"(len) : "s"(ls), "i"(s)); asm("v_writelane_b32 %0, %1, %2" : "+v"(c0) : "s"(cus[s]), "i"(s)); } while (0)
    SEQ(0); SEQ(1); SEQ(2); SEQ(3); SEQ(4); SEQ(5); SEQ(6); SEQ(7);
    if (__builtin_expect(nseq > 8, 0)) { SEQ(8); SEQ(9); SEQ(10); SEQ(11); SEQ(12); SEQ(13); SEQ(14); }
    const int incl = wave_incl_scan(cnt);
    const int N = __builtin_amdgcn_readlane(incl, 63);
    const int N2 = min(max(N - CUS_PER_XCD, 0), (PFMODE != 0) ? PF0 - CUS_PER_XCD : CUS_PER_XCD);
    (void)N2;
    const int rank = (k >= CUS_PER_XCD && N <= 2 * CUS_PER_XCD) ? (k < N ? N - 1 - (k - CUS_PER_XCD) : N) : k;
    bool valid = rank < N;
    const int a = N - rank;
    const int lv = (__ffsll((long long)__ballot(incl >= a)) - 1) & 63;
    const int idx = __builtin_amdgcn_readlane(incl, lv) - a;
    int p0 = __builtin_amdgcn_readlane(thr, lv);
    int isx = __builtin_amdgcn_readlane(r3, lv) < 2;
    const bool has = len > p0;
    const unsigned long long mk = __ballot(has);
    const int below = __builtin_amdgcn_mbcnt_hi((unsigned)(mk >> 32), __builtin_amdgcn_mbcnt_lo((unsigned)mk, 0));
    const int sq = (__ffsll((long long)__ballot(has && below == idx)) - 1) & 63;
    int s0 = __builtin_amdgcn_readlane(c0, sq), L = __builtin_amdgcn_readlane(len, sq);
    if (__builtin_expect(nseq > 15 || __ballot(len > 32 * 42) != 0, 0)) {
        int a0 = 0, a1 = 0, a2 = 0, a3 = 0;
        const int v = decode_slow(cu, nseq, k, a0, a1, a2, a3);
        valid = rfl(v) != 0; s0 = rfl(a0); L = rfl(a1); p0 = rfl(a2); isx = rfl(a3);
    }
    if (!valid) return;
    s0 = rfl(s0); L = rfl(L); p0 = rfl(p0); isx = rfl(isx);
    if (PRIO && k < CUS_PER_XCD) __builtin_amdgcn_s_setprio(PRIO);
    const unsigned lds0 = (unsigned)(size_t)(lds_char_ptr)smem_;
    if (isx) {
        const int n = (min(p0 + 32, L) + 31) >> 5;
        attn_item<2>(qp, kp, vp, op, grp, s0, L, p0, n, lds0 STPASS);
    } else {
        const int n = (min(p0 + 64, L) + 31) >> 5;
        attn_item<1>(qp, kp, vp, op, grp, s0, L, p0, n, lds0 STPASS);
    }
}

extern "C" void kernel_launch(void* const* d_in, const int* in_sizes, int n_in, void* d_out, int out_size, void* d_ws, size_t ws_size,
                              hipStream_t stream) {
    (void)n_in; (void)out_size; (void)d_ws; (void)ws_size;
    const int T = in_sizes[0] / (HQ * D);
    const int nseq = in_sizes[3] - 1;
    const int per_head = T / 32 + nseq + 1 + 8;
    attn_varlen_kernel<<<dim3(8 * per_head), dim3(256), 0, stream>>>((const char*)d_in[0], (const char*)d_in[1], (const char*)d_in[2],
                                                                    (const int*)d_in[3], (char*)d_out, nseq, T
                                                                    );
}
```

```cpp
#include <hip/hip_runtime.h>
#include <hip/hip_bf16.h>
#include <stdint.h>

typedef __bf16 bf16x8 __attribute__((ext_vector_type(8)));
typedef __bf16 bf16x2 __attribute__((ext_vector_type(2)));
typedef short s16x4 __attribute__((ext_vector_type(4)));
typedef short s16x8 __attribute__((ext_vector_type(8)));
typedef float f32x2 __attribute__((ext_vector_type(2)));
typedef float f32x4 __attribute__((ext_vector_type(4)));
typedef float f32x16 __attribute__((ext_vector_type(16)));
typedef unsigned u32x2 __attribute__((ext_vector_type(2)));
typedef unsigned u32x4 __attribute__((ext_vector_type(4)));
#define LDSP(T) __attribute__((address_space(3))) T*
typedef LDSP(char) lds_char_ptr;

#define DI __device__ __forceinline__
#define LDPOL ""
#define FTPOL ""
#define PSTRIDE 65536
#define PAIRMODE 1
#define PRIO 2
#define QPOL ""
#define STAUX 16
#define PFPOL ""
#define PFMODE 3
#define CODEPF 0
#define CODEWG 2
#define STAMP(i) do {} while (0)
#define STARG
#define STPASS

constexpr int HQ = 16, HKV = 8, D = 192;
constexpr unsigned QSTRIDE = HQ * D * 2;
constexpr unsigned KSTRIDE = HKV * D * 2;
constexpr unsigned ROWB = D * 2;
constexpr unsigned SLOT = 32 * ROWB;
constexpr unsigned ORS = 400;
constexpr unsigned LDS_BYTES = 77824;
constexpr float SCALE_LOG2E = 0.07216878364870322f * 1.4426950408889634f;
constexpr float THR_RAW = 8.0f / SCALE_LOG2E;
constexpr int CUS_PER_XCD = 32;
constexpr int PF0 = 56;
constexpr int BSPLIT = 8;

DI int rfl(int x) { return __builtin_amdgcn_readfirstlane(x); }
DI unsigned cvtpk(float lo, float hi) {
    f32x2 v = {lo, hi}; bf16x2 b = __builtin_convertvector(v, bf16x2); return __builtin_bit_cast(unsigned, b);
}
DI float max2f(float a, float b) { return __builtin_elementwise_maximum(a, b); }
DI float max3f(float a, float b, float c) { return max2f(max2f(a, b), c); }
DI float swap_max(float x) {
    auto r = __builtin_amdgcn_permlane32_swap(__float_as_uint(x), __float_as_uint(x), false, false);
    return max2f(__uint_as_float(r[0]), __uint_as_float(r[1]));
}
DI float swap_add(float x) {
    auto r = __builtin_amdgcn_permlane32_swap(__float_as_uint(x), __float_as_uint(x), false, false);
    return __uint_as_float(r[0]) + __uint_as_float(r[1]);
}
DI u32x4 make_srd(const void* base, unsigned nbytes) {
    unsigned long long a = (unsigned long long)base;
    u32x4 s; s[0] = rfl((unsigned)a); s[1] = rfl((unsigned)(a >> 32) & 0xffffu); s[2] = rfl(nbytes); s[3] = 0x00020000u;
    return s;
}
template <int IMM> DI void dma16(unsigned m0v, unsigned voff, u32x4 srd, unsigned soff) {
    asm volatile("s_mov_b32 m0, %0\n\ts_nop 0\n\tbuffer_load_dwordx4 %1, %2, %3 offen offset:%4 lds"
                 :: "s"(m0v), "v"(voff), "s"(srd), "s"(soff), "i"(IMM) : "memory");
}
template <int IMM, int NEXT> DI void dma16c_nt(unsigned voff, u32x4 srd, unsigned soff, unsigned nbase) {
    asm volatile("buffer_load_dwordx4 %0, %1, %2 offen offset:%3 " FTPOL " lds\n\ts_add_u32 m0, %4, %5"
                 :: "v"(voff), "s"(srd), "s"(soff), "i"(IMM), "s"(nbase), "i"(NEXT) : "memory", "scc");
}
template <int IMM> DI void dma16e_nt(unsigned voff, u32x4 srd, unsigned soff) {
    asm volatile("buffer_load_dwordx4 %0, %1, %2 offen offset:%3 " FTPOL " lds" :: "v"(voff), "s"(srd), "s"(soff), "i"(IMM) : "memory");
}
template <int IMM, int NEXT> DI void dma16c(unsigned voff, u32x4 srd, unsigned soff, unsigned nbase) {
    asm volatile("buffer_load_dwordx4 %0, %1, %2 offen offset:%3 " LDPOL " lds\n\ts_add_u32 m0, %4, %5"
                 :: "v"(voff), "s"(srd), "s"(soff), "i"(IMM), "s"(nbase), "i"(NEXT) : "memory", "scc");
}
template <int IMM, int NEXT> DI void dmaqc(unsigned voff, u32x4 srd, unsigned soff, unsigned nbase) {
    asm volatile("buffer_load_dwordx4 %0, %1, %2 offen offset:%3 " QPOL " lds\n\ts_add_u32 m0, %4, %5"
                 :: "v"(voff), "s"(srd), "s"(soff), "i"(IMM), "s"(nbase), "i"(NEXT) : "memory", "scc");
}
template <int IMM> DI void dmaqe(unsigned voff, u32x4 srd, unsigned soff) {
    asm volatile("buffer_load_dwordx4 %0, %1, %2 offen offset:%3 " QPOL " lds" :: "v"(voff), "s"(srd), "s"(soff), "i"(IMM) : "memory");
}
template <int IMM> DI void dma16e(unsigned voff, u32x4 srd, unsigned soff) {
    asm volatile("buffer_load_dwordx4 %0, %1, %2 offen offset:%3 " LDPOL " lds" :: "v"(voff), "s"(srd), "s"(soff), "i"(IMM) : "memory");
}
template <int N> DI void wait_vm() { asm volatile("s_waitcnt vmcnt(%0)" :: "i"(N) : "memory"); }
DI void barrier() { asm volatile("s_waitcnt lgkmcnt(0)\n\ts_barrier" ::: "memory"); }
#define SB() __builtin_amdgcn_sched_barrier(0)
#define MFMA(a, b, c) __builtin_amdgcn_mfma_f32_32x32x16_bf16((a), (b), (c), 0, 0, 0)

DI bf16x8 ldk(unsigned addr) { return *(LDSP(bf16x8))(size_t)addr; }
DI bf16x8 ldv(unsigned addr) {
    const s16x4 lo = __builtin_amdgcn_ds_read_tr16_b64_v4i16((LDSP(s16x4))(size_t)addr);
    const s16x4 hi = __builtin_amdgcn_ds_read_tr16_b64_v4i16((LDSP(s16x4))(size_t)(addr + 1024));
    return __builtin_bit_cast(bf16x8, __builtin_shufflevector(lo, hi, 0, 1, 2, 3, 4, 5, 6, 7));
}

template <int V> struct IC { static constexpr int value = V; };

template <int S>
DI void attn_item(const char* __restrict__ qp, const char* __restrict__ kp, const char* __restrict__ vp, char* __restrict__ op,
                  int grp, int s0, int L, int p0, int n, unsigned lds0 STARG) {
    constexpr int NS = (S == 1) ? 6 : 3;
    constexpr int PW = (S == 1) ? 3 : 6;
    constexpr unsigned TSTEP = 32 * S * KSTRIDE;
    const int tid = threadIdx.x, lane = tid & 63;
    const int w = rfl(tid >> 6);
    const int hh = w & 1, up = w >> 1;
    const int r32 = lane & 31, h = lane >> 5;
    const int sig = (S == 1) ? 0 : up;
    const int pw = (S == 1) ? p0 + 32 * up : p0;
    const int steps = (S == 1) ? n : (n + 1) >> 1;
    const int tdiag = pw >> 5;

    const int kend = min(L, 32 * n);
    const u32x4 ksrd = make_srd(kp + (size_t)s0 * KSTRIDE + grp * ROWB, (unsigned)(kend - 1) * KSTRIDE + ROWB);
    const u32x4 vsrd = make_srd(vp + (size_t)s0 * KSTRIDE + grp * ROWB, (unsigned)(kend - 1) * KSTRIDE + ROWB);
    const u32x4 qsrd = make_srd(qp + (size_t)s0 * QSTRIDE, (unsigned)L * QSTRIDE);

    const unsigned ring = lds0 + sig * NS * SLOT;
    const unsigned mW = ring + ((S == 1) ? w * 1024 : hh * 1024);
    const unsigned drow = (unsigned)(8 * hh + (lane >> 3) + ((S == 1) ? 16 * up : 0)) * KSTRIDE;
    const unsigned voffK = drow + 16u * ((lane & 7) ^ ((hh << 2) | (lane >> 4)));
    const unsigned voffV = drow + 16u * ((lane & 7) ^ (((lane >> 4) & 1) << 2));
    const unsigned voffK2 = voffK + 16 * KSTRIDE, voffV2 = voffV + 16 * KSTRIDE;
#define PIECE(ISV, j, so, mb, MO) do { const u32x4 sr_ = (ISV) ? vsrd : ksrd;                                                            \
        const unsigned v1_ = (ISV) ? voffV : voffK, v2_ = (ISV) ? voffV2 : voffK2;                                                       \
        if (j == 0) dma16c<0, (MO) + 4096 - 128>(v1_, sr_, so, mb);                                                                      \
        else if (j == 1) dma16c<128, (MO) + 8192 - 256>(v1_, sr_, so, mb);                                                               \
        else if (j == 2) { if (S == 1) dma16e<256>(v1_, sr_, so); else dma16c<256, (MO) + 2048>(v1_, sr_, so, mb); }                      \
        else if (j == 3) dma16c<0, (MO) + 2048 + 4096 - 128>(v2_, sr_, so, mb);                                                          \
        else if (j == 4) dma16c<128, (MO) + 2048 + 8192 - 256>(v2_, sr_, so, mb);                                                        \
        else dma16e<256>(v2_, sr_, so); } while (0)
#define PIECEN(ISV, j, so, mb, MO) do { const u32x4 sr_ = (ISV) ? vsrd : ksrd;                                                           \
        const unsigned v1_ = (ISV) ? voffV : voffK, v2_ = (ISV) ? voffV2 : voffK2;                                                       \
        if (j == 0) dma16c_nt<0, (MO) + 4096 - 128>(v1_, sr_, so, mb);                                                                   \
        else if (j == 1) dma16c_nt<128, (MO) + 8192 - 256>(v1_, sr_, so, mb);                                                            \
        else if (j == 2) { if (S == 1) dma16e_nt<256>(v1_, sr_, so); else dma16c_nt<256, (MO) + 2048>(v1_, sr_, so, mb); }                \
        else if (j == 3) dma16c_nt<0, (MO) + 2048 + 4096 - 128>(v2_, sr_, so, mb);                                                       \
        else if (j == 4) dma16c_nt<128, (MO) + 2048 + 8192 - 256>(v2_, sr_, so, mb);                                                     \
        else dma16e_nt<256>(v2_, sr_, so); } while (0)
#define ELEMN(ISV, so, mb, MO) do { SETM0(mb, MO); PIECEN(ISV, 0, so, mb, MO); NOP0(); PIECEN(ISV, 1, so, mb, MO); NOP0(); PIECEN(ISV, 2, so, mb, MO);  \
        if (S == 2) { NOP0(); PIECEN(ISV, 3, so, mb, MO); NOP0(); PIECEN(ISV, 4, so, mb, MO); NOP0(); PIECEN(ISV, 5, so, mb, MO); } } while (0)
#define SETM0(mb, MO) asm volatile("s_add_u32 m0, %0, %1\n\ts_nop 0" :: "s"(mb), "i"(MO) : "memory", "scc")
#define ELEM(ISV, so, mb, MO) do { SETM0(mb, MO); PIECE(ISV, 0, so, mb, MO); NOP0(); PIECE(ISV, 1, so, mb, MO); NOP0(); PIECE(ISV, 2, so, mb, MO);      \
        if (S == 2) { NOP0(); PIECE(ISV, 3, so, mb, MO); NOP0(); PIECE(ISV, 4, so, mb, MO); NOP0(); PIECE(ISV, 5, so, mb, MO); } } while (0)
#define NOP0() asm volatile("s_nop 0" ::: "memory")
    const unsigned so0 = (unsigned)(32 * (n - 1 - sig)) * KSTRIDE;

    STAMP(2);
    ELEMN(false, so0, mW, 0);
    const unsigned qslot = (S == 1) ? lds0 + (2 + w) * SLOT : lds0 + (3 * hh + 2) * SLOT;
    {
        const unsigned qrow = (unsigned)(2 * grp + hh) * ROWB + (unsigned)pw * QSTRIDE;
        const unsigned l8 = (unsigned)(lane >> 3) * QSTRIDE;
        if (S == 1) {
            const unsigned vq0 = l8 + 16u * ((lane & 7) ^ (lane >> 4)), vq1 = l8 + 8 * QSTRIDE + 16u * ((lane & 7) ^ (4 | (lane >> 4)));
            const unsigned so2 = qrow + 16 * QSTRIDE;
            SETM0(qslot, 0);
            dmaqc<0, 4096 - 128>(vq0, qsrd, qrow, qslot); NOP0(); dmaqc<128, 8192 - 256>(vq0, qsrd, qrow, qslot); NOP0(); dmaqc<256, 1024>(vq0, qsrd, qrow, qslot); NOP0();
            dmaqc<0, 1024 + 4096 - 128>(vq1, qsrd, qrow, qslot); NOP0(); dmaqc<128, 1024 + 8192 - 256>(vq1, qsrd, qrow, qslot); NOP0(); dmaqc<256, 2048>(vq1, qsrd, qrow, qslot); NOP0();
            dmaqc<0, 2048 + 4096 - 128>(vq0, qsrd, so2, qslot); NOP0(); dmaqc<128, 2048 + 8192 - 256>(vq0, qsrd, so2, qslot); NOP0(); dmaqc<256, 3072>(vq0, qsrd, so2, qslot); NOP0();
            dmaqc<0, 3072 + 4096 - 128>(vq1, qsrd, so2, qslot); NOP0(); dmaqc<128, 3072 + 8192 - 256>(vq1, qsrd, so2, qslot); NOP0(); dmaqe<256>(vq1, qsrd, so2);
        } else {
            const unsigned vq = l8 + (unsigned)(8 * up) * QSTRIDE + 16u * ((lane & 7) ^ ((up << 2) | (lane >> 4))), vq2 = vq + 16 * QSTRIDE;
            const unsigned qm = qslot + up * 1024;
            SETM0(qm, 0);
            dmaqc<0, 4096 - 128>(vq, qsrd, qrow, qm); NOP0(); dmaqc<128, 8192 - 256>(vq, qsrd, qrow, qm); NOP0(); dmaqc<256, 2048>(vq, qsrd, qrow, qm); NOP0();
            dmaqc<0, 2048 + 4096 - 128>(vq2, qsrd, qrow, qm); NOP0(); dmaqc<128, 2048 + 8192 - 256>(vq2, qsrd, qrow, qm); NOP0(); dmaqe<256>(vq2, qsrd, qrow);
        }
    }
    if (S == 1) ELEMN(false, so0 - TSTEP, mW, SLOT); else ELEM(false, so0 - TSTEP, mW, SLOT);

    const bool act = (S == 2) || pw < L;
    const bool late = (S == 1) && act && tdiag != n - 1;
    const int itend = (S == 1) ? n - 1 : ((n - sig + 1) >> 1) - 1;
    const unsigned kb0 = r32 * 128 + 16 * (h ^ ((r32 >> 1) & 7));
    constexpr unsigned KB = (S == 1) ? SLOT : 0;
    unsigned kA = ring + KB + kb0, kBb = kA ^ 32, kC = kA ^ 64, kD = kA ^ 96;
    const int q4 = (lane & 15) >> 2, p4 = lane & 3, blk = (lane >> 4) & 1;
    unsigned vA = ring + (4 * h + q4) * 128 + 64 * (q4 >> 1) + 32 * blk + 8 * p4, vB = vA ^ 64;
    f32x16 o[6];
#pragma unroll
    for (int c = 0; c < 6; ++c) { o[c] = f32x16{}; asm volatile("" : "+v"(o[c])); }
    float m = -1e30f, l = 0.f, alpha = 1.f, mthr, mc;
    int pend = 0;
    int dq0 = pw + r32 - 4 * h;
    asm volatile("" : "+v"(kA), "+v"(kBb), "+v"(kC), "+v"(kD), "+v"(vA), "+v"(vB), "+v"(dq0));
    f32x16 s;
    float x[16];
#define KRD(i, OFF) ldk((((i) & 3) == 0 ? kA : ((i) & 3) == 1 ? kBb : ((i) & 3) == 2 ? kC : kD) + (OFF) + ((i) >> 2) * 4096)
#define VRD(i, OFF) ldv(((((i) % 6) & 1) ? vB : vA) + (OFF) + (((i) % 6) >> 1) * 4096 + ((i) / 6) * 2048)

    wait_vm<PW>();
    STAMP(4);
    barrier();
    bf16x8 q[12];
    {
        const unsigned a0 = qslot + kb0;
#pragma unroll
        for (int i = 0; i < 12; ++i) q[i] = ldk((a0 ^ (32 * (i & 3))) + (i >> 2) * 4096);
    }
    bf16x8 kf[12];
    {
        const unsigned a0 = ring + kb0;
#pragma unroll
        for (int i = 0; i < 6; ++i) kf[i] = ldk((a0 ^ (32 * (i & 3))) + (i >> 2) * 4096);
    }
    barrier();
    ELEMN(true, so0, mW, 2 * SLOT);
    if (S == 1) {
        ELEM(false, so0 - 2 * TSTEP, mW, 3 * SLOT);
        ELEMN(true, so0 - TSTEP, mW, 4 * SLOT);
        ELEM(false, so0 - 3 * TSTEP, mW, 5 * SLOT);
    }
    STAMP(5);

#define GATE(mb, MO) do { __builtin_amdgcn_s_waitcnt(((NS - 2) * PW) | 0x70);                                                            \
        asm volatile("s_barrier\n\ts_add_u32 m0, %0, %1" :: "s"(mb), "i"(MO) : "memory", "scc"); } while (0)
#define PIN1(a) asm volatile("" : "+v"(a))
#define PIN2(a, b) asm volatile("" : "+v"(a), "+v"(b))
#define PIN4(a, b, c, d) asm volatile("" : "+v"(a), "+v"(b), "+v"(c), "+v"(d))
    auto mask_diag = [&]() {
        int dq = dq0 - 32 * tdiag;
        asm volatile("" : "+v"(dq));
#pragma unroll
        for (int i = 0; i < 16; ++i) { const int c = (i & 3) + 8 * (i >> 2); s[i] = (c <= dq) ? s[i] : -__builtin_inff(); }
    };

    if (act && !late) {
        const unsigned a0 = ring + kb0;
#pragma unroll
        for (int i = 6; i < 12; ++i) kf[i] = ldk((a0 ^ (32 * (i & 3))) + (i >> 2) * 4096);
        s = MFMA(kf[0], q[0], f32x16{});
#pragma unroll
        for (int i = 1; i < 12; ++i) s = MFMA(kf[i], q[i], s);
    }
    GATE(mW, 0);
    constexpr unsigned KS0 = (S == 1) ? 0 : SLOT;
    kf[0] = KRD(0, KS0);
    PIECE(S == 1, 0, so0 - 2 * TSTEP, mW, 0); kf[1] = KRD(1, KS0);
    PIECE(S == 1, 1, so0 - 2 * TSTEP, mW, 0); kf[2] = KRD(2, KS0);
    PIECE(S == 1, 2, so0 - 2 * TSTEP, mW, 0); kf[3] = KRD(3, KS0);
    if (S == 2) PIECE(false, 3, so0 - 2 * TSTEP, mW, 0);
    kf[4] = KRD(4, KS0);
    if (S == 2) { PIECE(false, 4, so0 - 2 * TSTEP, mW, 0); NOP0(); PIECE(false, 5, so0 - 2 * TSTEP, mW, 0); }
    auto first_max = [&]() {
        float a = max3f(s[0], s[1], s[2]), b = max3f(s[8], s[9], s[10]);
        a = max3f(a, s[3], s[4]); b = max3f(b, s[11], s[12]);
        a = max3f(a, s[5], s[6]); b = max3f(b, s[13], s[14]);
        a = max3f(a, s[7], s[15]);
        m = max2f(m, swap_max(max2f(a, b)));
        mthr = m + THR_RAW; mc = -m * SCALE_LOG2E;
#pragma unroll
        for (int i = 0; i < 16; ++i) x[i] = fmaf(s[i], SCALE_LOG2E, mc);
    };
    mthr = m; mc = 0.f;
    if (act && !late) {
        if (S == 1 || sig == 0) mask_diag();
        first_max();
    }
    STAMP(6);

    int it = 0;
    unsigned soA = so0 - ((S == 1) ? 4 : 1) * TSTEP, soB = so0 - 3 * TSTEP;
    u32x4 pa, pb;
#define EXP2X(i, j) do { x[i] = __builtin_amdgcn_exp2f(x[i]); x[j] = __builtin_amdgcn_exp2f(x[j]); PIN2(x[i], x[j]); } while (0)
#define FMAX2(i, j) do { x[i] = fmaf(s[i], SCALE_LOG2E, mc); x[j] = fmaf(s[j], SCALE_LOG2E, mc); PIN2(x[i], x[j]); } while (0)
    auto iter = [&](auto PHC) {
        constexpr int PH = decltype(PHC)::value, PN = (PH + 1) % 3;
        constexpr unsigned SK = ((S == 1) ? 1 + 2 * PH : (PH == 0 ? 1 : PH == 1 ? 0 : 2)) * SLOT;
        constexpr unsigned SV = ((S == 1) ? (2 + 2 * PH) % 6 : 2 - PH) * SLOT;
        constexpr unsigned SKN = ((S == 1) ? 1 + 2 * PN : (PN == 0 ? 1 : PN == 1 ? 0 : 2)) * SLOT;
        constexpr unsigned RK = SK - KB, RKN = SKN - KB;
        float ls0, ls1;
        PIN2(kf[0], kf[1]);
        kf[5] = KRD(5, RK);
        s = MFMA(kf[0], q[0], f32x16{}); EXP2X(0, 1); SB();
        kf[6] = KRD(6, RK);
        s = MFMA(kf[1], q[1], s); EXP2X(2, 3); SB();
        PIN2(kf[2], kf[3]);
        kf[7] = KRD(7, RK);
        s = MFMA(kf[2], q[2], s); EXP2X(4, 5); SB();
        kf[8] = KRD(8, RK);
        s = MFMA(kf[3], q[3], s); EXP2X(6, 7); SB();
        PIN2(kf[4], kf[5]);
        kf[9] = KRD(9, RK);
        s = MFMA(kf[4], q[4], s); EXP2X(8, 9); ls0 = x[0] + x[2]; ls1 = x[1] + x[3]; PIN2(ls0, ls1); SB();
        kf[10] = KRD(10, RK); kf[11] = KRD(11, RK);
        s = MFMA(kf[5], q[5], s); EXP2X(10, 11); ls0 += x[4]; ls1 += x[5]; PIN2(ls0, ls1); SB();
        PIN2(kf[6], kf[7]);
        s = MFMA(kf[6], q[6], s); EXP2X(12, 13); pa[0] = cvtpk(x[0], x[1]); pa[1] = cvtpk(x[2], x[3]); PIN2(pa[0], pa[1]); SB();
        s = MFMA(kf[7], q[7], s); EXP2X(14, 15); pa[2] = cvtpk(x[4], x[5]); pa[3] = cvtpk(x[6], x[7]); PIN2(pa[2], pa[3]); SB();
        GATE(mW, SK);
        bf16x8 vf[12];
        vf[0] = VRD(0, SV); SB();
        s = MFMA(kf[8], q[8], s); vf[1] = VRD(1, SV); ls0 += x[6]; ls1 += x[7]; PIN2(ls0, ls1); SB();
        s = MFMA(kf[9], q[9], s); vf[2] = VRD(2, SV); PIECE(S == 2, 0, soA, mW, SK); ls0 += x[8]; ls1 += x[9]; PIN2(ls0, ls1); SB();
        s = MFMA(kf[10], q[10], s); vf[3] = VRD(3, SV); PIECE(S == 2, 1, soA, mW, SK); ls0 += x[10]; ls1 += x[11]; PIN2(ls0, ls1); SB();
        s = MFMA(kf[11], q[11], s); vf[4] = VRD(4, SV); PIECE(S == 2, 2, soA, mW, SK); ls0 += x[12]; ls1 += x[13]; PIN2(ls0, ls1); SB();
        const bf16x8 p0f = __builtin_bit_cast(bf16x8, pa);
        PIN2(vf[0], vf[1]);
        vf[5] = VRD(5, SV);
        o[0] = MFMA(vf[0], p0f, o[0]); if (S == 2) PIECE(true, 3, soA, mW, SK);
        pb[0] = cvtpk(x[8], x[9]); pb[1] = cvtpk(x[10], x[11]); PIN2(pb[0], pb[1]); ls0 += x[14]; ls1 += x[15]; PIN2(ls0, ls1); SB();
        vf[6] = VRD(6, SV);
        o[1] = MFMA(vf[1], p0f, o[1]); if (S == 2) PIECE(true, 4, soA, mW, SK);
        pb[2] = cvtpk(x[12], x[13]); pb[3] = cvtpk(x[14], x[15]); PIN2(pb[2], pb[3]); l += ls0; PIN1(l); SB();
        PIN2(vf[2], vf[3]);
        vf[7] = VRD(7, SV);
        o[2] = MFMA(vf[2], p0f, o[2]); if (S == 2) PIECE(true, 5, soA, mW, SK);
        l += ls1; PIN1(l); SB();
        const bf16x8 p1f = __builtin_bit_cast(bf16x8, pb);
        vf[8] = VRD(8, SV);
        o[3] = MFMA(vf[3], p0f, o[3]); SB();
        float ma = max3f(s[0], s[1], s[2]), mb = max3f(s[8], s[9], s[10]);
        ma = max3f(ma, s[3], s[4]); mb = max3f(mb, s[11], s[12]);
        ma = max3f(ma, s[5], s[6]); mb = max3f(mb, s[13], s[14]); PIN2(ma, mb); SB();
        PIN2(vf[4], vf[5]);
        vf[9] = VRD(9, SV);
        o[4] = MFMA(vf[4], p0f, o[4]);
        ma = max3f(ma, s[7], s[15]);
        float pm = swap_max(max2f(ma, mb)); PIN1(pm); SB();
        vf[10] = VRD(10, SV); vf[11] = VRD(11, SV);
        o[5] = MFMA(vf[5], p0f, o[5]); SB();
        if (__builtin_expect(__any(pm > mthr), 0)) {
            const float mn = max2f(m, pm);
            alpha = __builtin_amdgcn_exp2f((m - mn) * SCALE_LOG2E);
            m = mn; mthr = m + THR_RAW; mc = -m * SCALE_LOG2E; pend = 1;
        }
        PIN2(vf[6], vf[7]);
        o[0] = MFMA(vf[6], p1f, o[0]); FMAX2(0, 1); FMAX2(2, 3); FMAX2(4, 5); SB();
        o[1] = MFMA(vf[7], p1f, o[1]); FMAX2(6, 7); FMAX2(8, 9); FMAX2(10, 11); SB();
        GATE(mW, SV);
        kf[0] = KRD(0, RKN); SB();
        o[2] = MFMA(vf[8], p1f, o[2]); kf[1] = KRD(1, RKN); PIECE(S == 1, 0, soB, mW, SV); FMAX2(12, 13); SB();
        o[3] = MFMA(vf[9], p1f, o[3]); kf[2] = KRD(2, RKN); PIECE(S == 1, 1, soB, mW, SV); FMAX2(14, 15); SB();
        o[4] = MFMA(vf[10], p1f, o[4]); PIECE(S == 1, 2, soB, mW, SV); kf[3] = KRD(3, RKN); if (S == 2) PIECE(false, 3, soB, mW, SV); SB();
        o[5] = MFMA(vf[11], p1f, o[5]); if (S == 2) PIECE(false, 4, soB, mW, SV); kf[4] = KRD(4, RKN); if (S == 2) PIECE(false, 5, soB, mW, SV); SB();
        soA -= TSTEP; soB -= TSTEP;
        if (__builtin_expect(pend, 0)) {
            l *= alpha;
#pragma unroll
            for (int c = 0; c < 6; ++c) o[c] *= alpha;
            pend = 0;
        }
        ++it;
    };
    bool skip0 = false;
    if (S == 1 && late) {
        constexpr unsigned SK = SLOT, SV = 2 * SLOT, RKN = 3 * SLOT - KB;
        kf[5] = KRD(5, 0); kf[6] = KRD(6, 0); kf[7] = KRD(7, 0); kf[8] = KRD(8, 0);
        s = MFMA(kf[0], q[0], f32x16{});
        kf[9] = KRD(9, 0); kf[10] = KRD(10, 0); kf[11] = KRD(11, 0);
#pragma unroll
        for (int i = 1; i < 8; ++i) s = MFMA(kf[i], q[i], s);
        GATE(mW, SK);
        s = MFMA(kf[8], q[8], s); SB();
        PIECE(false, 0, soA, mW, SK); s = MFMA(kf[9], q[9], s); SB();
        PIECE(false, 1, soA, mW, SK); s = MFMA(kf[10], q[10], s); SB();
        PIECE(false, 2, soA, mW, SK); s = MFMA(kf[11], q[11], s); SB();
        GATE(mW, SV);
        kf[0] = KRD(0, RKN); PIECE(true, 0, soB, mW, SV); kf[1] = KRD(1, RKN); PIECE(true, 1, soB, mW, SV); kf[2] = KRD(2, RKN); PIECE(true, 2, soB, mW, SV);
        kf[3] = KRD(3, RKN); kf[4] = KRD(4, RKN);
        mask_diag();
        first_max();
        soA -= TSTEP; soB -= TSTEP;
        it = 1; skip0 = true;
    }
    if (act && it < itend) {
        for (;;) {
            if (!skip0) { iter(IC<0>{}); if (it >= itend) break; }
            skip0 = false;
            iter(IC<1>{}); if (it >= itend) break;
            iter(IC<2>{}); if (it >= itend) break;
        }
    }
    STAMP(7);
    const int ph = it % 3;
    unsigned tk = ((S == 1) ? 1 + 2 * ph : (ph == 0 ? 1 : ph == 1 ? 0 : 2)) * SLOT;
    unsigned tv = ((S == 1) ? (ph == 2 ? 0 : 2 + 2 * ph) : 2 - ph) * SLOT;
    auto adv2 = [&](unsigned a) { const unsigned t = a + 2 * SLOT; return t >= NS * SLOT ? t - NS * SLOT : t; };
    if (act) {
        const unsigned mk = mW + tk, mv = mW + tv;
        GATE(mk, 0);
        const unsigned va = vA + tv, vb = vB + tv;
        bf16x8 vf[12];
#define VRT(i) ldv(((((i) % 6) & 1) ? vb : va) + (((i) % 6) >> 1) * 4096 + ((i) / 6) * 2048)
        vf[0] = VRT(0); PIECE(S == 2, 0, soA, mk, 0); vf[1] = VRT(1); PIECE(S == 2, 1, soA, mk, 0); vf[2] = VRT(2); PIECE(S == 2, 2, soA, mk, 0);
        vf[3] = VRT(3); if (S == 2) PIECE(true, 3, soA, mk, 0); vf[4] = VRT(4); if (S == 2) PIECE(true, 4, soA, mk, 0); vf[5] = VRT(5); if (S == 2) PIECE(true, 5, soA, mk, 0);
        float ls0 = 0.f, ls1 = 0.f;
#pragma unroll
        for (int i = 0; i < 16; i += 2) { x[i] = __builtin_amdgcn_exp2f(x[i]); x[i + 1] = __builtin_amdgcn_exp2f(x[i + 1]); ls0 += x[i]; ls1 += x[i + 1]; }
        l += ls0 + ls1;
#pragma unroll
        for (int i = 0; i < 4; ++i) { pa[i] = cvtpk(x[2 * i], x[2 * i + 1]); pb[i] = cvtpk(x[8 + 2 * i], x[9 + 2 * i]); }
        PIN4(pa[0], pa[1], pa[2], pa[3]); PIN4(pb[0], pb[1], pb[2], pb[3]);
        const bf16x8 p0f = __builtin_bit_cast(bf16x8, pa), p1f = __builtin_bit_cast(bf16x8, pb);
        SB();
        vf[6] = VRT(6); vf[7] = VRT(7);
        o[0] = MFMA(vf[0], p0f, o[0]); SB();
        vf[8] = VRT(8); vf[9] = VRT(9);
        o[1] = MFMA(vf[1], p0f, o[1]); SB();
        vf[10] = VRT(10); vf[11] = VRT(11);
        o[2] = MFMA(vf[2], p0f, o[2]); SB();
        o[3] = MFMA(vf[3], p0f, o[3]); o[4] = MFMA(vf[4], p0f, o[4]); o[5] = MFMA(vf[5], p0f, o[5]);
        o[0] = MFMA(vf[6], p1f, o[0]); o[1] = MFMA(vf[7], p1f, o[1]); SB();
        if (it + 1 < steps) {
            GATE(mv, 0);
            NOP0();
            PIECE(S == 1, 0, soB, mv, 0); NOP0(); PIECE(S == 1, 1, soB, mv, 0); NOP0(); PIECE(S == 1, 2, soB, mv, 0);
            if (S == 2) { NOP0(); PIECE(false, 3, soB, mv, 0); NOP0(); PIECE(false, 4, soB, mv, 0); NOP0(); PIECE(false, 5, soB, mv, 0); }
        }
        o[2] = MFMA(vf[8], p1f, o[2]); o[3] = MFMA(vf[9], p1f, o[3]); o[4] = MFMA(vf[10], p1f, o[4]); o[5] = MFMA(vf[11], p1f, o[5]);
        tk = adv2(tk); tv = adv2(tv); soA -= TSTEP; soB -= TSTEP;
        ++it;
    }
    STAMP(8);
    for (; it < steps; ++it) {
        const unsigned mk = mW + tk, mv = mW + tv;
        GATE(mk, 0);
        NOP0();
        PIECE(S == 2, 0, soA, mk, 0); NOP0(); PIECE(S == 2, 1, soA, mk, 0); NOP0(); PIECE(S == 2, 2, soA, mk, 0);
        if (S == 2) { NOP0(); PIECE(true, 3, soA, mk, 0); NOP0(); PIECE(true, 4, soA, mk, 0); NOP0(); PIECE(true, 5, soA, mk, 0); }
        if (it + 1 < steps) {
            GATE(mv, 0);
            NOP0();
            PIECE(S == 1, 0, soB, mv, 0); NOP0(); PIECE(S == 1, 1, soB, mv, 0); NOP0(); PIECE(S == 1, 2, soB, mv, 0);
            if (S == 2) { NOP0(); PIECE(false, 3, soB, mv, 0); NOP0(); PIECE(false, 4, soB, mv, 0); NOP0(); PIECE(false, 5, soB, mv, 0); }
        }
        tk = adv2(tk); tv = adv2(tv); soA -= TSTEP; soB -= TSTEP;
    }

    STAMP(10);
    l = swap_add(l);
    const __amdgpu_buffer_rsrc_t orsrc = __builtin_amdgcn_make_buffer_rsrc((void*)(op + (size_t)s0 * QSTRIDE), 0, (unsigned)L * QSTRIDE, 0x00020000);
    const unsigned stw = r32 * ORS + 8 * h;
    auto stage_block = [&](unsigned base, int c, const f32x16& z) {
#pragma unroll
        for (int g = 0; g < 4; ++g) {
            u32x2 v = {cvtpk(z[4 * g], z[4 * g + 1]), cvtpk(z[4 * g + 2], z[4 * g + 3])};
            *(LDSP(u32x2))(size_t)(base + (32 * c + 8 * g) * 2) = v;
        }
    };
    unsigned lrd[3], gof[3];
#pragma unroll
    for (int j = 0; j < 3; ++j) { const unsigned idx = 64 * j + lane, row = (idx * 2731) >> 16, ch = idx - 24 * row; lrd[j] = row * ORS + ch * 16; gof[j] = row * QSTRIDE + ch * 16; }
    wait_vm<0>();
    barrier();
    STAMP(11);
    if (S == 1) {
        if (act) {
            const unsigned stg = lds0 + w * (32 * ORS);
            const float inv = __builtin_amdgcn_rcpf(l);
#pragma unroll
            for (int c = 0; c < 6; ++c) { const f32x16 z = o[c] * inv; stage_block(stg + stw, c, z); }
            STAMP(15);
            const unsigned obase = (unsigned)pw * QSTRIDE + (unsigned)(2 * grp + hh) * ROWB;
#pragma unroll
            for (int k = 0; k < 4; ++k)
#pragma unroll
                for (int j = 0; j < 3; ++j) {
                    const u32x4 v = *(LDSP(u32x4))(size_t)(stg + lrd[j] + 8 * k * ORS);
                    __builtin_amdgcn_raw_buffer_store_b128(v, orsrc, gof[j], obase + 8 * k * QSTRIDE, STAUX);
                }
        }
    } else {
        const unsigned mine = lds0 + w * SLOT + lane * 16, other = lds0 + (w ^ 2) * SLOT + lane * 16;
        {
            f32x2 ml = {m, l};
            *(LDSP(f32x2))(size_t)(lds0 + 4 * SLOT + w * 512 + lane * 8) = ml;
        }
        auto send = [&](const f32x16& z, int c) {
#pragma unroll
            for (int g = 0; g < 4; ++g) { f32x4 y = {z[4 * g], z[4 * g + 1], z[4 * g + 2], z[4 * g + 3]}; *(LDSP(f32x4))(size_t)(mine + (c * 4 + g) * 1024) = y; }
        };
        if (sig) { send(o[0], 0); send(o[1], 1); send(o[2], 2); } else { send(o[3], 0); send(o[4], 1); send(o[5], 2); }
        STAMP(12);
        barrier();
        STAMP(13);
        const f32x2 mlo = *(LDSP(f32x2))(size_t)(lds0 + 4 * SLOT + (w ^ 2) * 512 + lane * 8);
        const float mx = max2f(m, mlo[0]);
        const float a0 = __builtin_amdgcn_exp2f((m - mx) * SCALE_LOG2E), a1 = __builtin_amdgcn_exp2f((mlo[0] - mx) * SCALE_LOG2E);
        const float inv = __builtin_amdgcn_rcpf(l * a0 + mlo[1] * a1);
        const float f0 = a0 * inv, f1 = a1 * inv;
        const unsigned stg = lds0 + 51200 + hh * (32 * ORS);
        auto merge = [&](const f32x16& xo, int c, int cd) {
            f32x16 z;
#pragma unroll
            for (int g = 0; g < 4; ++g) {
                const f32x4 y = *(LDSP(f32x4))(size_t)(other + (c * 4 + g) * 1024);
                const f32x4 t = {xo[4 * g], xo[4 * g + 1], xo[4 * g + 2], xo[4 * g + 3]};
                const f32x4 u = t * f0 + y * f1;
                z[4 * g] = u[0]; z[4 * g + 1] = u[1]; z[4 * g + 2] = u[2]; z[4 * g + 3] = u[3];
            }
            stage_block(stg + stw, cd, z);
        };
        if (sig) { merge(o[3], 0, 3); merge(o[4], 1, 4); merge(o[5], 2, 5); } else { merge(o[0], 0, 0); merge(o[1], 1, 1); merge(o[2], 2, 2); }
        STAMP(14);
        barrier();
        STAMP(15);
        const unsigned obase = (unsigned)(pw + 16 * sig) * QSTRIDE + (unsigned)(2 * grp + hh) * ROWB;
        const unsigned stgr = stg + 16 * sig * ORS;
#pragma unroll
        for (int k = 0; k < 2; ++k)
#pragma unroll
            for (int j = 0; j < 3; ++j) {
                const u32x4 v = *(LDSP(u32x4))(size_t)(stgr + lrd[j] + 8 * k * ORS);
                __builtin_amdgcn_raw_buffer_store_b128(v, orsrc, gof[j], obase + 8 * k * QSTRIDE, STAUX);
            }
    }
    STAMP(16);
}

template <int CTRL> DI int dpp_mov(int old, int x) { return __builtin_amdgcn_update_dpp(old, x, CTRL, 0xf, 0xf, false); }
DI int wave_incl_scan(int x) {
    x += dpp_mov<0x111>(0, x);
    x += dpp_mov<0x112>(0, x);
    x += dpp_mov<0x114>(0, x);
    x += dpp_mov<0x118>(0, x);
    x += __builtin_amdgcn_update_dpp(0, x, 0x142, 0xa, 0xf, false);
    x += __builtin_amdgcn_update_dpp(0, x, 0x143, 0xc, 0xf, false);
    return x;
}
DI int wave_max_nonneg(int x) {
    x = max(x, dpp_mov<0x111>(0, x));
    x = max(x, dpp_mov<0x112>(0, x));
    x = max(x, dpp_mov<0x114>(0, x));
    x = max(x, dpp_mov<0x118>(0, x));
    x = max(x, __builtin_amdgcn_update_dpp(0, x, 0x142, 0xa, 0xf, false));
    x = max(x, __builtin_amdgcn_update_dpp(0, x, 0x143, 0xc, 0xf, false));
    return __builtin_amdgcn_readlane(x, 63);
}

DI bool decode_slow(const int* __restrict__ cu, int nseq, int k, int& s0, int& L, int& p0, int& isx) {
    int nmax = 0;
    for (int s = 0; s < nseq; ++s) nmax = max(nmax, (cu[s + 1] - cu[s] + 31) >> 5);
    const int B = BSPLIT;
    int N = 0;
    for (int s = 0; s < nseq; ++s) { const int n = (cu[s + 1] - cu[s] + 31) >> 5; N += max(n - B, 0) + min(B >> 1, (n + 1) >> 1); }
    int rank = k;
    if (k < CUS_PER_XCD) { const int N2 = min(max(N - CUS_PER_XCD, 0), (PFMODE != 0) ? PF0 - CUS_PER_XCD : CUS_PER_XCD); rank = (k < N2) ? CUS_PER_XCD - 1 - k : k - N2; }
    if (rank >= N) return false;
    int jx = nmax, my = min(B >> 1, (nmax + 1) >> 1);
    for (;;) {
        const int x = (jx > B) && (((jx + 1) >> 1) >= 2 * my);
        const int thr = x ? 32 * (jx - 1) : 64 * (my - 1);
        for (int s = 0; s < nseq; ++s) {
            const int len = cu[s + 1] - cu[s];
            if (len > thr) { if (rank == 0) { s0 = cu[s]; L = len; p0 = thr; isx = x; return true; } --rank; }
        }
        if (x) --jx; else --my;
    }
}

__global__ __launch_bounds__(256, 2) void attn_varlen_kernel(const char* __restrict__ qp, const char* __restrict__ kp, const char* __restrict__ vp,
                                                             const int* __restrict__ cu, char* __restrict__ op, int nseq, int T
                                                             ) {
    __shared__ __attribute__((aligned(16))) char smem_[LDS_BYTES];
    const int grp = blockIdx.x & 7;
    const int kraw = blockIdx.x >> 3;
    const int lane = threadIdx.x & 63;

    asm volatile("" :: "s"(qp), "s"(kp), "s"(vp), "s"(op), "s"(cu), "s"(nseq));
    if (CODEPF > 0 && kraw < CODEWG) {
        unsigned long long pc; asm volatile("s_getpc_b64 %0" : "=s"(pc));
        const char* cb = (const char*)(pc & ~127ull);
        const unsigned mscr0 = rfl((unsigned)(size_t)(lds_char_ptr)smem_ + 73728 + 1024 + (threadIdx.x >> 6) * 256);
        asm volatile("s_mov_b32 m0, %0" :: "s"(mscr0) : "memory");
        for (unsigned o = threadIdx.x * 128; o < CODEPF; o += 256 * 128)
            asm volatile("global_load_lds_dword %0, off" :: "v"(cb + o) : "memory");
    }
    if (PFMODE != 0 && kraw >= PF0 && kraw < PF0 + 8) {
        const int wv = rfl(threadIdx.x >> 6);
        const unsigned mscr = rfl((unsigned)(size_t)(lds_char_ptr)smem_ + wv * 256);
        asm volatile("s_mov_b32 m0, %0" :: "s"(mscr) : "memory");
        const unsigned j = (kraw - PF0) * 256 + threadIdx.x;
        const u32x4 ro = make_srd(op, (unsigned)T * QSTRIDE), rq = make_srd(qp, (unsigned)T * QSTRIDE);
        const u32x4 rk = make_srd(kp, (unsigned)T * KSTRIDE), rv = make_srd(vp, (unsigned)T * KSTRIDE);
        asm volatile("s_nop 4" :: "s"(ro), "s"(rq), "s"(rk), "s"(rv) : "memory");
        for (unsigned off = j * PSTRIDE; off < (unsigned)T * QSTRIDE; off += 2048 * PSTRIDE) {
            if (PFMODE & 1) asm volatile("buffer_load_dword %0, %1, 0 offen nt lds" :: "v"(off), "s"(ro) : "memory");
            if (PFMODE & 2) {
                asm volatile("buffer_load_dword %0, %1, 0 offen nt lds" :: "v"(off), "s"(rq) : "memory");
                asm volatile("buffer_load_dword %0, %1, 0 offen nt lds" :: "v"(off), "s"(rk) : "memory");
                asm volatile("buffer_load_dword %0, %1, 0 offen nt lds" :: "v"(off), "s"(rv) : "memory");
            }
        }
        return;
    }
    const int k = (PFMODE != 0 && kraw >= PF0 + 8) ? kraw - 8 : kraw;
    typedef unsigned u32x16 __attribute__((ext_vector_type(16)));
    u32x16 cus;
    {
        const u32x4 cr = make_srd(cu, (unsigned)(nseq + 1) * 4);
        asm volatile("s_buffer_load_dwordx16 %0, %1, 0" : "=s"(cus) : "s"(cr) : "memory");
    }
    const int l3 = (lane * 43) >> 7, r3 = lane - 3 * l3, st = l3 + 1;
    const int jx = 2 * st - 1 + r3;
    const bool okx = (r3 < 2) & (jx > BSPLIT) & (lane < 63), oky = (r3 == 2) & !(st & 1) & (st <= BSPLIT);
    int thr = oky ? 32 * (st - 2) : 0x7fffffff;
    thr = okx ? 32 * (jx - 1) : thr;
    asm volatile("s_waitcnt lgkmcnt(0)" : "+s"(cus) :: "memory");
    int len = 0, c0 = 0, cnt = 0;
#define SEQ(s) do { const int ls = (int)cus[(s) + 1] - (int)cus[s];                         \
        cnt += (ls > thr) ? 1 : 0;                                                                                                      \
        asm("v_writelane_b32 %0, %1, %2" : "+v"(len) : "s"(ls), "i"(s)); asm("v_writelane_b32 %0, %1, %2" : "+v"(c0) : "s"(cus[s]), "i"(s)); } while (0)
    SEQ(0); SEQ(1); SEQ(2); SEQ(3); SEQ(4); SEQ(5); SEQ(6); SEQ(7);
    if (__builtin_expect(nseq > 8, 0)) { SEQ(8); SEQ(9); SEQ(10); SEQ(11); SEQ(12); SEQ(13); SEQ(14); }
    const int incl = wave_incl_scan(cnt);
    const int N = __builtin_amdgcn_readlane(incl, 63);
    const int N2 = min(max(N - CUS_PER_XCD, 0), (PFMODE != 0) ? PF0 - CUS_PER_XCD : CUS_PER_XCD);
    (void)N2;
    const int rank = (k >= CUS_PER_XCD && N <= ((PFMODE != 0) ? PF0 : 2 * CUS_PER_XCD)) ? (k < N ? N - 1 - (k - CUS_PER_XCD) : N) : k;
    bool valid = rank < N;
    const int a = N - rank;
    const int lv = (__ffsll((long long)__ballot(incl >= a)) - 1) & 63;
    const int idx = __builtin_amdgcn_readlane(incl, lv) - a;
    int p0 = __builtin_amdgcn_readlane(thr, lv);
    int isx = __builtin_amdgcn_readlane(r3, lv) < 2;
    const bool has = len > p0;
    const unsigned long long mk = __ballot(has);
    const int below = __builtin_amdgcn_mbcnt_hi((unsigned)(mk >> 32), __builtin_amdgcn_mbcnt_lo((unsigned)mk, 0));
    const int sq = (__ffsll((long long)__ballot(has && below == idx)) - 1) & 63;
    int s0 = __builtin_amdgcn_readlane(c0, sq), L = __builtin_amdgcn_readlane(len, sq);
    if (__builtin_expect(nseq > 15 || __ballot(len > 32 * 42) != 0, 0)) {
        int a0 = 0, a1 = 0, a2 = 0, a3 = 0;
        const int v = decode_slow(cu, nseq, k, a0, a1, a2, a3);
        valid = rfl(v) != 0; s0 = rfl(a0); L = rfl(a1); p0 = rfl(a2); isx = rfl(a3);
    }
    if (!valid) return;
    s0 = rfl(s0); L = rfl(L); p0 = rfl(p0); isx = rfl(isx);
    if (PRIO && k < CUS_PER_XCD) __builtin_amdgcn_s_setprio(PRIO);
    const unsigned lds0 = (unsigned)(size_t)(lds_char_ptr)smem_;
    if (isx) {
        const int n = (min(p0 + 32, L) + 31) >> 5;
        attn_item<2>(qp, kp, vp, op, grp, s0, L, p0, n, lds0 STPASS);
    } else {
        const int n = (min(p0 + 64, L) + 31) >> 5;
        attn_item<1>(qp, kp, vp, op, grp, s0, L, p0, n, lds0 STPASS);
    }
}

extern "C" void kernel_launch(void* const* d_in, const int* in_sizes, int n_in, void* d_out, int out_size, void* d_ws, size_t ws_size,
                              hipStream_t stream) {
    (void)n_in; (void)out_size; (void)d_ws; (void)ws_size;
    const int T = in_sizes[0] / (HQ * D);
    const int nseq = in_sizes[3] - 1;
    const int per_head = T / 32 + nseq + 1 + 8;
    attn_varlen_kernel<<<dim3(8 * per_head), dim3(256), 0, stream>>>((const char*)d_in[0], (const char*)d_in[1], (const char*)d_in[2],
                                                                    (const int*)d_in[3], (char*)d_out, nseq, T
                                                                    );
}
```

```cpp
#define DIAG_PHASE 9
#define DIAG_BLK 0
#include <hip/hip_runtime.h>
#include <hip/hip_bf16.h>
#include <stdint.h>

typedef __bf16 bf16x8 __attribute__((ext_vector_type(8)));
typedef __bf16 bf16x2 __attribute__((ext_vector_type(2)));
typedef short s16x4 __attribute__((ext_vector_type(4)));
typedef short s16x8 __attribute__((ext_vector_type(8)));
typedef float f32x2 __attribute__((ext_vector_type(2)));
typedef float f32x4 __attribute__((ext_vector_type(4)));
typedef float f32x16 __attribute__((ext_vector_type(16)));
typedef unsigned u32x2 __attribute__((ext_vector_type(2)));
typedef unsigned u32x4 __attribute__((ext_vector_type(4)));
#define LDSP(T) __attribute__((address_space(3))) T*
typedef LDSP(char) lds_char_ptr;

#define DI __device__ __forceinline__
#define PH(p) do { if ((p) == DIAG_PHASE) { unsigned long long t_; asm volatile("s_memrealtime %0\n\ts_waitcnt lgkmcnt(0)" : "=s"(t_) :: "memory"); g_tphase = (unsigned)t_; } } while (0)
#define PHARG , unsigned& g_tphase
#define PHPASS , g_tphase
#define LDPOL ""
#define FTPOL ""
#define PSTRIDE 4096
#define PAIRMODE 1
#define PRIO 2
#define QPOL ""
#define STAUX 16
#define PFPOL ""
#define PFMODE 0
#define CODEPF 0
#define CODEWG 2
#define STAMP(i) do {} while (0)
#define STARG
#define STPASS

constexpr int HQ = 16, HKV = 8, D = 192;
constexpr unsigned QSTRIDE = HQ * D * 2;
constexpr unsigned KSTRIDE = HKV * D * 2;
constexpr unsigned ROWB = D * 2;
constexpr unsigned SLOT = 32 * ROWB;
constexpr unsigned ORS = 400;
constexpr unsigned LDS_BYTES = 77824;
constexpr float SCALE_LOG2E = 0.07216878364870322f * 1.4426950408889634f;
constexpr float THR_RAW = 8.0f / SCALE_LOG2E;
constexpr int CUS_PER_XCD = 32;
constexpr int PF0 = 56;
constexpr int BSPLIT = 8;

DI int rfl(int x) { return __builtin_amdgcn_readfirstlane(x); }
DI unsigned cvtpk(float lo, float hi) {
    f32x2 v = {lo, hi}; bf16x2 b = __builtin_convertvector(v, bf16x2); return __builtin_bit_cast(unsigned, b);
}
DI float max2f(float a, float b) { return __builtin_elementwise_maximum(a, b); }
DI float max3f(float a, float b, float c) { return max2f(max2f(a, b), c); }
DI float swap_max(float x) {
    auto r = __builtin_amdgcn_permlane32_swap(__float_as_uint(x), __float_as_uint(x), false, false);
    return max2f(__uint_as_float(r[0]), __uint_as_float(r[1]));
}
DI float swap_add(float x) {
    auto r = __builtin_amdgcn_permlane32_swap(__float_as_uint(x), __float_as_uint(x), false, false);
    return __uint_as_float(r[0]) + __uint_as_float(r[1]);
}
DI u32x4 make_srd(const void* base, unsigned nbytes) {
    unsigned long long a = (unsigned long long)base;
    u32x4 s; s[0] = rfl((unsigned)a); s[1] = rfl((unsigned)(a >> 32) & 0xffffu); s[2] = rfl(nbytes); s[3] = 0x00020000u;
    return s;
}
template <int IMM> DI void dma16(unsigned m0v, unsigned voff, u32x4 srd, unsigned soff) {
    asm volatile("s_mov_b32 m0, %0\n\ts_nop 0\n\tbuffer_load_dwordx4 %1, %2, %3 offen offset:%4 lds"
                 :: "s"(m0v), "v"(voff), "s"(srd), "s"(soff), "i"(IMM) : "memory");
}
template <int IMM, int NEXT> DI void dma16c_nt(unsigned voff, u32x4 srd, unsigned soff, unsigned nbase) {
    asm volatile("buffer_load_dwordx4 %0, %1, %2 offen offset:%3 " FTPOL " lds\n\ts_add_u32 m0, %4, %5"
                 :: "v"(voff), "s"(srd), "s"(soff), "i"(IMM), "s"(nbase), "i"(NEXT) : "memory", "scc");
}
template <int IMM> DI void dma16e_nt(unsigned voff, u32x4 srd, unsigned soff) {
    asm volatile("buffer_load_dwordx4 %0, %1, %2 offen offset:%3 " FTPOL " lds" :: "v"(voff), "s"(srd), "s"(soff), "i"(IMM) : "memory");
}
template <int IMM, int NEXT> DI void dma16c(unsigned voff, u32x4 srd, unsigned soff, unsigned nbase) {
    asm volatile("buffer_load_dwordx4 %0, %1, %2 offen offset:%3 " LDPOL " lds\n\ts_add_u32 m0, %4, %5"
                 :: "v"(voff), "s"(srd), "s"(soff), "i"(IMM), "s"(nbase), "i"(NEXT) : "memory", "scc");
}
template <int IMM, int NEXT> DI void dmaqc(unsigned voff, u32x4 srd, unsigned soff, unsigned nbase) {
    asm volatile("buffer_load_dwordx4 %0, %1, %2 offen offset:%3 " QPOL " lds\n\ts_add_u32 m0, %4, %5"
                 :: "v"(voff), "s"(srd), "s"(soff), "i"(IMM), "s"(nbase), "i"(NEXT) : "memory", "scc");
}
template <int IMM> DI void dmaqe(unsigned voff, u32x4 srd, unsigned soff) {
    asm volatile("buffer_load_dwordx4 %0, %1, %2 offen offset:%3 " QPOL " lds" :: "v"(voff), "s"(srd), "s"(soff), "i"(IMM) : "memory");
}
template <int IMM> DI void dma16e(unsigned voff, u32x4 srd, unsigned soff) {
    asm volatile("buffer_load_dwordx4 %0, %1, %2 offen offset:%3 " LDPOL " lds" :: "v"(voff), "s"(srd), "s"(soff), "i"(IMM) : "memory");
}
template <int N> DI void wait_vm() { asm volatile("s_waitcnt vmcnt(%0)" :: "i"(N) : "memory"); }
DI void barrier() { asm volatile("s_waitcnt lgkmcnt(0)\n\ts_barrier" ::: "memory"); }
#define SB() __builtin_amdgcn_sched_barrier(0)
#define MFMA(a, b, c) __builtin_amdgcn_mfma_f32_32x32x16_bf16((a), (b), (c), 0, 0, 0)

DI bf16x8 ldk(unsigned addr) { return *(LDSP(bf16x8))(size_t)addr; }
DI bf16x8 ldv(unsigned addr) {
    const s16x4 lo = __builtin_amdgcn_ds_read_tr16_b64_v4i16((LDSP(s16x4))(size_t)addr);
    const s16x4 hi = __builtin_amdgcn_ds_read_tr16_b64_v4i16((LDSP(s16x4))(size_t)(addr + 1024));
    return __builtin_bit_cast(bf16x8, __builtin_shufflevector(lo, hi, 0, 1, 2, 3, 4, 5, 6, 7));
}

template <int V> struct IC { static constexpr int value = V; };

template <int S>
DI void attn_item(const char* __restrict__ qp, const char* __restrict__ kp, const char* __restrict__ vp, char* __restrict__ op,
                  int grp, int s0, int L, int p0, int n, unsigned lds0 STARG PHARG) {
    constexpr int NS = (S == 1) ? 6 : 3;
    constexpr int PW = (S == 1) ? 3 : 6;
    constexpr unsigned TSTEP = 32 * S * KSTRIDE;
    const int tid = threadIdx.x, lane = tid & 63;
    const int w = rfl(tid >> 6);
    const int hh = w & 1, up = w >> 1;
    const int r32 = lane & 31, h = lane >> 5;
    const int sig = (S == 1) ? 0 : up;
    const int pw = (S == 1) ? p0 + 32 * up : p0;
    const int steps = (S == 1) ? n : (n + 1) >> 1;
    const int tdiag = pw >> 5;

    const int kend = min(L, 32 * n);
    const u32x4 ksrd = make_srd(kp + (size_t)s0 * KSTRIDE + grp * ROWB, (unsigned)(kend - 1) * KSTRIDE + ROWB);
    const u32x4 vsrd = make_srd(vp + (size_t)s0 * KSTRIDE + grp * ROWB, (unsigned)(kend - 1) * KSTRIDE + ROWB);
    const u32x4 qsrd = make_srd(qp + (size_t)s0 * QSTRIDE, (unsigned)L * QSTRIDE);

    const unsigned ring = lds0 + sig * NS * SLOT;
    const unsigned mW = ring + ((S == 1) ? w * 1024 : hh * 1024);
    const unsigned drow = (unsigned)(8 * hh + (lane >> 3) + ((S == 1) ? 16 * up : 0)) * KSTRIDE;
    const unsigned voffK = drow + 16u * ((lane & 7) ^ ((hh << 2) | (lane >> 4)));
    const unsigned voffV = drow + 16u * ((lane & 7) ^ (((lane >> 4) & 1) << 2));
    const unsigned voffK2 = voffK + 16 * KSTRIDE, voffV2 = voffV + 16 * KSTRIDE;
#define PIECE(ISV, j, so, mb, MO) do { const u32x4 sr_ = (ISV) ? vsrd : ksrd;                                                            \
        const unsigned v1_ = (ISV) ? voffV : voffK, v2_ = (ISV) ? voffV2 : voffK2;                                                       \
        if (j == 0) dma16c<0, (MO) + 4096 - 128>(v1_, sr_, so, mb);                                                                      \
        else if (j == 1) dma16c<128, (MO) + 8192 - 256>(v1_, sr_, so, mb);                                                               \
        else if (j == 2) { if (S == 1) dma16e<256>(v1_, sr_, so); else dma16c<256, (MO) + 2048>(v1_, sr_, so, mb); }                      \
        else if (j == 3) dma16c<0, (MO) + 2048 + 4096 - 128>(v2_, sr_, so, mb);                                                          \
        else if (j == 4) dma16c<128, (MO) + 2048 + 8192 - 256>(v2_, sr_, so, mb);                                                        \
        else dma16e<256>(v2_, sr_, so); } while (0)
#define PIECEN(ISV, j, so, mb, MO) do { const u32x4 sr_ = (ISV) ? vsrd : ksrd;                                                           \
        const unsigned v1_ = (ISV) ? voffV : voffK, v2_ = (ISV) ? voffV2 : voffK2;                                                       \
        if (j == 0) dma16c_nt<0, (MO) + 4096 - 128>(v1_, sr_, so, mb);                                                                   \
        else if (j == 1) dma16c_nt<128, (MO) + 8192 - 256>(v1_, sr_, so, mb);                                                            \
        else if (j == 2) { if (S == 1) dma16e_nt<256>(v1_, sr_, so); else dma16c_nt<256, (MO) + 2048>(v1_, sr_, so, mb); }                \
        else if (j == 3) dma16c_nt<0, (MO) + 2048 + 4096 - 128>(v2_, sr_, so, mb);                                                       \
        else if (j == 4) dma16c_nt<128, (MO) + 2048 + 8192 - 256>(v2_, sr_, so, mb);                                                     \
        else dma16e_nt<256>(v2_, sr_, so); } while (0)
#define ELEMN(ISV, so, mb, MO) do { SETM0(mb, MO); PIECEN(ISV, 0, so, mb, MO); NOP0(); PIECEN(ISV, 1, so, mb, MO); NOP0(); PIECEN(ISV, 2, so, mb, MO);  \
        if (S == 2) { NOP0(); PIECEN(ISV, 3, so, mb, MO); NOP0(); PIECEN(ISV, 4, so, mb, MO); NOP0(); PIECEN(ISV, 5, so, mb, MO); } } while (0)
#define SETM0(mb, MO) asm volatile("s_add_u32 m0, %0, %1\n\ts_nop 0" :: "s"(mb), "i"(MO) : "memory", "scc")
#define ELEM(ISV, so, mb, MO) do { SETM0(mb, MO); PIECE(ISV, 0, so, mb, MO); NOP0(); PIECE(ISV, 1, so, mb, MO); NOP0(); PIECE(ISV, 2, so, mb, MO);      \
        if (S == 2) { NOP0(); PIECE(ISV, 3, so, mb, MO); NOP0(); PIECE(ISV, 4, so, mb, MO); NOP0(); PIECE(ISV, 5, so, mb, MO); } } while (0)
#define NOP0() asm volatile("s_nop 0" ::: "memory")
    const unsigned so0 = (unsigned)(32 * (n - 1 - sig)) * KSTRIDE;

    STAMP(2); PH(2);
    ELEMN(false, so0, mW, 0);
    const unsigned qslot = (S == 1) ? lds0 + (2 + w) * SLOT : lds0 + (3 * hh + 2) * SLOT;
    {
        const unsigned qrow = (unsigned)(2 * grp + hh) * ROWB + (unsigned)pw * QSTRIDE;
        const unsigned l8 = (unsigned)(lane >> 3) * QSTRIDE;
        if (S == 1) {
            const unsigned vq0 = l8 + 16u * ((lane & 7) ^ (lane >> 4)), vq1 = l8 + 8 * QSTRIDE + 16u * ((lane & 7) ^ (4 | (lane >> 4)));
            const unsigned so2 = qrow + 16 * QSTRIDE;
            SETM0(qslot, 0);
            dmaqc<0, 4096 - 128>(vq0, qsrd, qrow, qslot); NOP0(); dmaqc<128, 8192 - 256>(vq0, qsrd, qrow, qslot); NOP0(); dmaqc<256, 1024>(vq0, qsrd, qrow, qslot); NOP0();
            dmaqc<0, 1024 + 4096 - 128>(vq1, qsrd, qrow, qslot); NOP0(); dmaqc<128, 1024 + 8192 - 256>(vq1, qsrd, qrow, qslot); NOP0(); dmaqc<256, 2048>(vq1, qsrd, qrow, qslot); NOP0();
            dmaqc<0, 2048 + 4096 - 128>(vq0, qsrd, so2, qslot); NOP0(); dmaqc<128, 2048 + 8192 - 256>(vq0, qsrd, so2, qslot); NOP0(); dmaqc<256, 3072>(vq0, qsrd, so2, qslot); NOP0();
            dmaqc<0, 3072 + 4096 - 128>(vq1, qsrd, so2, qslot); NOP0(); dmaqc<128, 3072 + 8192 - 256>(vq1, qsrd, so2, qslot); NOP0(); dmaqe<256>(vq1, qsrd, so2);
        } else {
            const unsigned vq = l8 + (unsigned)(8 * up) * QSTRIDE + 16u * ((lane & 7) ^ ((up << 2) | (lane >> 4))), vq2 = vq + 16 * QSTRIDE;
            const unsigned qm = qslot + up * 1024;
            SETM0(qm, 0);
            dmaqc<0, 4096 - 128>(vq, qsrd, qrow, qm); NOP0(); dmaqc<128, 8192 - 256>(vq, qsrd, qrow, qm); NOP0(); dmaqc<256, 2048>(vq, qsrd, qrow, qm); NOP0();
            dmaqc<0, 2048 + 4096 - 128>(vq2, qsrd, qrow, qm); NOP0(); dmaqc<128, 2048 + 8192 - 256>(vq2, qsrd, qrow, qm); NOP0(); dmaqe<256>(vq2, qsrd, qrow);
        }
    }
    if (S == 1) ELEMN(false, so0 - TSTEP, mW, SLOT); else ELEM(false, so0 - TSTEP, mW, SLOT);

    const bool act = (S == 2) || pw < L;
    const bool late = (S == 1) && act && tdiag != n - 1;
    const int itend = (S == 1) ? n - 1 : ((n - sig + 1) >> 1) - 1;
    const unsigned kb0 = r32 * 128 + 16 * (h ^ ((r32 >> 1) & 7));
    constexpr unsigned KB = (S == 1) ? SLOT : 0;
    unsigned kA = ring + KB + kb0, kBb = kA ^ 32, kC = kA ^ 64, kD = kA ^ 96;
    const int q4 = (lane & 15) >> 2, p4 = lane & 3, blk = (lane >> 4) & 1;
    unsigned vA = ring + (4 * h + q4) * 128 + 64 * (q4 >> 1) + 32 * blk + 8 * p4, vB = vA ^ 64;
    f32x16 o[6];
#pragma unroll
    for (int c = 0; c < 6; ++c) { o[c] = f32x16{}; asm volatile("" : "+v"(o[c])); }
    float m = -1e30f, l = 0.f, alpha = 1.f, mthr, mc;
    int pend = 0;
    int dq0 = pw + r32 - 4 * h;
    asm volatile("" : "+v"(kA), "+v"(kBb), "+v"(kC), "+v"(kD), "+v"(vA), "+v"(vB), "+v"(dq0));
    f32x16 s;
    float x[16];
#define KRD(i, OFF) ldk((((i) & 3) == 0 ? kA : ((i) & 3) == 1 ? kBb : ((i) & 3) == 2 ? kC : kD) + (OFF) + ((i) >> 2) * 4096)
#define VRD(i, OFF) ldv(((((i) % 6) & 1) ? vB : vA) + (OFF) + (((i) % 6) >> 1) * 4096 + ((i) / 6) * 2048)

    wait_vm<PW>();
    STAMP(4); PH(4);
    barrier();
    bf16x8 q[12];
    {
        const unsigned a0 = qslot + kb0;
#pragma unroll
        for (int i = 0; i < 12; ++i) q[i] = ldk((a0 ^ (32 * (i & 3))) + (i >> 2) * 4096);
    }
    bf16x8 kf[12];
    {
        const unsigned a0 = ring + kb0;
#pragma unroll
        for (int i = 0; i < 6; ++i) kf[i] = ldk((a0 ^ (32 * (i & 3))) + (i >> 2) * 4096);
    }
    barrier();
    ELEMN(true, so0, mW, 2 * SLOT);
    if (S == 1) {
        ELEM(false, so0 - 2 * TSTEP, mW, 3 * SLOT);
        ELEMN(true, so0 - TSTEP, mW, 4 * SLOT);
        ELEM(false, so0 - 3 * TSTEP, mW, 5 * SLOT);
    }
    STAMP(5);

#define GATE(mb, MO) do { __builtin_amdgcn_s_waitcnt(((NS - 2) * PW) | 0x70);                                                            \
        asm volatile("s_barrier\n\ts_add_u32 m0, %0, %1" :: "s"(mb), "i"(MO) : "memory", "scc"); } while (0)
#define PIN1(a) asm volatile("" : "+v"(a))
#define PIN2(a, b) asm volatile("" : "+v"(a), "+v"(b))
#define PIN4(a, b, c, d) asm volatile("" : "+v"(a), "+v"(b), "+v"(c), "+v"(d))
    auto mask_diag = [&]() {
        int dq = dq0 - 32 * tdiag;
        asm volatile("" : "+v"(dq));
#pragma unroll
        for (int i = 0; i < 16; ++i) { const int c = (i & 3) + 8 * (i >> 2); s[i] = (c <= dq) ? s[i] : -__builtin_inff(); }
    };

    if (act && !late) {
        const unsigned a0 = ring + kb0;
#pragma unroll
        for (int i = 6; i < 12; ++i) kf[i] = ldk((a0 ^ (32 * (i & 3))) + (i >> 2) * 4096);
        s = MFMA(kf[0], q[0], f32x16{});
#pragma unroll
        for (int i = 1; i < 12; ++i) s = MFMA(kf[i], q[i], s);
    }
    GATE(mW, 0);
    constexpr unsigned KS0 = (S == 1) ? 0 : SLOT;
    kf[0] = KRD(0, KS0);
    PIECE(S == 1, 0, so0 - 2 * TSTEP, mW, 0); kf[1] = KRD(1, KS0);
    PIECE(S == 1, 1, so0 - 2 * TSTEP, mW, 0); kf[2] = KRD(2, KS0);
    PIECE(S == 1, 2, so0 - 2 * TSTEP, mW, 0); kf[3] = KRD(3, KS0);
    if (S == 2) PIECE(false, 3, so0 - 2 * TSTEP, mW, 0);
    kf[4] = KRD(4, KS0);
    if (S == 2) { PIECE(false, 4, so0 - 2 * TSTEP, mW, 0); NOP0(); PIECE(false, 5, so0 - 2 * TSTEP, mW, 0); }
    auto first_max = [&]() {
        float a = max3f(s[0], s[1], s[2]), b = max3f(s[8], s[9], s[10]);
        a = max3f(a, s[3], s[4]); b = max3f(b, s[11], s[12]);
        a = max3f(a, s[5], s[6]); b = max3f(b, s[13], s[14]);
        a = max3f(a, s[7], s[15]);
        m = max2f(m, swap_max(max2f(a, b)));
        mthr = m + THR_RAW; mc = -m * SCALE_LOG2E;
#pragma unroll
        for (int i = 0; i < 16; ++i) x[i] = fmaf(s[i], SCALE_LOG2E, mc);
    };
    mthr = m; mc = 0.f;
    if (act && !late) {
        if (S == 1 || sig == 0) mask_diag();
        first_max();
    }
    STAMP(6); PH(6);

    int it = 0;
    unsigned soA = so0 - ((S == 1) ? 4 : 1) * TSTEP, soB = so0 - 3 * TSTEP;
    u32x4 pa, pb;
#define EXP2X(i, j) do { x[i] = __builtin_amdgcn_exp2f(x[i]); x[j] = __builtin_amdgcn_exp2f(x[j]); PIN2(x[i], x[j]); } while (0)
#define FMAX2(i, j) do { x[i] = fmaf(s[i], SCALE_LOG2E, mc); x[j] = fmaf(s[j], SCALE_LOG2E, mc); PIN2(x[i], x[j]); } while (0)
    auto iter = [&](auto PHC) {
        constexpr int PH = decltype(PHC)::value, PN = (PH + 1) % 3;
        constexpr unsigned SK = ((S == 1) ? 1 + 2 * PH : (PH == 0 ? 1 : PH == 1 ? 0 : 2)) * SLOT;
        constexpr unsigned SV = ((S == 1) ? (2 + 2 * PH) % 6 : 2 - PH) * SLOT;
        constexpr unsigned SKN = ((S == 1) ? 1 + 2 * PN : (PN == 0 ? 1 : PN == 1 ? 0 : 2)) * SLOT;
        constexpr unsigned RK = SK - KB, RKN = SKN - KB;
        float ls0, ls1;
        PIN2(kf[0], kf[1]);
        kf[5] = KRD(5, RK);
        s = MFMA(kf[0], q[0], f32x16{}); EXP2X(0, 1); SB();
        kf[6] = KRD(6, RK);
        s = MFMA(kf[1], q[1], s); EXP2X(2, 3); SB();
        PIN2(kf[2], kf[3]);
        kf[7] = KRD(7, RK);
        s = MFMA(kf[2], q[2], s); EXP2X(4, 5); SB();
        kf[8] = KRD(8, RK);
        s = MFMA(kf[3], q[3], s); EXP2X(6, 7); SB();
        PIN2(kf[4], kf[5]);
        kf[9] = KRD(9, RK);
        s = MFMA(kf[4], q[4], s); EXP2X(8, 9); ls0 = x[0] + x[2]; ls1 = x[1] + x[3]; PIN2(ls0, ls1); SB();
        kf[10] = KRD(10, RK); kf[11] = KRD(11, RK);
        s = MFMA(kf[5], q[5], s); EXP2X(10, 11); ls0 += x[4]; ls1 += x[5]; PIN2(ls0, ls1); SB();
        PIN2(kf[6], kf[7]);
        s = MFMA(kf[6], q[6], s); EXP2X(12, 13); pa[0] = cvtpk(x[0], x[1]); pa[1] = cvtpk(x[2], x[3]); PIN2(pa[0], pa[1]); SB();
        s = MFMA(kf[7], q[7], s); EXP2X(14, 15); pa[2] = cvtpk(x[4], x[5]); pa[3] = cvtpk(x[6], x[7]); PIN2(pa[2], pa[3]); SB();
        GATE(mW, SK);
        bf16x8 vf[12];
        vf[0] = VRD(0, SV); SB();
        s = MFMA(kf[8], q[8], s); vf[1] = VRD(1, SV); ls0 += x[6]; ls1 += x[7]; PIN2(ls0, ls1); SB();
        s = MFMA(kf[9], q[9], s); vf[2] = VRD(2, SV); PIECE(S == 2, 0, soA, mW, SK); ls0 += x[8]; ls1 += x[9]; PIN2(ls0, ls1); SB();
        s = MFMA(kf[10], q[10], s); vf[3] = VRD(3, SV); PIECE(S == 2, 1, soA, mW, SK); ls0 += x[10]; ls1 += x[11]; PIN2(ls0, ls1); SB();
        s = MFMA(kf[11], q[11], s); vf[4] = VRD(4, SV); PIECE(S == 2, 2, soA, mW, SK); ls0 += x[12]; ls1 += x[13]; PIN2(ls0, ls1); SB();
        const bf16x8 p0f = __builtin_bit_cast(bf16x8, pa);
        PIN2(vf[0], vf[1]);
        vf[5] = VRD(5, SV);
        o[0] = MFMA(vf[0], p0f, o[0]); if (S == 2) PIECE(true, 3, soA, mW, SK);
        pb[0] = cvtpk(x[8], x[9]); pb[1] = cvtpk(x[10], x[11]); PIN2(pb[0], pb[1]); ls0 += x[14]; ls1 += x[15]; PIN2(ls0, ls1); SB();
        vf[6] = VRD(6, SV);
        o[1] = MFMA(vf[1], p0f, o[1]); if (S == 2) PIECE(true, 4, soA, mW, SK);
        pb[2] = cvtpk(x[12], x[13]); pb[3] = cvtpk(x[14], x[15]); PIN2(pb[2], pb[3]); l += ls0; PIN1(l); SB();
        PIN2(vf[2], vf[3]);
        vf[7] = VRD(7, SV);
        o[2] = MFMA(vf[2], p0f, o[2]); if (S == 2) PIECE(true, 5, soA, mW, SK);
        l += ls1; PIN1(l); SB();
        const bf16x8 p1f = __builtin_bit_cast(bf16x8, pb);
        vf[8] = VRD(8, SV);
        o[3] = MFMA(vf[3], p0f, o[3]); SB();
        float ma = max3f(s[0], s[1], s[2]), mb = max3f(s[8], s[9], s[10]);
        ma = max3f(ma, s[3], s[4]); mb = max3f(mb, s[11], s[12]);
        ma = max3f(ma, s[5], s[6]); mb = max3f(mb, s[13], s[14]); PIN2(ma, mb); SB();
        PIN2(vf[4], vf[5]);
        vf[9] = VRD(9, SV);
        o[4] = MFMA(vf[4], p0f, o[4]);
        ma = max3f(ma, s[7], s[15]);
        float pm = swap_max(max2f(ma, mb)); PIN1(pm); SB();
        vf[10] = VRD(10, SV); vf[11] = VRD(11, SV);
        o[5] = MFMA(vf[5], p0f, o[5]); SB();
        if (__builtin_expect(__any(pm > mthr), 0)) {
            const float mn = max2f(m, pm);
            alpha = __builtin_amdgcn_exp2f((m - mn) * SCALE_LOG2E);
            m = mn; mthr = m + THR_RAW; mc = -m * SCALE_LOG2E; pend = 1;
        }
        PIN2(vf[6], vf[7]);
        o[0] = MFMA(vf[6], p1f, o[0]); FMAX2(0, 1); FMAX2(2, 3); FMAX2(4, 5); SB();
        o[1] = MFMA(vf[7], p1f, o[1]); FMAX2(6, 7); FMAX2(8, 9); FMAX2(10, 11); SB();
        GATE(mW, SV);
        kf[0] = KRD(0, RKN); SB();
        o[2] = MFMA(vf[8], p1f, o[2]); kf[1] = KRD(1, RKN); PIECE(S == 1, 0, soB, mW, SV); FMAX2(12, 13); SB();
        o[3] = MFMA(vf[9], p1f, o[3]); kf[2] = KRD(2, RKN); PIECE(S == 1, 1, soB, mW, SV); FMAX2(14, 15); SB();
        o[4] = MFMA(vf[10], p1f, o[4]); PIECE(S == 1, 2, soB, mW, SV); kf[3] = KRD(3, RKN); if (S == 2) PIECE(false, 3, soB, mW, SV); SB();
        o[5] = MFMA(vf[11], p1f, o[5]); if (S == 2) PIECE(false, 4, soB, mW, SV); kf[4] = KRD(4, RKN); if (S == 2) PIECE(false, 5, soB, mW, SV); SB();
        soA -= TSTEP; soB -= TSTEP;
        if (__builtin_expect(pend, 0)) {
            l *= alpha;
#pragma unroll
            for (int c = 0; c < 6; ++c) o[c] *= alpha;
            pend = 0;
        }
        ++it;
    };
    bool skip0 = false;
    if (S == 1 && late) {
        constexpr unsigned SK = SLOT, SV = 2 * SLOT, RKN = 3 * SLOT - KB;
        kf[5] = KRD(5, 0); kf[6] = KRD(6, 0); kf[7] = KRD(7, 0); kf[8] = KRD(8, 0);
        s = MFMA(kf[0], q[0], f32x16{});
        kf[9] = KRD(9, 0); kf[10] = KRD(10, 0); kf[11] = KRD(11, 0);
#pragma unroll
        for (int i = 1; i < 8; ++i) s = MFMA(kf[i], q[i], s);
        GATE(mW, SK);
        s = MFMA(kf[8], q[8], s); SB();
        PIECE(false, 0, soA, mW, SK); s = MFMA(kf[9], q[9], s); SB();
        PIECE(false, 1, soA, mW, SK); s = MFMA(kf[10], q[10], s); SB();
        PIECE(false, 2, soA, mW, SK); s = MFMA(kf[11], q[11], s); SB();
        GATE(mW, SV);
        kf[0] = KRD(0, RKN); PIECE(true, 0, soB, mW, SV); kf[1] = KRD(1, RKN); PIECE(true, 1, soB, mW, SV); kf[2] = KRD(2, RKN); PIECE(true, 2, soB, mW, SV);
        kf[3] = KRD(3, RKN); kf[4] = KRD(4, RKN);
        mask_diag();
        first_max();
        soA -= TSTEP; soB -= TSTEP;
        it = 1; skip0 = true;
    }
    if (act && it < itend) {
        for (;;) {
            if (!skip0) { iter(IC<0>{}); if (it >= itend) break; }
            skip0 = false;
            iter(IC<1>{}); if (it >= itend) break;
            iter(IC<2>{}); if (it >= itend) break;
        }
    }
    STAMP(7); PH(7);
    const int ph = it % 3;
    unsigned tk = ((S == 1) ? 1 + 2 * ph : (ph == 0 ? 1 : ph == 1 ? 0 : 2)) * SLOT;
    unsigned tv = ((S == 1) ? (ph == 2 ? 0 : 2 + 2 * ph) : 2 - ph) * SLOT;
    auto adv2 = [&](unsigned a) { const unsigned t = a + 2 * SLOT; return t >= NS * SLOT ? t - NS * SLOT : t; };
    if (act) {
        const unsigned mk = mW + tk, mv = mW + tv;
        GATE(mk, 0);
        const unsigned va = vA + tv, vb = vB + tv;
        bf16x8 vf[12];
#define VRT(i) ldv(((((i) % 6) & 1) ? vb : va) + (((i) % 6) >> 1) * 4096 + ((i) / 6) * 2048)
        vf[0] = VRT(0); PIECE(S == 2, 0, soA, mk, 0); vf[1] = VRT(1); PIECE(S == 2, 1, soA, mk, 0); vf[2] = VRT(2); PIECE(S == 2, 2, soA, mk, 0);
        vf[3] = VRT(3); if (S == 2) PIECE(true, 3, soA, mk, 0); vf[4] = VRT(4); if (S == 2) PIECE(true, 4, soA, mk, 0); vf[5] = VRT(5); if (S == 2) PIECE(true, 5, soA, mk, 0);
        float ls0 = 0.f, ls1 = 0.f;
#pragma unroll
        for (int i = 0; i < 16; i += 2) { x[i] = __builtin_amdgcn_exp2f(x[i]); x[i + 1] = __builtin_amdgcn_exp2f(x[i + 1]); ls0 += x[i]; ls1 += x[i + 1]; }
        l += ls0 + ls1;
#pragma unroll
        for (int i = 0; i < 4; ++i) { pa[i] = cvtpk(x[2 * i], x[2 * i + 1]); pb[i] = cvtpk(x[8 + 2 * i], x[9 + 2 * i]); }
        PIN4(pa[0], pa[1], pa[2], pa[3]); PIN4(pb[0], pb[1], pb[2], pb[3]);
        const bf16x8 p0f = __builtin_bit_cast(bf16x8, pa), p1f = __builtin_bit_cast(bf16x8, pb);
        SB();
        vf[6] = VRT(6); vf[7] = VRT(7);
        o[0] = MFMA(vf[0], p0f, o[0]); SB();
        vf[8] = VRT(8); vf[9] = VRT(9);
        o[1] = MFMA(vf[1], p0f, o[1]); SB();
        vf[10] = VRT(10); vf[11] = VRT(11);
        o[2] = MFMA(vf[2], p0f, o[2]); SB();
        o[3] = MFMA(vf[3], p0f, o[3]); o[4] = MFMA(vf[4], p0f, o[4]); o[5] = MFMA(vf[5], p0f, o[5]);
        o[0] = MFMA(vf[6], p1f, o[0]); o[1] = MFMA(vf[7], p1f, o[1]); SB();
        if (it + 1 < steps) {
            GATE(mv, 0);
            NOP0();
            PIECE(S == 1, 0, soB, mv, 0); NOP0(); PIECE(S == 1, 1, soB, mv, 0); NOP0(); PIECE(S == 1, 2, soB, mv, 0);
            if (S == 2) { NOP0(); PIECE(false, 3, soB, mv, 0); NOP0(); PIECE(false, 4, soB, mv, 0); NOP0(); PIECE(false, 5, soB, mv, 0); }
        }
        o[2] = MFMA(vf[8], p1f, o[2]); o[3] = MFMA(vf[9], p1f, o[3]); o[4] = MFMA(vf[10], p1f, o[4]); o[5] = MFMA(vf[11], p1f, o[5]);
        tk = adv2(tk); tv = adv2(tv); soA -= TSTEP; soB -= TSTEP;
        ++it;
    }
    STAMP(8);
    for (; it < steps; ++it) {
        const unsigned mk = mW + tk, mv = mW + tv;
        GATE(mk, 0);
        NOP0();
        PIECE(S == 2, 0, soA, mk, 0); NOP0(); PIECE(S == 2, 1, soA, mk, 0); NOP0(); PIECE(S == 2, 2, soA, mk, 0);
        if (S == 2) { NOP0(); PIECE(true, 3, soA, mk, 0); NOP0(); PIECE(true, 4, soA, mk, 0); NOP0(); PIECE(true, 5, soA, mk, 0); }
        if (it + 1 < steps) {
            GATE(mv, 0);
            NOP0();
            PIECE(S == 1, 0, soB, mv, 0); NOP0(); PIECE(S == 1, 1, soB, mv, 0); NOP0(); PIECE(S == 1, 2, soB, mv, 0);
            if (S == 2) { NOP0(); PIECE(false, 3, soB, mv, 0); NOP0(); PIECE(false, 4, soB, mv, 0); NOP0(); PIECE(false, 5, soB, mv, 0); }
        }
        tk = adv2(tk); tv = adv2(tv); soA -= TSTEP; soB -= TSTEP;
    }

    STAMP(10); PH(10);
    l = swap_add(l);
    const __amdgpu_buffer_rsrc_t orsrc = __builtin_amdgcn_make_buffer_rsrc((void*)(op + (size_t)s0 * QSTRIDE), 0, (unsigned)L * QSTRIDE, 0x00020000);
    const unsigned stw = r32 * ORS + 8 * h;
    auto stage_block = [&](unsigned base, int c, const f32x16& z) {
#pragma unroll
        for (int g = 0; g < 4; ++g) {
            u32x2 v = {cvtpk(z[4 * g], z[4 * g + 1]), cvtpk(z[4 * g + 2], z[4 * g + 3])};
            *(LDSP(u32x2))(size_t)(base + (32 * c + 8 * g) * 2) = v;
        }
    };
    unsigned lrd[3], gof[3];
#pragma unroll
    for (int j = 0; j < 3; ++j) { const unsigned idx = 64 * j + lane, row = (idx * 2731) >> 16, ch = idx - 24 * row; lrd[j] = row * ORS + ch * 16; gof[j] = row * QSTRIDE + ch * 16; }
    wait_vm<0>();
    barrier();
    STAMP(11);
    if (S == 1) {
        if (act) {
            const unsigned stg = lds0 + w * (32 * ORS);
            const float inv = __builtin_amdgcn_rcpf(l);
#pragma unroll
            for (int c = 0; c < 6; ++c) { const f32x16 z = o[c] * inv; stage_block(stg + stw, c, z); }
            STAMP(15);
            const unsigned obase = (unsigned)pw * QSTRIDE + (unsigned)(2 * grp + hh) * ROWB;
#pragma unroll
            for (int k = 0; k < 4; ++k)
#pragma unroll
                for (int j = 0; j < 3; ++j) {
                    const u32x4 v = *(LDSP(u32x4))(size_t)(stg + lrd[j] + 8 * k * ORS);
                    __builtin_amdgcn_raw_buffer_store_b128(v, orsrc, gof[j], obase + 8 * k * QSTRIDE, STAUX);
                }
        }
    } else {
        const unsigned mine = lds0 + w * SLOT + lane * 16, other = lds0 + (w ^ 2) * SLOT + lane * 16;
        {
            f32x2 ml = {m, l};
            *(LDSP(f32x2))(size_t)(lds0 + 4 * SLOT + w * 512 + lane * 8) = ml;
        }
        auto send = [&](const f32x16& z, int c) {
#pragma unroll
            for (int g = 0; g < 4; ++g) { f32x4 y = {z[4 * g], z[4 * g + 1], z[4 * g + 2], z[4 * g + 3]}; *(LDSP(f32x4))(size_t)(mine + (c * 4 + g) * 1024) = y; }
        };
        if (sig) { send(o[0], 0); send(o[1], 1); send(o[2], 2); } else { send(o[3], 0); send(o[4], 1); send(o[5], 2); }
        STAMP(12);
        barrier();
        STAMP(13);
        const f32x2 mlo = *(LDSP(f32x2))(size_t)(lds0 + 4 * SLOT + (w ^ 2) * 512 + lane * 8);
        const float mx = max2f(m, mlo[0]);
        const float a0 = __builtin_amdgcn_exp2f((m - mx) * SCALE_LOG2E), a1 = __builtin_amdgcn_exp2f((mlo[0] - mx) * SCALE_LOG2E);
        const float inv = __builtin_amdgcn_rcpf(l * a0 + mlo[1] * a1);
        const float f0 = a0 * inv, f1 = a1 * inv;
        const unsigned stg = lds0 + 51200 + hh * (32 * ORS);
        auto merge = [&](const f32x16& xo, int c, int cd) {
            f32x16 z;
#pragma unroll
            for (int g = 0; g < 4; ++g) {
                const f32x4 y = *(LDSP(f32x4))(size_t)(other + (c * 4 + g) * 1024);
                const f32x4 t = {xo[4 * g], xo[4 * g + 1], xo[4 * g + 2], xo[4 * g + 3]};
                const f32x4 u = t * f0 + y * f1;
                z[4 * g] = u[0]; z[4 * g + 1] = u[1]; z[4 * g + 2] = u[2]; z[4 * g + 3] = u[3];
            }
            stage_block(stg + stw, cd, z);
        };
        if (sig) { merge(o[3], 0, 3); merge(o[4], 1, 4); merge(o[5], 2, 5); } else { merge(o[0], 0, 0); merge(o[1], 1, 1); merge(o[2], 2, 2); }
        STAMP(14);
        barrier();
        STAMP(15);
        const unsigned obase = (unsigned)(pw + 16 * sig) * QSTRIDE + (unsigned)(2 * grp + hh) * ROWB;
        const unsigned stgr = stg + 16 * sig * ORS;
#pragma unroll
        for (int k = 0; k < 2; ++k)
#pragma unroll
            for (int j = 0; j < 3; ++j) {
                const u32x4 v = *(LDSP(u32x4))(size_t)(stgr + lrd[j] + 8 * k * ORS);
                __builtin_amdgcn_raw_buffer_store_b128(v, orsrc, gof[j], obase + 8 * k * QSTRIDE, STAUX);
            }
    }
    STAMP(16); PH(16);
}

template <int CTRL> DI int dpp_mov(int old, int x) { return __builtin_amdgcn_update_dpp(old, x, CTRL, 0xf, 0xf, false); }
DI int wave_incl_scan(int x) {
    x += dpp_mov<0x111>(0, x);
    x += dpp_mov<0x112>(0, x);
    x += dpp_mov<0x114>(0, x);
    x += dpp_mov<0x118>(0, x);
    x += __builtin_amdgcn_update_dpp(0, x, 0x142, 0xa, 0xf, false);
    x += __builtin_amdgcn_update_dpp(0, x, 0x143, 0xc, 0xf, false);
    return x;
}
DI int wave_max_nonneg(int x) {
    x = max(x, dpp_mov<0x111>(0, x));
    x = max(x, dpp_mov<0x112>(0, x));
    x = max(x, dpp_mov<0x114>(0, x));
    x = max(x, dpp_mov<0x118>(0, x));
    x = max(x, __builtin_amdgcn_update_dpp(0, x, 0x142, 0xa, 0xf, false));
    x = max(x, __builtin_amdgcn_update_dpp(0, x, 0x143, 0xc, 0xf, false));
    return __builtin_amdgcn_readlane(x, 63);
}

DI bool decode_slow(const int* __restrict__ cu, int nseq, int k, int& s0, int& L, int& p0, int& isx) {
    int nmax = 0;
    for (int s = 0; s < nseq; ++s) nmax = max(nmax, (cu[s + 1] - cu[s] + 31) >> 5);
    const int B = BSPLIT;
    int N = 0;
    for (int s = 0; s < nseq; ++s) { const int n = (cu[s + 1] - cu[s] + 31) >> 5; N += max(n - B, 0) + min(B >> 1, (n + 1) >> 1); }
    int rank = k;
    if (k < CUS_PER_XCD) { const int N2 = min(max(N - CUS_PER_XCD, 0), (PFMODE != 0) ? PF0 - CUS_PER_XCD : CUS_PER_XCD); rank = (k < N2) ? CUS_PER_XCD - 1 - k : k - N2; }
    if (rank >= N) return false;
    int jx = nmax, my = min(B >> 1, (nmax + 1) >> 1);
    for (;;) {
        const int x = (jx > B) && (((jx + 1) >> 1) >= 2 * my);
        const int thr = x ? 32 * (jx - 1) : 64 * (my - 1);
        for (int s = 0; s < nseq; ++s) {
            const int len = cu[s + 1] - cu[s];
            if (len > thr) { if (rank == 0) { s0 = cu[s]; L = len; p0 = thr; isx = x; return true; } --rank; }
        }
        if (x) --jx; else --my;
    }
}

__global__ __launch_bounds__(256, 2) void attn_varlen_kernel(const char* __restrict__ qp, const char* __restrict__ kp, const char* __restrict__ vp,
                                                             const int* __restrict__ cu, char* __restrict__ op, int nseq, int T
                                                             ) {
    __shared__ __attribute__((aligned(16))) char smem_[LDS_BYTES];
    const unsigned long long rt_start = __builtin_amdgcn_s_memrealtime();
    unsigned g_tphase = (unsigned)rt_start;
    const int grp = blockIdx.x & 7;
    const int kraw = blockIdx.x >> 3;
    const int lane = threadIdx.x & 63;

    asm volatile("" :: "s"(qp), "s"(kp), "s"(vp), "s"(op), "s"(cu), "s"(nseq));
    if (CODEPF > 0 && kraw < CODEWG) {
        unsigned long long pc; asm volatile("s_getpc_b64 %0" : "=s"(pc));
        const char* cb = (const char*)(pc & ~127ull);
        const unsigned mscr0 = rfl((unsigned)(size_t)(lds_char_ptr)smem_ + 73728 + 1024 + (threadIdx.x >> 6) * 256);
        asm volatile("s_mov_b32 m0, %0" :: "s"(mscr0) : "memory");
        for (unsigned o = threadIdx.x * 128; o < CODEPF; o += 256 * 128)
            asm volatile("global_load_lds_dword %0, off" :: "v"(cb + o) : "memory");
    }
    if (PFMODE != 0 && kraw >= PF0 && kraw < PF0 + 8) {
        const int wv = rfl(threadIdx.x >> 6);
        const unsigned mscr = rfl((unsigned)(size_t)(lds_char_ptr)smem_ + wv * 256);
        asm volatile("s_mov_b32 m0, %0" :: "s"(mscr) : "memory");
        const unsigned j = (kraw - PF0) * 256 + threadIdx.x;
        const u32x4 ro = make_srd(op, (unsigned)T * QSTRIDE), rq = make_srd(qp, (unsigned)T * QSTRIDE);
        const u32x4 rk = make_srd(kp, (unsigned)T * KSTRIDE), rv = make_srd(vp, (unsigned)T * KSTRIDE);
        asm volatile("s_nop 4" :: "s"(ro), "s"(rq), "s"(rk), "s"(rv) : "memory");
        for (unsigned off = j * PSTRIDE; off < (unsigned)T * QSTRIDE; off += 2048 * PSTRIDE) {
            if (PFMODE & 1) asm volatile("buffer_load_dword %0, %1, 0 offen nt lds" :: "v"(off), "s"(ro) : "memory");
            if (PFMODE & 2) {
                asm volatile("buffer_load_dword %0, %1, 0 offen nt lds" :: "v"(off), "s"(rq) : "memory");
                asm volatile("buffer_load_dword %0, %1, 0 offen nt lds" :: "v"(off), "s"(rk) : "memory");
                asm volatile("buffer_load_dword %0, %1, 0 offen nt lds" :: "v"(off), "s"(rv) : "memory");
            }
        }
        return;
    }
    const int k = (PFMODE != 0 && kraw >= PF0 + 8) ? kraw - 8 : kraw;
    typedef unsigned u32x16 __attribute__((ext_vector_type(16)));
    u32x16 cus;
    {
        const u32x4 cr = make_srd(cu, (unsigned)(nseq + 1) * 4);
        asm volatile("s_buffer_load_dwordx16 %0, %1, 0" : "=s"(cus) : "s"(cr) : "memory");
    }
    const int l3 = (lane * 43) >> 7, r3 = lane - 3 * l3, st = l3 + 1;
    const int jx = 2 * st - 1 + r3;
    const bool okx = (r3 < 2) & (jx > BSPLIT) & (lane < 63), oky = (r3 == 2) & !(st & 1) & (st <= BSPLIT);
    int thr = oky ? 32 * (st - 2) : 0x7fffffff;
    thr = okx ? 32 * (jx - 1) : thr;
    asm volatile("s_waitcnt lgkmcnt(0)" : "+s"(cus) :: "memory");
    PH(9);
    int len = 0, c0 = 0, cnt = 0;
#define SEQ(s) do { const int ls = (int)cus[(s) + 1] - (int)cus[s];                         \
        cnt += (ls > thr) ? 1 : 0;                                                                                                      \
        asm("v_writelane_b32 %0, %1, %2" : "+v"(len) : "s"(ls), "i"(s)); asm("v_writelane_b32 %0, %1, %2" : "+v"(c0) : "s"(cus[s]), "i"(s)); } while (0)
    SEQ(0); SEQ(1); SEQ(2); SEQ(3); SEQ(4); SEQ(5); SEQ(6); SEQ(7);
    if (__builtin_expect(nseq > 8, 0)) { SEQ(8); SEQ(9); SEQ(10); SEQ(11); SEQ(12); SEQ(13); SEQ(14); }
    const int incl = wave_incl_scan(cnt);
    const int N = __builtin_amdgcn_readlane(incl, 63);
    const int N2 = min(max(N - CUS_PER_XCD, 0), (PFMODE != 0) ? PF0 - CUS_PER_XCD : CUS_PER_XCD);
    (void)N2;
    const int rank = (k >= CUS_PER_XCD && N <= ((PFMODE != 0) ? PF0 : 2 * CUS_PER_XCD)) ? (k < N ? N - 1 - (k - CUS_PER_XCD) : N) : k;
    bool valid = rank < N;
    const int a = N - rank;
    const int lv = (__ffsll((long long)__ballot(incl >= a)) - 1) & 63;
    const int idx = __builtin_amdgcn_readlane(incl, lv) - a;
    int p0 = __builtin_amdgcn_readlane(thr, lv);
    int isx = __builtin_amdgcn_readlane(r3, lv) < 2;
    const bool has = len > p0;
    const unsigned long long mk = __ballot(has);
    const int below = __builtin_amdgcn_mbcnt_hi((unsigned)(mk >> 32), __builtin_amdgcn_mbcnt_lo((unsigned)mk, 0));
    const int sq = (__ffsll((long long)__ballot(has && below == idx)) - 1) & 63;
    int s0 = __builtin_amdgcn_readlane(c0, sq), L = __builtin_amdgcn_readlane(len, sq);
    if (__builtin_expect(nseq > 15 || __ballot(len > 32 * 42) != 0, 0)) {
        int a0 = 0, a1 = 0, a2 = 0, a3 = 0;
        const int v = decode_slow(cu, nseq, k, a0, a1, a2, a3);
        valid = rfl(v) != 0; s0 = rfl(a0); L = rfl(a1); p0 = rfl(a2); isx = rfl(a3);
    }
    if (!valid) return;
    s0 = rfl(s0); L = rfl(L); p0 = rfl(p0); isx = rfl(isx);
    if (PRIO && k < CUS_PER_XCD) __builtin_amdgcn_s_setprio(PRIO);
    const unsigned lds0 = (unsigned)(size_t)(lds_char_ptr)smem_;
    if (isx) {
        const int n = (min(p0 + 32, L) + 31) >> 5;
        attn_item<2>(qp, kp, vp, op, grp, s0, L, p0, n, lds0 STPASS PHPASS);
    } else {
        const int n = (min(p0 + 64, L) + 31) >> 5;
        attn_item<1>(qp, kp, vp, op, grp, s0, L, p0, n, lds0 STPASS PHPASS);
    }
    if (blockIdx.x == DIAG_BLK) { const unsigned d = g_tphase - (unsigned)rt_start; while (__builtin_amdgcn_s_memrealtime() - rt_start < 3000ull + d) __builtin_amdgcn_s_sleep(1); }
}

extern "C" void kernel_launch(void* const* d_in, const int* in_sizes, int n_in, void* d_out, int out_size, void* d_ws, size_t ws_size,
                              hipStream_t stream) {
    (void)n_in; (void)out_size; (void)d_ws; (void)ws_size;
    const int T = in_sizes[0] / (HQ * D);
    const int nseq = in_sizes[3] - 1;
    const int per_head = T / 32 + nseq + 1 + 8;
    attn_varlen_kernel<<<dim3(8 * per_head), dim3(256), 0, stream>>>((const char*)d_in[0], (const char*)d_in[1], (const char*)d_in[2],
                                                                    (const int*)d_in[3], (char*)d_out, nseq, T
                                                                    );
}
```

```cpp
#define DIAG_ABSRT 3000ull
#include <hip/hip_runtime.h>
#include <hip/hip_bf16.h>
#include <stdint.h>

typedef __bf16 bf16x8 __attribute__((ext_vector_type(8)));
typedef __bf16 bf16x2 __attribute__((ext_vector_type(2)));
typedef short s16x4 __attribute__((ext_vector_type(4)));
typedef short s16x8 __attribute__((ext_vector_type(8)));
typedef float f32x2 __attribute__((ext_vector_type(2)));
typedef float f32x4 __attribute__((ext_vector_type(4)));
typedef float f32x16 __attribute__((ext_vector_type(16)));
typedef unsigned u32x2 __attribute__((ext_vector_type(2)));
typedef unsigned u32x4 __attribute__((ext_vector_type(4)));
#define LDSP(T) __attribute__((address_space(3))) T*
typedef LDSP(char) lds_char_ptr;

#define DI __device__ __forceinline__
#define LDPOL ""
#define FTPOL ""
#define PSTRIDE 4096
#define PAIRMODE 1
#define PRIO 2
#define QPOL ""
#define STAUX 16
#define PFPOL ""
#define PFMODE 0
#define CODEPF 0
#define CODEWG 2
#define STAMP(i) do {} while (0)
#define STARG
#define STPASS

constexpr int HQ = 16, HKV = 8, D = 192;
constexpr unsigned QSTRIDE = HQ * D * 2;
constexpr unsigned KSTRIDE = HKV * D * 2;
constexpr unsigned ROWB = D * 2;
constexpr unsigned SLOT = 32 * ROWB;
constexpr unsigned ORS = 400;
constexpr unsigned LDS_BYTES = 77824;
constexpr float SCALE_LOG2E = 0.07216878364870322f * 1.4426950408889634f;
constexpr float THR_RAW = 8.0f / SCALE_LOG2E;
constexpr int CUS_PER_XCD = 32;
constexpr int PF0 = 56;
constexpr int BSPLIT = 8;

DI int rfl(int x) { return __builtin_amdgcn_readfirstlane(x); }
DI unsigned cvtpk(float lo, float hi) {
    f32x2 v = {lo, hi}; bf16x2 b = __builtin_convertvector(v, bf16x2); return __builtin_bit_cast(unsigned, b);
}
DI float max2f(float a, float b) { return __builtin_elementwise_maximum(a, b); }
DI float max3f(float a, float b, float c) { return max2f(max2f(a, b), c); }
DI float swap_max(float x) {
    auto r = __builtin_amdgcn_permlane32_swap(__float_as_uint(x), __float_as_uint(x), false, false);
    return max2f(__uint_as_float(r[0]), __uint_as_float(r[1]));
}
DI float swap_add(float x) {
    auto r = __builtin_amdgcn_permlane32_swap(__float_as_uint(x), __float_as_uint(x), false, false);
    return __uint_as_float(r[0]) + __uint_as_float(r[1]);
}
DI u32x4 make_srd(const void* base, unsigned nbytes) {
    unsigned long long a = (unsigned long long)base;
    u32x4 s; s[0] = rfl((unsigned)a); s[1] = rfl((unsigned)(a >> 32) & 0xffffu); s[2] = rfl(nbytes); s[3] = 0x00020000u;
    return s;
}
template <int IMM> DI void dma16(unsigned m0v, unsigned voff, u32x4 srd, unsigned soff) {
    asm volatile("s_mov_b32 m0, %0\n\ts_nop 0\n\tbuffer_load_dwordx4 %1, %2, %3 offen offset:%4 lds"
                 :: "s"(m0v), "v"(voff), "s"(srd), "s"(soff), "i"(IMM) : "memory");
}
template <int IMM, int NEXT> DI void dma16c_nt(unsigned voff, u32x4 srd, unsigned soff, unsigned nbase) {
    asm volatile("buffer_load_dwordx4 %0, %1, %2 offen offset:%3 " FTPOL " lds\n\ts_add_u32 m0, %4, %5"
                 :: "v"(voff), "s"(srd), "s"(soff), "i"(IMM), "s"(nbase), "i"(NEXT) : "memory", "scc");
}
template <int IMM> DI void dma16e_nt(unsigned voff, u32x4 srd, unsigned soff) {
    asm volatile("buffer_load_dwordx4 %0, %1, %2 offen offset:%3 " FTPOL " lds" :: "v"(voff), "s"(srd), "s"(soff), "i"(IMM) : "memory");
}
template <int IMM, int NEXT> DI void dma16c(unsigned voff, u32x4 srd, unsigned soff, unsigned nbase) {
    asm volatile("buffer_load_dwordx4 %0, %1, %2 offen offset:%3 " LDPOL " lds\n\ts_add_u32 m0, %4, %5"
                 :: "v"(voff), "s"(srd), "s"(soff), "i"(IMM), "s"(nbase), "i"(NEXT) : "memory", "scc");
}
template <int IMM, int NEXT> DI void dmaqc(unsigned voff, u32x4 srd, unsigned soff, unsigned nbase) {
    asm volatile("buffer_load_dwordx4 %0, %1, %2 offen offset:%3 " QPOL " lds\n\ts_add_u32 m0, %4, %5"
                 :: "v"(voff), "s"(srd), "s"(soff), "i"(IMM), "s"(nbase), "i"(NEXT) : "memory", "scc");
}
template <int IMM> DI void dmaqe(unsigned voff, u32x4 srd, unsigned soff) {
    asm volatile("buffer_load_dwordx4 %0, %1, %2 offen offset:%3 " QPOL " lds" :: "v"(voff), "s"(srd), "s"(soff), "i"(IMM) : "memory");
}
template <int IMM> DI void dma16e(unsigned voff, u32x4 srd, unsigned soff) {
    asm volatile("buffer_load_dwordx4 %0, %1, %2 offen offset:%3 " LDPOL " lds" :: "v"(voff), "s"(srd), "s"(soff), "i"(IMM) : "memory");
}
template <int N> DI void wait_vm() { asm volatile("s_waitcnt vmcnt(%0)" :: "i"(N) : "memory"); }
DI void barrier() { asm volatile("s_waitcnt lgkmcnt(0)\n\ts_barrier" ::: "memory"); }
#define SB() __builtin_amdgcn_sched_barrier(0)
#define MFMA(a, b, c) __builtin_amdgcn_mfma_f32_32x32x16_bf16((a), (b), (c), 0, 0, 0)

DI bf16x8 ldk(unsigned addr) { return *(LDSP(bf16x8))(size_t)addr; }
DI bf16x8 ldv(unsigned addr) {
    const s16x4 lo = __builtin_amdgcn_ds_read_tr16_b64_v4i16((LDSP(s16x4))(size_t)addr);
    const s16x4 hi = __builtin_amdgcn_ds_read_tr16_b64_v4i16((LDSP(s16x4))(size_t)(addr + 1024));
    return __builtin_bit_cast(bf16x8, __builtin_shufflevector(lo, hi, 0, 1, 2, 3, 4, 5, 6, 7));
}

template <int V> struct IC { static constexpr int value = V; };

template <int S>
DI void attn_item(const char* __restrict__ qp, const char* __restrict__ kp, const char* __restrict__ vp, char* __restrict__ op,
                  int grp, int s0, int L, int p0, int n, unsigned lds0 STARG) {
    constexpr int NS = (S == 1) ? 6 : 3;
    constexpr int PW = (S == 1) ? 3 : 6;
    constexpr unsigned TSTEP = 32 * S * KSTRIDE;
    const int tid = threadIdx.x, lane = tid & 63;
    const int w = rfl(tid >> 6);
    const int hh = w & 1, up = w >> 1;
    const int r32 = lane & 31, h = lane >> 5;
    const int sig = (S == 1) ? 0 : up;
    const int pw = (S == 1) ? p0 + 32 * up : p0;
    const int steps = (S == 1) ? n : (n + 1) >> 1;
    const int tdiag = pw >> 5;

    const int kend = min(L, 32 * n);
    const u32x4 ksrd = make_srd(kp + (size_t)s0 * KSTRIDE + grp * ROWB, (unsigned)(kend - 1) * KSTRIDE + ROWB);
    const u32x4 vsrd = make_srd(vp + (size_t)s0 * KSTRIDE + grp * ROWB, (unsigned)(kend - 1) * KSTRIDE + ROWB);
    const u32x4 qsrd = make_srd(qp + (size_t)s0 * QSTRIDE, (unsigned)L * QSTRIDE);

    const unsigned ring = lds0 + sig * NS * SLOT;
    const unsigned mW = ring + ((S == 1) ? w * 1024 : hh * 1024);
    const unsigned drow = (unsigned)(8 * hh + (lane >> 3) + ((S == 1) ? 16 * up : 0)) * KSTRIDE;
    const unsigned voffK = drow + 16u * ((lane & 7) ^ ((hh << 2) | (lane >> 4)));
    const unsigned voffV = drow + 16u * ((lane & 7) ^ (((lane >> 4) & 1) << 2));
    const unsigned voffK2 = voffK + 16 * KSTRIDE, voffV2 = voffV + 16 * KSTRIDE;
#define PIECE(ISV, j, so, mb, MO) do { const u32x4 sr_ = (ISV) ? vsrd : ksrd;                                                            \
        const unsigned v1_ = (ISV) ? voffV : voffK, v2_ = (ISV) ? voffV2 : voffK2;                                                       \
        if (j == 0) dma16c<0, (MO) + 4096 - 128>(v1_, sr_, so, mb);                                                                      \
        else if (j == 1) dma16c<128, (MO) + 8192 - 256>(v1_, sr_, so, mb);                                                               \
        else if (j == 2) { if (S == 1) dma16e<256>(v1_, sr_, so); else dma16c<256, (MO) + 2048>(v1_, sr_, so, mb); }                      \
        else if (j == 3) dma16c<0, (MO) + 2048 + 4096 - 128>(v2_, sr_, so, mb);                                                          \
        else if (j == 4) dma16c<128, (MO) + 2048 + 8192 - 256>(v2_, sr_, so, mb);                                                        \
        else dma16e<256>(v2_, sr_, so); } while (0)
#define PIECEN(ISV, j, so, mb, MO) do { const u32x4 sr_ = (ISV) ? vsrd : ksrd;                                                           \
        const unsigned v1_ = (ISV) ? voffV : voffK, v2_ = (ISV) ? voffV2 : voffK2;                                                       \
        if (j == 0) dma16c_nt<0, (MO) + 4096 - 128>(v1_, sr_, so, mb);                                                                   \
        else if (j == 1) dma16c_nt<128, (MO) + 8192 - 256>(v1_, sr_, so, mb);                                                            \
        else if (j == 2) { if (S == 1) dma16e_nt<256>(v1_, sr_, so); else dma16c_nt<256, (MO) + 2048>(v1_, sr_, so, mb); }                \
        else if (j == 3) dma16c_nt<0, (MO) + 2048 + 4096 - 128>(v2_, sr_, so, mb);                                                       \
        else if (j == 4) dma16c_nt<128, (MO) + 2048 + 8192 - 256>(v2_, sr_, so, mb);                                                     \
        else dma16e_nt<256>(v2_, sr_, so); } while (0)
#define ELEMN(ISV, so, mb, MO) do { SETM0(mb, MO); PIECEN(ISV, 0, so, mb, MO); NOP0(); PIECEN(ISV, 1, so, mb, MO); NOP0(); PIECEN(ISV, 2, so, mb, MO);  \
        if (S == 2) { NOP0(); PIECEN(ISV, 3, so, mb, MO); NOP0(); PIECEN(ISV, 4, so, mb, MO); NOP0(); PIECEN(ISV, 5, so, mb, MO); } } while (0)
#define SETM0(mb, MO) asm volatile("s_add_u32 m0, %0, %1\n\ts_nop 0" :: "s"(mb), "i"(MO) : "memory", "scc")
#define ELEM(ISV, so, mb, MO) do { SETM0(mb, MO); PIECE(ISV, 0, so, mb, MO); NOP0(); PIECE(ISV, 1, so, mb, MO); NOP0(); PIECE(ISV, 2, so, mb, MO);      \
        if (S == 2) { NOP0(); PIECE(ISV, 3, so, mb, MO); NOP0(); PIECE(ISV, 4, so, mb, MO); NOP0(); PIECE(ISV, 5, so, mb, MO); } } while (0)
#define NOP0() asm volatile("s_nop 0" ::: "memory")
    const unsigned so0 = (unsigned)(32 * (n - 1 - sig)) * KSTRIDE;

    STAMP(2);
    ELEMN(false, so0, mW, 0);
    const unsigned qslot = (S == 1) ? lds0 + (2 + w) * SLOT : lds0 + (3 * hh + 2) * SLOT;
    {
        const unsigned qrow = (unsigned)(2 * grp + hh) * ROWB + (unsigned)pw * QSTRIDE;
        const unsigned l8 = (unsigned)(lane >> 3) * QSTRIDE;
        if (S == 1) {
            const unsigned vq0 = l8 + 16u * ((lane & 7) ^ (lane >> 4)), vq1 = l8 + 8 * QSTRIDE + 16u * ((lane & 7) ^ (4 | (lane >> 4)));
            const unsigned so2 = qrow + 16 * QSTRIDE;
            SETM0(qslot, 0);
            dmaqc<0, 4096 - 128>(vq0, qsrd, qrow, qslot); NOP0(); dmaqc<128, 8192 - 256>(vq0, qsrd, qrow, qslot); NOP0(); dmaqc<256, 1024>(vq0, qsrd, qrow, qslot); NOP0();
            dmaqc<0, 1024 + 4096 - 128>(vq1, qsrd, qrow, qslot); NOP0(); dmaqc<128, 1024 + 8192 - 256>(vq1, qsrd, qrow, qslot); NOP0(); dmaqc<256, 2048>(vq1, qsrd, qrow, qslot); NOP0();
            dmaqc<0, 2048 + 4096 - 128>(vq0, qsrd, so2, qslot); NOP0(); dmaqc<128, 2048 + 8192 - 256>(vq0, qsrd, so2, qslot); NOP0(); dmaqc<256, 3072>(vq0, qsrd, so2, qslot); NOP0();
            dmaqc<0, 3072 + 4096 - 128>(vq1, qsrd, so2, qslot); NOP0(); dmaqc<128, 3072 + 8192 - 256>(vq1, qsrd, so2, qslot); NOP0(); dmaqe<256>(vq1, qsrd, so2);
        } else {
            const unsigned vq = l8 + (unsigned)(8 * up) * QSTRIDE + 16u * ((lane & 7) ^ ((up << 2) | (lane >> 4))), vq2 = vq + 16 * QSTRIDE;
            const unsigned qm = qslot + up * 1024;
            SETM0(qm, 0);
            dmaqc<0, 4096 - 128>(vq, qsrd, qrow, qm); NOP0(); dmaqc<128, 8192 - 256>(vq, qsrd, qrow, qm); NOP0(); dmaqc<256, 2048>(vq, qsrd, qrow, qm); NOP0();
            dmaqc<0, 2048 + 4096 - 128>(vq2, qsrd, qrow, qm); NOP0(); dmaqc<128, 2048 + 8192 - 256>(vq2, qsrd, qrow, qm); NOP0(); dmaqe<256>(vq2, qsrd, qrow);
        }
    }
    if (S == 1) ELEMN(false, so0 - TSTEP, mW, SLOT); else ELEM(false, so0 - TSTEP, mW, SLOT);

    const bool act = (S == 2) || pw < L;
    const bool late = (S == 1) && act && tdiag != n - 1;
    const int itend = (S == 1) ? n - 1 : ((n - sig + 1) >> 1) - 1;
    const unsigned kb0 = r32 * 128 + 16 * (h ^ ((r32 >> 1) & 7));
    constexpr unsigned KB = (S == 1) ? SLOT : 0;
    unsigned kA = ring + KB + kb0, kBb = kA ^ 32, kC = kA ^ 64, kD = kA ^ 96;
    const int q4 = (lane & 15) >> 2, p4 = lane & 3, blk = (lane >> 4) & 1;
    unsigned vA = ring + (4 * h + q4) * 128 + 64 * (q4 >> 1) + 32 * blk + 8 * p4, vB = vA ^ 64;
    f32x16 o[6];
#pragma unroll
    for (int c = 0; c < 6; ++c) { o[c] = f32x16{}; asm volatile("" : "+v"(o[c])); }
    float m = -1e30f, l = 0.f, alpha = 1.f, mthr, mc;
    int pend = 0;
    int dq0 = pw + r32 - 4 * h;
    asm volatile("" : "+v"(kA), "+v"(kBb), "+v"(kC), "+v"(kD), "+v"(vA), "+v"(vB), "+v"(dq0));
    f32x16 s;
    float x[16];
#define KRD(i, OFF) ldk((((i) & 3) == 0 ? kA : ((i) & 3) == 1 ? kBb : ((i) & 3) == 2 ? kC : kD) + (OFF) + ((i) >> 2) * 4096)
#define VRD(i, OFF) ldv(((((i) % 6) & 1) ? vB : vA) + (OFF) + (((i) % 6) >> 1) * 4096 + ((i) / 6) * 2048)

    wait_vm<PW>();
    STAMP(4);
    barrier();
    bf16x8 q[12];
    {
        const unsigned a0 = qslot + kb0;
#pragma unroll
        for (int i = 0; i < 12; ++i) q[i] = ldk((a0 ^ (32 * (i & 3))) + (i >> 2) * 4096);
    }
    bf16x8 kf[12];
    {
        const unsigned a0 = ring + kb0;
#pragma unroll
        for (int i = 0; i < 6; ++i) kf[i] = ldk((a0 ^ (32 * (i & 3))) + (i >> 2) * 4096);
    }
    barrier();
    ELEMN(true, so0, mW, 2 * SLOT);
    if (S == 1) {
        ELEM(false, so0 - 2 * TSTEP, mW, 3 * SLOT);
        ELEMN(true, so0 - TSTEP, mW, 4 * SLOT);
        ELEM(false, so0 - 3 * TSTEP, mW, 5 * SLOT);
    }
    STAMP(5);

#define GATE(mb, MO) do { __builtin_amdgcn_s_waitcnt(((NS - 2) * PW) | 0x70);                                                            \
        asm volatile("s_barrier\n\ts_add_u32 m0, %0, %1" :: "s"(mb), "i"(MO) : "memory", "scc"); } while (0)
#define PIN1(a) asm volatile("" : "+v"(a))
#define PIN2(a, b) asm volatile("" : "+v"(a), "+v"(b))
#define PIN4(a, b, c, d) asm volatile("" : "+v"(a), "+v"(b), "+v"(c), "+v"(d))
    auto mask_diag = [&]() {
        int dq = dq0 - 32 * tdiag;
        asm volatile("" : "+v"(dq));
#pragma unroll
        for (int i = 0; i < 16; ++i) { const int c = (i & 3) + 8 * (i >> 2); s[i] = (c <= dq) ? s[i] : -__builtin_inff(); }
    };

    if (act && !late) {
        const unsigned a0 = ring + kb0;
#pragma unroll
        for (int i = 6; i < 12; ++i) kf[i] = ldk((a0 ^ (32 * (i & 3))) + (i >> 2) * 4096);
        s = MFMA(kf[0], q[0], f32x16{});
#pragma unroll
        for (int i = 1; i < 12; ++i) s = MFMA(kf[i], q[i], s);
    }
    GATE(mW, 0);
    constexpr unsigned KS0 = (S == 1) ? 0 : SLOT;
    kf[0] = KRD(0, KS0);
    PIECE(S == 1, 0, so0 - 2 * TSTEP, mW, 0); kf[1] = KRD(1, KS0);
    PIECE(S == 1, 1, so0 - 2 * TSTEP, mW, 0); kf[2] = KRD(2, KS0);
    PIECE(S == 1, 2, so0 - 2 * TSTEP, mW, 0); kf[3] = KRD(3, KS0);
    if (S == 2) PIECE(false, 3, so0 - 2 * TSTEP, mW, 0);
    kf[4] = KRD(4, KS0);
    if (S == 2) { PIECE(false, 4, so0 - 2 * TSTEP, mW, 0); NOP0(); PIECE(false, 5, so0 - 2 * TSTEP, mW, 0); }
    auto first_max = [&]() {
        float a = max3f(s[0], s[1], s[2]), b = max3f(s[8], s[9], s[10]);
        a = max3f(a, s[3], s[4]); b = max3f(b, s[11], s[12]);
        a = max3f(a, s[5], s[6]); b = max3f(b, s[13], s[14]);
        a = max3f(a, s[7], s[15]);
        m = max2f(m, swap_max(max2f(a, b)));
        mthr = m + THR_RAW; mc = -m * SCALE_LOG2E;
#pragma unroll
        for (int i = 0; i < 16; ++i) x[i] = fmaf(s[i], SCALE_LOG2E, mc);
    };
    mthr = m; mc = 0.f;
    if (act && !late) {
        if (S == 1 || sig == 0) mask_diag();
        first_max();
    }
    STAMP(6);

    int it = 0;
    unsigned soA = so0 - ((S == 1) ? 4 : 1) * TSTEP, soB = so0 - 3 * TSTEP;
    u32x4 pa, pb;
#define EXP2X(i, j) do { x[i] = __builtin_amdgcn_exp2f(x[i]); x[j] = __builtin_amdgcn_exp2f(x[j]); PIN2(x[i], x[j]); } while (0)
#define FMAX2(i, j) do { x[i] = fmaf(s[i], SCALE_LOG2E, mc); x[j] = fmaf(s[j], SCALE_LOG2E, mc); PIN2(x[i], x[j]); } while (0)
    auto iter = [&](auto PHC) {
        constexpr int PH = decltype(PHC)::value, PN = (PH + 1) % 3;
        constexpr unsigned SK = ((S == 1) ? 1 + 2 * PH : (PH == 0 ? 1 : PH == 1 ? 0 : 2)) * SLOT;
        constexpr unsigned SV = ((S == 1) ? (2 + 2 * PH) % 6 : 2 - PH) * SLOT;
        constexpr unsigned SKN = ((S == 1) ? 1 + 2 * PN : (PN == 0 ? 1 : PN == 1 ? 0 : 2)) * SLOT;
        constexpr unsigned RK = SK - KB, RKN = SKN - KB;
        float ls0, ls1;
        PIN2(kf[0], kf[1]);
        kf[5] = KRD(5, RK);
        s = MFMA(kf[0], q[0], f32x16{}); EXP2X(0, 1); SB();
        kf[6] = KRD(6, RK);
        s = MFMA(kf[1], q[1], s); EXP2X(2, 3); SB();
        PIN2(kf[2], kf[3]);
        kf[7] = KRD(7, RK);
        s = MFMA(kf[2], q[2], s); EXP2X(4, 5); SB();
        kf[8] = KRD(8, RK);
        s = MFMA(kf[3], q[3], s); EXP2X(6, 7); SB();
        PIN2(kf[4], kf[5]);
        kf[9] = KRD(9, RK);
        s = MFMA(kf[4], q[4], s); EXP2X(8, 9); ls0 = x[0] + x[2]; ls1 = x[1] + x[3]; PIN2(ls0, ls1); SB();
        kf[10] = KRD(10, RK); kf[11] = KRD(11, RK);
        s = MFMA(kf[5], q[5], s); EXP2X(10, 11); ls0 += x[4]; ls1 += x[5]; PIN2(ls0, ls1); SB();
        PIN2(kf[6], kf[7]);
        s = MFMA(kf[6], q[6], s); EXP2X(12, 13); pa[0] = cvtpk(x[0], x[1]); pa[1] = cvtpk(x[2], x[3]); PIN2(pa[0], pa[1]); SB();
        s = MFMA(kf[7], q[7], s); EXP2X(14, 15); pa[2] = cvtpk(x[4], x[5]); pa[3] = cvtpk(x[6], x[7]); PIN2(pa[2], pa[3]); SB();
        GATE(mW, SK);
        bf16x8 vf[12];
        vf[0] = VRD(0, SV); SB();
        s = MFMA(kf[8], q[8], s); vf[1] = VRD(1, SV); ls0 += x[6]; ls1 += x[7]; PIN2(ls0, ls1); SB();
        s = MFMA(kf[9], q[9], s); vf[2] = VRD(2, SV); PIECE(S == 2, 0, soA, mW, SK); ls0 += x[8]; ls1 += x[9]; PIN2(ls0, ls1); SB();
        s = MFMA(kf[10], q[10], s); vf[3] = VRD(3, SV); PIECE(S == 2, 1, soA, mW, SK); ls0 += x[10]; ls1 += x[11]; PIN2(ls0, ls1); SB();
        s = MFMA(kf[11], q[11], s); vf[4] = VRD(4, SV); PIECE(S == 2, 2, soA, mW, SK); ls0 += x[12]; ls1 += x[13]; PIN2(ls0, ls1); SB();
        const bf16x8 p0f = __builtin_bit_cast(bf16x8, pa);
        PIN2(vf[0], vf[1]);
        vf[5] = VRD(5, SV);
        o[0] = MFMA(vf[0], p0f, o[0]); if (S == 2) PIECE(true, 3, soA, mW, SK);
        pb[0] = cvtpk(x[8], x[9]); pb[1] = cvtpk(x[10], x[11]); PIN2(pb[0], pb[1]); ls0 += x[14]; ls1 += x[15]; PIN2(ls0, ls1); SB();
        vf[6] = VRD(6, SV);
        o[1] = MFMA(vf[1], p0f, o[1]); if (S == 2) PIECE(true, 4, soA, mW, SK);
        pb[2] = cvtpk(x[12], x[13]); pb[3] = cvtpk(x[14], x[15]); PIN2(pb[2], pb[3]); l += ls0; PIN1(l); SB();
        PIN2(vf[2], vf[3]);
        vf[7] = VRD(7, SV);
        o[2] = MFMA(vf[2], p0f, o[2]); if (S == 2) PIECE(true, 5, soA, mW, SK);
        l += ls1; PIN1(l); SB();
        const bf16x8 p1f = __builtin_bit_cast(bf16x8, pb);
        vf[8] = VRD(8, SV);
        o[3] = MFMA(vf[3], p0f, o[3]); SB();
        float ma = max3f(s[0], s[1], s[2]), mb = max3f(s[8], s[9], s[10]);
        ma = max3f(ma, s[3], s[4]); mb = max3f(mb, s[11], s[12]);
        ma = max3f(ma, s[5], s[6]); mb = max3f(mb, s[13], s[14]); PIN2(ma, mb); SB();
        PIN2(vf[4], vf[5]);
        vf[9] = VRD(9, SV);
        o[4] = MFMA(vf[4], p0f, o[4]);
        ma = max3f(ma, s[7], s[15]);
        float pm = swap_max(max2f(ma, mb)); PIN1(pm); SB();
        vf[10] = VRD(10, SV); vf[11] = VRD(11, SV);
        o[5] = MFMA(vf[5], p0f, o[5]); SB();
        if (__builtin_expect(__any(pm > mthr), 0)) {
            const float mn = max2f(m, pm);
            alpha = __builtin_amdgcn_exp2f((m - mn) * SCALE_LOG2E);
            m = mn; mthr = m + THR_RAW; mc = -m * SCALE_LOG2E; pend = 1;
        }
        PIN2(vf[6], vf[7]);
        o[0] = MFMA(vf[6], p1f, o[0]); FMAX2(0, 1); FMAX2(2, 3); FMAX2(4, 5); SB();
        o[1] = MFMA(vf[7], p1f, o[1]); FMAX2(6, 7); FMAX2(8, 9); FMAX2(10, 11); SB();
        GATE(mW, SV);
        kf[0] = KRD(0, RKN); SB();
        o[2] = MFMA(vf[8], p1f, o[2]); kf[1] = KRD(1, RKN); PIECE(S == 1, 0, soB, mW, SV); FMAX2(12, 13); SB();
        o[3] = MFMA(vf[9], p1f, o[3]); kf[2] = KRD(2, RKN); PIECE(S == 1, 1, soB, mW, SV); FMAX2(14, 15); SB();
        o[4] = MFMA(vf[10], p1f, o[4]); PIECE(S == 1, 2, soB, mW, SV); kf[3] = KRD(3, RKN); if (S == 2) PIECE(false, 3, soB, mW, SV); SB();
        o[5] = MFMA(vf[11], p1f, o[5]); if (S == 2) PIECE(false, 4, soB, mW, SV); kf[4] = KRD(4, RKN); if (S == 2) PIECE(false, 5, soB, mW, SV); SB();
        soA -= TSTEP; soB -= TSTEP;
        if (__builtin_expect(pend, 0)) {
            l *= alpha;
#pragma unroll
            for (int c = 0; c < 6; ++c) o[c] *= alpha;
            pend = 0;
        }
        ++it;
    };
    bool skip0 = false;
    if (S == 1 && late) {
        constexpr unsigned SK = SLOT, SV = 2 * SLOT, RKN = 3 * SLOT - KB;
        kf[5] = KRD(5, 0); kf[6] = KRD(6, 0); kf[7] = KRD(7, 0); kf[8] = KRD(8, 0);
        s = MFMA(kf[0], q[0], f32x16{});
        kf[9] = KRD(9, 0); kf[10] = KRD(10, 0); kf[11] = KRD(11, 0);
#pragma unroll
        for (int i = 1; i < 8; ++i) s = MFMA(kf[i], q[i], s);
        GATE(mW, SK);
        s = MFMA(kf[8], q[8], s); SB();
        PIECE(false, 0, soA, mW, SK); s = MFMA(kf[9], q[9], s); SB();
        PIECE(false, 1, soA, mW, SK); s = MFMA(kf[10], q[10], s); SB();
        PIECE(false, 2, soA, mW, SK); s = MFMA(kf[11], q[11], s); SB();
        GATE(mW, SV);
        kf[0] = KRD(0, RKN); PIECE(true, 0, soB, mW, SV); kf[1] = KRD(1, RKN); PIECE(true, 1, soB, mW, SV); kf[2] = KRD(2, RKN); PIECE(true, 2, soB, mW, SV);
        kf[3] = KRD(3, RKN); kf[4] = KRD(4, RKN);
        mask_diag();
        first_max();
        soA -= TSTEP; soB -= TSTEP;
        it = 1; skip0 = true;
    }
    if (act && it < itend) {
        for (;;) {
            if (!skip0) { iter(IC<0>{}); if (it >= itend) break; }
            skip0 = false;
            iter(IC<1>{}); if (it >= itend) break;
            iter(IC<2>{}); if (it >= itend) break;
        }
    }
    STAMP(7);
    const int ph = it % 3;
    unsigned tk = ((S == 1) ? 1 + 2 * ph : (ph == 0 ? 1 : ph == 1 ? 0 : 2)) * SLOT;
    unsigned tv = ((S == 1) ? (ph == 2 ? 0 : 2 + 2 * ph) : 2 - ph) * SLOT;
    auto adv2 = [&](unsigned a) { const unsigned t = a + 2 * SLOT; return t >= NS * SLOT ? t - NS * SLOT : t; };
    if (act) {
        const unsigned mk = mW + tk, mv = mW + tv;
        GATE(mk, 0);
        const unsigned va = vA + tv, vb = vB + tv;
        bf16x8 vf[12];
#define VRT(i) ldv(((((i) % 6) & 1) ? vb : va) + (((i) % 6) >> 1) * 4096 + ((i) / 6) * 2048)
        vf[0] = VRT(0); PIECE(S == 2, 0, soA, mk, 0); vf[1] = VRT(1); PIECE(S == 2, 1, soA, mk, 0); vf[2] = VRT(2); PIECE(S == 2, 2, soA, mk, 0);
        vf[3] = VRT(3); if (S == 2) PIECE(true, 3, soA, mk, 0); vf[4] = VRT(4); if (S == 2) PIECE(true, 4, soA, mk, 0); vf[5] = VRT(5); if (S == 2) PIECE(true, 5, soA, mk, 0);
        float ls0 = 0.f, ls1 = 0.f;
#pragma unroll
        for (int i = 0; i < 16; i += 2) { x[i] = __builtin_amdgcn_exp2f(x[i]); x[i + 1] = __builtin_amdgcn_exp2f(x[i + 1]); ls0 += x[i]; ls1 += x[i + 1]; }
        l += ls0 + ls1;
#pragma unroll
        for (int i = 0; i < 4; ++i) { pa[i] = cvtpk(x[2 * i], x[2 * i + 1]); pb[i] = cvtpk(x[8 + 2 * i], x[9 + 2 * i]); }
        PIN4(pa[0], pa[1], pa[2], pa[3]); PIN4(pb[0], pb[1], pb[2], pb[3]);
        const bf16x8 p0f = __builtin_bit_cast(bf16x8, pa), p1f = __builtin_bit_cast(bf16x8, pb);
        SB();
        vf[6] = VRT(6); vf[7] = VRT(7);
        o[0] = MFMA(vf[0], p0f, o[0]); SB();
        vf[8] = VRT(8); vf[9] = VRT(9);
        o[1] = MFMA(vf[1], p0f, o[1]); SB();
        vf[10] = VRT(10); vf[11] = VRT(11);
        o[2] = MFMA(vf[2], p0f, o[2]); SB();
        o[3] = MFMA(vf[3], p0f, o[3]); o[4] = MFMA(vf[4], p0f, o[4]); o[5] = MFMA(vf[5], p0f, o[5]);
        o[0] = MFMA(vf[6], p1f, o[0]); o[1] = MFMA(vf[7], p1f, o[1]); SB();
        if (it + 1 < steps) {
            GATE(mv, 0);
            NOP0();
            PIECE(S == 1, 0, soB, mv, 0); NOP0(); PIECE(S == 1, 1, soB, mv, 0); NOP0(); PIECE(S == 1, 2, soB, mv, 0);
            if (S == 2) { NOP0(); PIECE(false, 3, soB, mv, 0); NOP0(); PIECE(false, 4, soB, mv, 0); NOP0(); PIECE(false, 5, soB, mv, 0); }
        }
        o[2] = MFMA(vf[8], p1f, o[2]); o[3] = MFMA(vf[9], p1f, o[3]); o[4] = MFMA(vf[10], p1f, o[4]); o[5] = MFMA(vf[11], p1f, o[5]);
        tk = adv2(tk); tv = adv2(tv); soA -= TSTEP; soB -= TSTEP;
        ++it;
    }
    STAMP(8);
    for (; it < steps; ++it) {
        const unsigned mk = mW + tk, mv = mW + tv;
        GATE(mk, 0);
        NOP0();
        PIECE(S == 2, 0, soA, mk, 0); NOP0(); PIECE(S == 2, 1, soA, mk, 0); NOP0(); PIECE(S == 2, 2, soA, mk, 0);
        if (S == 2) { NOP0(); PIECE(true, 3, soA, mk, 0); NOP0(); PIECE(true, 4, soA, mk, 0); NOP0(); PIECE(true, 5, soA, mk, 0); }
        if (it + 1 < steps) {
            GATE(mv, 0);
            NOP0();
            PIECE(S == 1, 0, soB, mv, 0); NOP0(); PIECE(S == 1, 1, soB, mv, 0); NOP0(); PIECE(S == 1, 2, soB, mv, 0);
            if (S == 2) { NOP0(); PIECE(false, 3, soB, mv, 0); NOP0(); PIECE(false, 4, soB, mv, 0); NOP0(); PIECE(false, 5, soB, mv, 0); }
        }
        tk = adv2(tk); tv = adv2(tv); soA -= TSTEP; soB -= TSTEP;
    }

    STAMP(10);
    l = swap_add(l);
    const __amdgpu_buffer_rsrc_t orsrc = __builtin_amdgcn_make_buffer_rsrc((void*)(op + (size_t)s0 * QSTRIDE), 0, (unsigned)L * QSTRIDE, 0x00020000);
    const unsigned stw = r32 * ORS + 8 * h;
    auto stage_block = [&](unsigned base, int c, const f32x16& z) {
#pragma unroll
        for (int g = 0; g < 4; ++g) {
            u32x2 v = {cvtpk(z[4 * g], z[4 * g + 1]), cvtpk(z[4 * g + 2], z[4 * g + 3])};
            *(LDSP(u32x2))(size_t)(base + (32 * c + 8 * g) * 2) = v;
        }
    };
    unsigned lrd[3], gof[3];
#pragma unroll
    for (int j = 0; j < 3; ++j) { const unsigned idx = 64 * j + lane, row = (idx * 2731) >> 16, ch = idx - 24 * row; lrd[j] = row * ORS + ch * 16; gof[j] = row * QSTRIDE + ch * 16; }
    wait_vm<0>();
    barrier();
    STAMP(11);
    if (S == 1) {
        if (act) {
            const unsigned stg = lds0 + w * (32 * ORS);
            const float inv = __builtin_amdgcn_rcpf(l);
#pragma unroll
            for (int c = 0; c < 6; ++c) { const f32x16 z = o[c] * inv; stage_block(stg + stw, c, z); }
            STAMP(15);
            const unsigned obase = (unsigned)pw * QSTRIDE + (unsigned)(2 * grp + hh) * ROWB;
#pragma unroll
            for (int k = 0; k < 4; ++k)
#pragma unroll
                for (int j = 0; j < 3; ++j) {
                    const u32x4 v = *(LDSP(u32x4))(size_t)(stg + lrd[j] + 8 * k * ORS);
                    __builtin_amdgcn_raw_buffer_store_b128(v, orsrc, gof[j], obase + 8 * k * QSTRIDE, STAUX);
                }
        }
    } else {
        const unsigned mine = lds0 + w * SLOT + lane * 16, other = lds0 + (w ^ 2) * SLOT + lane * 16;
        {
            f32x2 ml = {m, l};
            *(LDSP(f32x2))(size_t)(lds0 + 4 * SLOT + w * 512 + lane * 8) = ml;
        }
        auto send = [&](const f32x16& z, int c) {
#pragma unroll
            for (int g = 0; g < 4; ++g) { f32x4 y = {z[4 * g], z[4 * g + 1], z[4 * g + 2], z[4 * g + 3]}; *(LDSP(f32x4))(size_t)(mine + (c * 4 + g) * 1024) = y; }
        };
        if (sig) { send(o[0], 0); send(o[1], 1); send(o[2], 2); } else { send(o[3], 0); send(o[4], 1); send(o[5], 2); }
        STAMP(12);
        barrier();
        STAMP(13);
        const f32x2 mlo = *(LDSP(f32x2))(size_t)(lds0 + 4 * SLOT + (w ^ 2) * 512 + lane * 8);
        const float mx = max2f(m, mlo[0]);
        const float a0 = __builtin_amdgcn_exp2f((m - mx) * SCALE_LOG2E), a1 = __builtin_amdgcn_exp2f((mlo[0] - mx) * SCALE_LOG2E);
        const float inv = __builtin_amdgcn_rcpf(l * a0 + mlo[1] * a1);
        const float f0 = a0 * inv, f1 = a1 * inv;
        const unsigned stg = lds0 + 51200 + hh * (32 * ORS);
        auto merge = [&](const f32x16& xo, int c, int cd) {
            f32x16 z;
#pragma unroll
            for (int g = 0; g < 4; ++g) {
                const f32x4 y = *(LDSP(f32x4))(size_t)(other + (c * 4 + g) * 1024);
                const f32x4 t = {xo[4 * g], xo[4 * g + 1], xo[4 * g + 2], xo[4 * g + 3]};
                const f32x4 u = t * f0 + y * f1;
                z[4 * g] = u[0]; z[4 * g + 1] = u[1]; z[4 * g + 2] = u[2]; z[4 * g + 3] = u[3];
            }
            stage_block(stg + stw, cd, z);
        };
        if (sig) { merge(o[3], 0, 3); merge(o[4], 1, 4); merge(o[5], 2, 5); } else { merge(o[0], 0, 0); merge(o[1], 1, 1); merge(o[2], 2, 2); }
        STAMP(14);
        barrier();
        STAMP(15);
        const unsigned obase = (unsigned)(pw + 16 * sig) * QSTRIDE + (unsigned)(2 * grp + hh) * ROWB;
        const unsigned stgr = stg + 16 * sig * ORS;
#pragma unroll
        for (int k = 0; k < 2; ++k)
#pragma unroll
            for (int j = 0; j < 3; ++j) {
                const u32x4 v = *(LDSP(u32x4))(size_t)(stgr + lrd[j] + 8 * k * ORS);
                __builtin_amdgcn_raw_buffer_store_b128(v, orsrc, gof[j], obase + 8 * k * QSTRIDE, STAUX);
            }
    }
    STAMP(16);
}

template <int CTRL> DI int dpp_mov(int old, int x) { return __builtin_amdgcn_update_dpp(old, x, CTRL, 0xf, 0xf, false); }
DI int wave_incl_scan(int x) {
    x += dpp_mov<0x111>(0, x);
    x += dpp_mov<0x112>(0, x);
    x += dpp_mov<0x114>(0, x);
    x += dpp_mov<0x118>(0, x);
    x += __builtin_amdgcn_update_dpp(0, x, 0x142, 0xa, 0xf, false);
    x += __builtin_amdgcn_update_dpp(0, x, 0x143, 0xc, 0xf, false);
    return x;
}
DI int wave_max_nonneg(int x) {
    x = max(x, dpp_mov<0x111>(0, x));
    x = max(x, dpp_mov<0x112>(0, x));
    x = max(x, dpp_mov<0x114>(0, x));
    x = max(x, dpp_mov<0x118>(0, x));
    x = max(x, __builtin_amdgcn_update_dpp(0, x, 0x142, 0xa, 0xf, false));
    x = max(x, __builtin_amdgcn_update_dpp(0, x, 0x143, 0xc, 0xf, false));
    return __builtin_amdgcn_readlane(x, 63);
}

DI bool decode_slow(const int* __restrict__ cu, int nseq, int k, int& s0, int& L, int& p0, int& isx) {
    int nmax = 0;
    for (int s = 0; s < nseq; ++s) nmax = max(nmax, (cu[s + 1] - cu[s] + 31) >> 5);
    const int B = BSPLIT;
    int N = 0;
    for (int s = 0; s < nseq; ++s) { const int n = (cu[s + 1] - cu[s] + 31) >> 5; N += max(n - B, 0) + min(B >> 1, (n + 1) >> 1); }
    int rank = k;
    if (k < CUS_PER_XCD) { const int N2 = min(max(N - CUS_PER_XCD, 0), (PFMODE != 0) ? PF0 - CUS_PER_XCD : CUS_PER_XCD); rank = (k < N2) ? CUS_PER_XCD - 1 - k : k - N2; }
    if (rank >= N) return false;
    int jx = nmax, my = min(B >> 1, (nmax + 1) >> 1);
    for (;;) {
        const int x = (jx > B) && (((jx + 1) >> 1) >= 2 * my);
        const int thr = x ? 32 * (jx - 1) : 64 * (my - 1);
        for (int s = 0; s < nseq; ++s) {
            const int len = cu[s + 1] - cu[s];
            if (len > thr) { if (rank == 0) { s0 = cu[s]; L = len; p0 = thr; isx = x; return true; } --rank; }
        }
        if (x) --jx; else --my;
    }
}

__global__ __launch_bounds__(256, 2) void attn_varlen_kernel(const char* __restrict__ qp, const char* __restrict__ kp, const char* __restrict__ vp,
                                                             const int* __restrict__ cu, char* __restrict__ op, int nseq, int T
                                                             ) {
    const unsigned long long rt_start = __builtin_amdgcn_s_memrealtime();
    __builtin_amdgcn_sched_barrier(0);
    __shared__ __attribute__((aligned(16))) char smem_[LDS_BYTES];
    const int grp = blockIdx.x & 7;
    const int kraw = blockIdx.x >> 3;
    const int lane = threadIdx.x & 63;

    asm volatile("" :: "s"(qp), "s"(kp), "s"(vp), "s"(op), "s"(cu), "s"(nseq));
    if (CODEPF > 0 && kraw < CODEWG) {
        unsigned long long pc; asm volatile("s_getpc_b64 %0" : "=s"(pc));
        const char* cb = (const char*)(pc & ~127ull);
        const unsigned mscr0 = rfl((unsigned)(size_t)(lds_char_ptr)smem_ + 73728 + 1024 + (threadIdx.x >> 6) * 256);
        asm volatile("s_mov_b32 m0, %0" :: "s"(mscr0) : "memory");
        for (unsigned o = threadIdx.x * 128; o < CODEPF; o += 256 * 128)
            asm volatile("global_load_lds_dword %0, off" :: "v"(cb + o) : "memory");
    }
    if (PFMODE != 0 && kraw >= PF0 && kraw < PF0 + 8) {
        const int wv = rfl(threadIdx.x >> 6);
        const unsigned mscr = rfl((unsigned)(size_t)(lds_char_ptr)smem_ + wv * 256);
        asm volatile("s_mov_b32 m0, %0" :: "s"(mscr) : "memory");
        const unsigned j = (kraw - PF0) * 256 + threadIdx.x;
        const u32x4 ro = make_srd(op, (unsigned)T * QSTRIDE), rq = make_srd(qp, (unsigned)T * QSTRIDE);
        const u32x4 rk = make_srd(kp, (unsigned)T * KSTRIDE), rv = make_srd(vp, (unsigned)T * KSTRIDE);
        asm volatile("s_nop 4" :: "s"(ro), "s"(rq), "s"(rk), "s"(rv) : "memory");
        for (unsigned off = j * PSTRIDE; off < (unsigned)T * QSTRIDE; off += 2048 * PSTRIDE) {
            if (PFMODE & 1) asm volatile("buffer_load_dword %0, %1, 0 offen nt lds" :: "v"(off), "s"(ro) : "memory");
            if (PFMODE & 2) {
                asm volatile("buffer_load_dword %0, %1, 0 offen nt lds" :: "v"(off), "s"(rq) : "memory");
                asm volatile("buffer_load_dword %0, %1, 0 offen nt lds" :: "v"(off), "s"(rk) : "memory");
                asm volatile("buffer_load_dword %0, %1, 0 offen nt lds" :: "v"(off), "s"(rv) : "memory");
            }
        }
        return;
    }
    const int k = (PFMODE != 0 && kraw >= PF0 + 8) ? kraw - 8 : kraw;
    typedef unsigned u32x16 __attribute__((ext_vector_type(16)));
    u32x16 cus;
    {
        const u32x4 cr = make_srd(cu, (unsigned)(nseq + 1) * 4);
        asm volatile("s_buffer_load_dwordx16 %0, %1, 0" : "=s"(cus) : "s"(cr) : "memory");
    }
    const int l3 = (lane * 43) >> 7, r3 = lane - 3 * l3, st = l3 + 1;
    const int jx = 2 * st - 1 + r3;
    const bool okx = (r3 < 2) & (jx > BSPLIT) & (lane < 63), oky = (r3 == 2) & !(st & 1) & (st <= BSPLIT);
    int thr = oky ? 32 * (st - 2) : 0x7fffffff;
    thr = okx ? 32 * (jx - 1) : thr;
    asm volatile("s_waitcnt lgkmcnt(0)" : "+s"(cus) :: "memory");
    int len = 0, c0 = 0, cnt = 0;
#define SEQ(s) do { const int ls = (int)cus[(s) + 1] - (int)cus[s];                         \
        cnt += (ls > thr) ? 1 : 0;                                                                                                      \
        asm("v_writelane_b32 %0, %1, %2" : "+v"(len) : "s"(ls), "i"(s)); asm("v_writelane_b32 %0, %1, %2" : "+v"(c0) : "s"(cus[s]), "i"(s)); } while (0)
    SEQ(0); SEQ(1); SEQ(2); SEQ(3); SEQ(4); SEQ(5); SEQ(6); SEQ(7);
    if (__builtin_expect(nseq > 8, 0)) { SEQ(8); SEQ(9); SEQ(10); SEQ(11); SEQ(12); SEQ(13); SEQ(14); }
    const int incl = wave_incl_scan(cnt);
    const int N = __builtin_amdgcn_readlane(incl, 63);
    const int N2 = min(max(N - CUS_PER_XCD, 0), (PFMODE != 0) ? PF0 - CUS_PER_XCD : CUS_PER_XCD);
    (void)N2;
    const int rank = (k >= CUS_PER_XCD && N <= ((PFMODE != 0) ? PF0 : 2 * CUS_PER_XCD)) ? (k < N ? N - 1 - (k - CUS_PER_XCD) : N) : k;
    bool valid = rank < N;
    const int a = N - rank;
    const int lv = (__ffsll((long long)__ballot(incl >= a)) - 1) & 63;
    const int idx = __builtin_amdgcn_readlane(incl, lv) - a;
    int p0 = __builtin_amdgcn_readlane(thr, lv);
    int isx = __builtin_amdgcn_readlane(r3, lv) < 2;
    const bool has = len > p0;
    const unsigned long long mk = __ballot(has);
    const int below = __builtin_amdgcn_mbcnt_hi((unsigned)(mk >> 32), __builtin_amdgcn_mbcnt_lo((unsigned)mk, 0));
    const int sq = (__ffsll((long long)__ballot(has && below == idx)) - 1) & 63;
    int s0 = __builtin_amdgcn_readlane(c0, sq), L = __builtin_amdgcn_readlane(len, sq);
    if (__builtin_expect(nseq > 15 || __ballot(len > 32 * 42) != 0, 0)) {
        int a0 = 0, a1 = 0, a2 = 0, a3 = 0;
        const int v = decode_slow(cu, nseq, k, a0, a1, a2, a3);
        valid = rfl(v) != 0; s0 = rfl(a0); L = rfl(a1); p0 = rfl(a2); isx = rfl(a3);
    }
    if (!valid) return;
    s0 = rfl(s0); L = rfl(L); p0 = rfl(p0); isx = rfl(isx);
    if (PRIO && k < CUS_PER_XCD) __builtin_amdgcn_s_setprio(PRIO);
    const unsigned lds0 = (unsigned)(size_t)(lds_char_ptr)smem_;
    if (isx) {
        const int n = (min(p0 + 32, L) + 31) >> 5;
        attn_item<2>(qp, kp, vp, op, grp, s0, L, p0, n, lds0 STPASS);
    } else {
        const int n = (min(p0 + 64, L) + 31) >> 5;
        attn_item<1>(qp, kp, vp, op, grp, s0, L, p0, n, lds0 STPASS);
    }
    if (blockIdx.x == 0) { while (__builtin_amdgcn_s_memrealtime() - rt_start < DIAG_ABSRT) __builtin_amdgcn_s_sleep(1); }
}

extern "C" void kernel_launch(void* const* d_in, const int* in_sizes, int n_in, void* d_out, int out_size, void* d_ws, size_t ws_size,
                              hipStream_t stream) {
    (void)n_in; (void)out_size; (void)d_ws; (void)ws_size;
    const int T = in_sizes[0] / (HQ * D);
    const int nseq = in_sizes[3] - 1;
    const int per_head = T / 32 + nseq + 1 + 8;
    attn_varlen_kernel<<<dim3(8 * per_head), dim3(256), 0, stream>>>((const char*)d_in[0], (const char*)d_in[1], (const char*)d_in[2],
                                                                    (const int*)d_in[3], (char*)d_out, nseq, T
                                                                    );
}
```

```cpp
#include <hip/hip_runtime.h>
#include <hip/hip_bf16.h>
#include <stdint.h>

typedef __bf16 bf16x8 __attribute__((ext_vector_type(8)));
typedef __bf16 bf16x2 __attribute__((ext_vector_type(2)));
typedef short s16x4 __attribute__((ext_vector_type(4)));
typedef short s16x8 __attribute__((ext_vector_type(8)));
typedef float f32x2 __attribute__((ext_vector_type(2)));
typedef float f32x4 __attribute__((ext_vector_type(4)));
typedef float f32x16 __attribute__((ext_vector_type(16)));
typedef unsigned u32x2 __attribute__((ext_vector_type(2)));
typedef unsigned u32x4 __attribute__((ext_vector_type(4)));
#define LDSP(T) __attribute__((address_space(3))) T*
typedef LDSP(char) lds_char_ptr;

#define DI __device__ __forceinline__
#define LDPOL ""
#define FTPOL ""
#define PSTRIDE 4096
#define EARLY 1
#define EPOL ""
#define PAIRMODE 1
#define PRIO 2
#define QPOL ""
#define STAUX 16
#define PFPOL ""
#define PFMODE 0
#define CODEPF 26624
#define CODEWG 8
#define STAMP(i) do {} while (0)
#define STARG
#define STPASS

constexpr int HQ = 16, HKV = 8, D = 192;
constexpr unsigned QSTRIDE = HQ * D * 2;
constexpr unsigned KSTRIDE = HKV * D * 2;
constexpr unsigned ROWB = D * 2;
constexpr unsigned SLOT = 32 * ROWB;
constexpr unsigned ORS = 400;
constexpr unsigned LDS_BYTES = 77824;
constexpr float SCALE_LOG2E = 0.07216878364870322f * 1.4426950408889634f;
constexpr float THR_RAW = 8.0f / SCALE_LOG2E;
constexpr int CUS_PER_XCD = 32;
constexpr int PF0 = 56;
constexpr int BSPLIT = 8;

DI int rfl(int x) { return __builtin_amdgcn_readfirstlane(x); }
DI unsigned cvtpk(float lo, float hi) {
    f32x2 v = {lo, hi}; bf16x2 b = __builtin_convertvector(v, bf16x2); return __builtin_bit_cast(unsigned, b);
}
DI float max2f(float a, float b) { return __builtin_elementwise_maximum(a, b); }
DI float max3f(float a, float b, float c) { return max2f(max2f(a, b), c); }
DI float swap_max(float x) {
    auto r = __builtin_amdgcn_permlane32_swap(__float_as_uint(x), __float_as_uint(x), false, false);
    return max2f(__uint_as_float(r[0]), __uint_as_float(r[1]));
}
DI float swap_add(float x) {
    auto r = __builtin_amdgcn_permlane32_swap(__float_as_uint(x), __float_as_uint(x), false, false);
    return __uint_as_float(r[0]) + __uint_as_float(r[1]);
}
DI u32x4 make_srd(const void* base, unsigned nbytes) {
    unsigned long long a = (unsigned long long)base;
    u32x4 s; s[0] = rfl((unsigned)a); s[1] = rfl((unsigned)(a >> 32) & 0xffffu); s[2] = rfl(nbytes); s[3] = 0x00020000u;
    return s;
}
template <int IMM> DI void dma16(unsigned m0v, unsigned voff, u32x4 srd, unsigned soff) {
    asm volatile("s_mov_b32 m0, %0\n\ts_nop 0\n\tbuffer_load_dwordx4 %1, %2, %3 offen offset:%4 lds"
                 :: "s"(m0v), "v"(voff), "s"(srd), "s"(soff), "i"(IMM) : "memory");
}
template <int IMM, int NEXT> DI void dma16c_nt(unsigned voff, u32x4 srd, unsigned soff, unsigned nbase) {
    asm volatile("buffer_load_dwordx4 %0, %1, %2 offen offset:%3 " FTPOL " lds\n\ts_add_u32 m0, %4, %5"
                 :: "v"(voff), "s"(srd), "s"(soff), "i"(IMM), "s"(nbase), "i"(NEXT) : "memory", "scc");
}
template <int IMM> DI void dma16e_nt(unsigned voff, u32x4 srd, unsigned soff) {
    asm volatile("buffer_load_dwordx4 %0, %1, %2 offen offset:%3 " FTPOL " lds" :: "v"(voff), "s"(srd), "s"(soff), "i"(IMM) : "memory");
}
template <int IMM, int NEXT> DI void dma16c(unsigned voff, u32x4 srd, unsigned soff, unsigned nbase) {
    asm volatile("buffer_load_dwordx4 %0, %1, %2 offen offset:%3 " LDPOL " lds\n\ts_add_u32 m0, %4, %5"
                 :: "v"(voff), "s"(srd), "s"(soff), "i"(IMM), "s"(nbase), "i"(NEXT) : "memory", "scc");
}
template <int IMM, int NEXT> DI void dmaqc(unsigned voff, u32x4 srd, unsigned soff, unsigned nbase) {
    asm volatile("buffer_load_dwordx4 %0, %1, %2 offen offset:%3 " QPOL " lds\n\ts_add_u32 m0, %4, %5"
                 :: "v"(voff), "s"(srd), "s"(soff), "i"(IMM), "s"(nbase), "i"(NEXT) : "memory", "scc");
}
template <int IMM> DI void dmaqe(unsigned voff, u32x4 srd, unsigned soff) {
    asm volatile("buffer_load_dwordx4 %0, %1, %2 offen offset:%3 " QPOL " lds" :: "v"(voff), "s"(srd), "s"(soff), "i"(IMM) : "memory");
}
template <int IMM> DI void dma16e(unsigned voff, u32x4 srd, unsigned soff) {
    asm volatile("buffer_load_dwordx4 %0, %1, %2 offen offset:%3 " LDPOL " lds" :: "v"(voff), "s"(srd), "s"(soff), "i"(IMM) : "memory");
}
template <int N> DI void wait_vm() { asm volatile("s_waitcnt vmcnt(%0)" :: "i"(N) : "memory"); }
DI void barrier() { asm volatile("s_waitcnt lgkmcnt(0)\n\ts_barrier" ::: "memory"); }
#define SB() __builtin_amdgcn_sched_barrier(0)
#define MFMA(a, b, c) __builtin_amdgcn_mfma_f32_32x32x16_bf16((a), (b), (c), 0, 0, 0)

DI bf16x8 ldk(unsigned addr) { return *(LDSP(bf16x8))(size_t)addr; }
DI bf16x8 ldv(unsigned addr) {
    const s16x4 lo = __builtin_amdgcn_ds_read_tr16_b64_v4i16((LDSP(s16x4))(size_t)addr);
    const s16x4 hi = __builtin_amdgcn_ds_read_tr16_b64_v4i16((LDSP(s16x4))(size_t)(addr + 1024));
    return __builtin_bit_cast(bf16x8, __builtin_shufflevector(lo, hi, 0, 1, 2, 3, 4, 5, 6, 7));
}

template <int V> struct IC { static constexpr int value = V; };

template <int S>
DI void attn_item(const char* __restrict__ qp, const char* __restrict__ kp, const char* __restrict__ vp, char* __restrict__ op,
                  int grp, int s0, int L, int p0, int n, unsigned lds0 STARG) {
    constexpr int NS = (S == 1) ? 6 : 3;
    constexpr int PW = (S == 1) ? 3 : 6;
    constexpr unsigned TSTEP = 32 * S * KSTRIDE;
    const int tid = threadIdx.x, lane = tid & 63;
    const int w = rfl(tid >> 6);
    const int hh = w & 1, up = w >> 1;
    const int r32 = lane & 31, h = lane >> 5;
    const int sig = (S == 1) ? 0 : up;
    const int pw = (S == 1) ? p0 + 32 * up : p0;
    const int steps = (S == 1) ? n : (n + 1) >> 1;
    const int tdiag = pw >> 5;

    const int kend = min(L, 32 * n);
    const u32x4 ksrd = make_srd(kp + (size_t)s0 * KSTRIDE + grp * ROWB, (unsigned)(kend - 1) * KSTRIDE + ROWB);
    const u32x4 vsrd = make_srd(vp + (size_t)s0 * KSTRIDE + grp * ROWB, (unsigned)(kend - 1) * KSTRIDE + ROWB);
    const u32x4 qsrd = make_srd(qp + (size_t)s0 * QSTRIDE, (unsigned)L * QSTRIDE);

    const unsigned ring = lds0 + sig * NS * SLOT;
    const unsigned mW = ring + ((S == 1) ? w * 1024 : hh * 1024);
    const unsigned drow = (unsigned)(8 * hh + (lane >> 3) + ((S == 1) ? 16 * up : 0)) * KSTRIDE;
    const unsigned voffK = drow + 16u * ((lane & 7) ^ ((hh << 2) | (lane >> 4)));
    const unsigned voffV = drow + 16u * ((lane & 7) ^ (((lane >> 4) & 1) << 2));
    const unsigned voffK2 = voffK + 16 * KSTRIDE, voffV2 = voffV + 16 * KSTRIDE;
#define PIECE(ISV, j, so, mb, MO) do { const u32x4 sr_ = (ISV) ? vsrd : ksrd;                                                            \
        const unsigned v1_ = (ISV) ? voffV : voffK, v2_ = (ISV) ? voffV2 : voffK2;                                                       \
        if (j == 0) dma16c<0, (MO) + 4096 - 128>(v1_, sr_, so, mb);                                                                      \
        else if (j == 1) dma16c<128, (MO) + 8192 - 256>(v1_, sr_, so, mb);                                                               \
        else if (j == 2) { if (S == 1) dma16e<256>(v1_, sr_, so); else dma16c<256, (MO) + 2048>(v1_, sr_, so, mb); }                      \
        else if (j == 3) dma16c<0, (MO) + 2048 + 4096 - 128>(v2_, sr_, so, mb);                                                          \
        else if (j == 4) dma16c<128, (MO) + 2048 + 8192 - 256>(v2_, sr_, so, mb);                                                        \
        else dma16e<256>(v2_, sr_, so); } while (0)
#define PIECEN(ISV, j, so, mb, MO) do { const u32x4 sr_ = (ISV) ? vsrd : ksrd;                                                           \
        const unsigned v1_ = (ISV) ? voffV : voffK, v2_ = (ISV) ? voffV2 : voffK2;                                                       \
        if (j == 0) dma16c_nt<0, (MO) + 4096 - 128>(v1_, sr_, so, mb);                                                                   \
        else if (j == 1) dma16c_nt<128, (MO) + 8192 - 256>(v1_, sr_, so, mb);                                                            \
        else if (j == 2) { if (S == 1) dma16e_nt<256>(v1_, sr_, so); else dma16c_nt<256, (MO) + 2048>(v1_, sr_, so, mb); }                \
        else if (j == 3) dma16c_nt<0, (MO) + 2048 + 4096 - 128>(v2_, sr_, so, mb);                                                       \
        else if (j == 4) dma16c_nt<128, (MO) + 2048 + 8192 - 256>(v2_, sr_, so, mb);                                                     \
        else dma16e_nt<256>(v2_, sr_, so); } while (0)
#define ELEMN(ISV, so, mb, MO) do { SETM0(mb, MO); PIECEN(ISV, 0, so, mb, MO); NOP0(); PIECEN(ISV, 1, so, mb, MO); NOP0(); PIECEN(ISV, 2, so, mb, MO);  \
        if (S == 2) { NOP0(); PIECEN(ISV, 3, so, mb, MO); NOP0(); PIECEN(ISV, 4, so, mb, MO); NOP0(); PIECEN(ISV, 5, so, mb, MO); } } while (0)
#define SETM0(mb, MO) asm volatile("s_add_u32 m0, %0, %1\n\ts_nop 0" :: "s"(mb), "i"(MO) : "memory", "scc")
#define ELEM(ISV, so, mb, MO) do { SETM0(mb, MO); PIECE(ISV, 0, so, mb, MO); NOP0(); PIECE(ISV, 1, so, mb, MO); NOP0(); PIECE(ISV, 2, so, mb, MO);      \
        if (S == 2) { NOP0(); PIECE(ISV, 3, so, mb, MO); NOP0(); PIECE(ISV, 4, so, mb, MO); NOP0(); PIECE(ISV, 5, so, mb, MO); } } while (0)
#define NOP0() asm volatile("s_nop 0" ::: "memory")
    const unsigned so0 = (unsigned)(32 * (n - 1 - sig)) * KSTRIDE;

    STAMP(2);
    ELEMN(false, so0, mW, 0);
    const unsigned qslot = (S == 1) ? lds0 + (2 + w) * SLOT : lds0 + (3 * hh + 2) * SLOT;
    {
        const unsigned qrow = (unsigned)(2 * grp + hh) * ROWB + (unsigned)pw * QSTRIDE;
        const unsigned l8 = (unsigned)(lane >> 3) * QSTRIDE;
        if (S == 1) {
            const unsigned vq0 = l8 + 16u * ((lane & 7) ^ (lane >> 4)), vq1 = l8 + 8 * QSTRIDE + 16u * ((lane & 7) ^ (4 | (lane >> 4)));
            const unsigned so2 = qrow + 16 * QSTRIDE;
            SETM0(qslot, 0);
            dmaqc<0, 4096 - 128>(vq0, qsrd, qrow, qslot); NOP0(); dmaqc<128, 8192 - 256>(vq0, qsrd, qrow, qslot); NOP0(); dmaqc<256, 1024>(vq0, qsrd, qrow, qslot); NOP0();
            dmaqc<0, 1024 + 4096 - 128>(vq1, qsrd, qrow, qslot); NOP0(); dmaqc<128, 1024 + 8192 - 256>(vq1, qsrd, qrow, qslot); NOP0(); dmaqc<256, 2048>(vq1, qsrd, qrow, qslot); NOP0();
            dmaqc<0, 2048 + 4096 - 128>(vq0, qsrd, so2, qslot); NOP0(); dmaqc<128, 2048 + 8192 - 256>(vq0, qsrd, so2, qslot); NOP0(); dmaqc<256, 3072>(vq0, qsrd, so2, qslot); NOP0();
            dmaqc<0, 3072 + 4096 - 128>(vq1, qsrd, so2, qslot); NOP0(); dmaqc<128, 3072 + 8192 - 256>(vq1, qsrd, so2, qslot); NOP0(); dmaqe<256>(vq1, qsrd, so2);
        } else {
            const unsigned vq = l8 + (unsigned)(8 * up) * QSTRIDE + 16u * ((lane & 7) ^ ((up << 2) | (lane >> 4))), vq2 = vq + 16 * QSTRIDE;
            const unsigned qm = qslot + up * 1024;
            SETM0(qm, 0);
            dmaqc<0, 4096 - 128>(vq, qsrd, qrow, qm); NOP0(); dmaqc<128, 8192 - 256>(vq, qsrd, qrow, qm); NOP0(); dmaqc<256, 2048>(vq, qsrd, qrow, qm); NOP0();
            dmaqc<0, 2048 + 4096 - 128>(vq2, qsrd, qrow, qm); NOP0(); dmaqc<128, 2048 + 8192 - 256>(vq2, qsrd, qrow, qm); NOP0(); dmaqe<256>(vq2, qsrd, qrow);
        }
    }
    if (S == 1) ELEMN(false, so0 - TSTEP, mW, SLOT); else ELEM(false, so0 - TSTEP, mW, SLOT);

    const bool act = (S == 2) || pw < L;
    const bool late = (S == 1) && act && tdiag != n - 1;
    const int itend = (S == 1) ? n - 1 : ((n - sig + 1) >> 1) - 1;
    const unsigned kb0 = r32 * 128 + 16 * (h ^ ((r32 >> 1) & 7));
    constexpr unsigned KB = (S == 1) ? SLOT : 0;
    unsigned kA = ring + KB + kb0, kBb = kA ^ 32, kC = kA ^ 64, kD = kA ^ 96;
    const int q4 = (lane & 15) >> 2, p4 = lane & 3, blk = (lane >> 4) & 1;
    unsigned vA = ring + (4 * h + q4) * 128 + 64 * (q4 >> 1) + 32 * blk + 8 * p4, vB = vA ^ 64;
    f32x16 o[6];
#pragma unroll
    for (int c = 0; c < 6; ++c) { o[c] = f32x16{}; asm volatile("" : "+v"(o[c])); }
    float m = -1e30f, l = 0.f, alpha = 1.f, mthr, mc;
    int pend = 0;
    int dq0 = pw + r32 - 4 * h;
    asm volatile("" : "+v"(kA), "+v"(kBb), "+v"(kC), "+v"(kD), "+v"(vA), "+v"(vB), "+v"(dq0));
    f32x16 s;
    float x[16];
#define KRD(i, OFF) ldk((((i) & 3) == 0 ? kA : ((i) & 3) == 1 ? kBb : ((i) & 3) == 2 ? kC : kD) + (OFF) + ((i) >> 2) * 4096)
#define VRD(i, OFF) ldv(((((i) % 6) & 1) ? vB : vA) + (OFF) + (((i) % 6) >> 1) * 4096 + ((i) / 6) * 2048)

    wait_vm<PW>();
    STAMP(4);
    barrier();
    bf16x8 q[12];
    {
        const unsigned a0 = qslot + kb0;
#pragma unroll
        for (int i = 0; i < 12; ++i) q[i] = ldk((a0 ^ (32 * (i & 3))) + (i >> 2) * 4096);
    }
    bf16x8 kf[12];
    {
        const unsigned a0 = ring + kb0;
#pragma unroll
        for (int i = 0; i < 6; ++i) kf[i] = ldk((a0 ^ (32 * (i & 3))) + (i >> 2) * 4096);
    }
    barrier();
    ELEMN(true, so0, mW, 2 * SLOT);
    if (S == 1) {
        ELEM(false, so0 - 2 * TSTEP, mW, 3 * SLOT);
        ELEMN(true, so0 - TSTEP, mW, 4 * SLOT);
        ELEM(false, so0 - 3 * TSTEP, mW, 5 * SLOT);
    }
    STAMP(5);

#define GATE(mb, MO) do { __builtin_amdgcn_s_waitcnt(((NS - 2) * PW) | 0x70);                                                            \
        asm volatile("s_barrier\n\ts_add_u32 m0, %0, %1" :: "s"(mb), "i"(MO) : "memory", "scc"); } while (0)
#define PIN1(a) asm volatile("" : "+v"(a))
#define PIN2(a, b) asm volatile("" : "+v"(a), "+v"(b))
#define PIN4(a, b, c, d) asm volatile("" : "+v"(a), "+v"(b), "+v"(c), "+v"(d))
    auto mask_diag = [&]() {
        int dq = dq0 - 32 * tdiag;
        asm volatile("" : "+v"(dq));
#pragma unroll
        for (int i = 0; i < 16; ++i) { const int c = (i & 3) + 8 * (i >> 2); s[i] = (c <= dq) ? s[i] : -__builtin_inff(); }
    };

    if (act && !late) {
        const unsigned a0 = ring + kb0;
#pragma unroll
        for (int i = 6; i < 12; ++i) kf[i] = ldk((a0 ^ (32 * (i & 3))) + (i >> 2) * 4096);
        s = MFMA(kf[0], q[0], f32x16{});
#pragma unroll
        for (int i = 1; i < 12; ++i) s = MFMA(kf[i], q[i], s);
    }
    GATE(mW, 0);
    constexpr unsigned KS0 = (S == 1) ? 0 : SLOT;
    kf[0] = KRD(0, KS0);
    PIECE(S == 1, 0, so0 - 2 * TSTEP, mW, 0); kf[1] = KRD(1, KS0);
    PIECE(S == 1, 1, so0 - 2 * TSTEP, mW, 0); kf[2] = KRD(2, KS0);
    PIECE(S == 1, 2, so0 - 2 * TSTEP, mW, 0); kf[3] = KRD(3, KS0);
    if (S == 2) PIECE(false, 3, so0 - 2 * TSTEP, mW, 0);
    kf[4] = KRD(4, KS0);
    if (S == 2) { PIECE(false, 4, so0 - 2 * TSTEP, mW, 0); NOP0(); PIECE(false, 5, so0 - 2 * TSTEP, mW, 0); }
    auto first_max = [&]() {
        float a = max3f(s[0], s[1], s[2]), b = max3f(s[8], s[9], s[10]);
        a = max3f(a, s[3], s[4]); b = max3f(b, s[11], s[12]);
        a = max3f(a, s[5], s[6]); b = max3f(b, s[13], s[14]);
        a = max3f(a, s[7], s[15]);
        m = max2f(m, swap_max(max2f(a, b)));
        mthr = m + THR_RAW; mc = -m * SCALE_LOG2E;
#pragma unroll
        for (int i = 0; i < 16; ++i) x[i] = fmaf(s[i], SCALE_LOG2E, mc);
    };
    mthr = m; mc = 0.f;
    if (act && !late) {
        if (S == 1 || sig == 0) mask_diag();
        first_max();
    }
    STAMP(6);

    int it = 0;
    unsigned soA = so0 - ((S == 1) ? 4 : 1) * TSTEP, soB = so0 - 3 * TSTEP;
    u32x4 pa, pb;
#define EXP2X(i, j) do { x[i] = __builtin_amdgcn_exp2f(x[i]); x[j] = __builtin_amdgcn_exp2f(x[j]); PIN2(x[i], x[j]); } while (0)
#define FMAX2(i, j) do { x[i] = fmaf(s[i], SCALE_LOG2E, mc); x[j] = fmaf(s[j], SCALE_LOG2E, mc); PIN2(x[i], x[j]); } while (0)
    auto iter = [&](auto PHC) {
        constexpr int PH = decltype(PHC)::value, PN = (PH + 1) % 3;
        constexpr unsigned SK = ((S == 1) ? 1 + 2 * PH : (PH == 0 ? 1 : PH == 1 ? 0 : 2)) * SLOT;
        constexpr unsigned SV = ((S == 1) ? (2 + 2 * PH) % 6 : 2 - PH) * SLOT;
        constexpr unsigned SKN = ((S == 1) ? 1 + 2 * PN : (PN == 0 ? 1 : PN == 1 ? 0 : 2)) * SLOT;
        constexpr unsigned RK = SK - KB, RKN = SKN - KB;
        float ls0, ls1;
        PIN2(kf[0], kf[1]);
        kf[5] = KRD(5, RK);
        s = MFMA(kf[0], q[0], f32x16{}); EXP2X(0, 1); SB();
        kf[6] = KRD(6, RK);
        s = MFMA(kf[1], q[1], s); EXP2X(2, 3); SB();
        PIN2(kf[2], kf[3]);
        kf[7] = KRD(7, RK);
        s = MFMA(kf[2], q[2], s); EXP2X(4, 5); SB();
        kf[8] = KRD(8, RK);
        s = MFMA(kf[3], q[3], s); EXP2X(6, 7); SB();
        PIN2(kf[4], kf[5]);
        kf[9] = KRD(9, RK);
        s = MFMA(kf[4], q[4], s); EXP2X(8, 9); ls0 = x[0] + x[2]; ls1 = x[1] + x[3]; PIN2(ls0, ls1); SB();
        kf[10] = KRD(10, RK); kf[11] = KRD(11, RK);
        s = MFMA(kf[5], q[5], s); EXP2X(10, 11); ls0 += x[4]; ls1 += x[5]; PIN2(ls0, ls1); SB();
        PIN2(kf[6], kf[7]);
        s = MFMA(kf[6], q[6], s); EXP2X(12, 13); pa[0] = cvtpk(x[0], x[1]); pa[1] = cvtpk(x[2], x[3]); PIN2(pa[0], pa[1]); SB();
        s = MFMA(kf[7], q[7], s); EXP2X(14, 15); pa[2] = cvtpk(x[4], x[5]); pa[3] = cvtpk(x[6], x[7]); PIN2(pa[2], pa[3]); SB();
        GATE(mW, SK);
        bf16x8 vf[12];
        vf[0] = VRD(0, SV); SB();
        s = MFMA(kf[8], q[8], s); vf[1] = VRD(1, SV); ls0 += x[6]; ls1 += x[7]; PIN2(ls0, ls1); SB();
        s = MFMA(kf[9], q[9], s); vf[2] = VRD(2, SV); PIECE(S == 2, 0, soA, mW, SK); ls0 += x[8]; ls1 += x[9]; PIN2(ls0, ls1); SB();
        s = MFMA(kf[10], q[10], s); vf[3] = VRD(3, SV); PIECE(S == 2, 1, soA, mW, SK); ls0 += x[10]; ls1 += x[11]; PIN2(ls0, ls1); SB();
        s = MFMA(kf[11], q[11], s); vf[4] = VRD(4, SV); PIECE(S == 2, 2, soA, mW, SK); ls0 += x[12]; ls1 += x[13]; PIN2(ls0, ls1); SB();
        const bf16x8 p0f = __builtin_bit_cast(bf16x8, pa);
        PIN2(vf[0], vf[1]);
        vf[5] = VRD(5, SV);
        o[0] = MFMA(vf[0], p0f, o[0]); if (S == 2) PIECE(true, 3, soA, mW, SK);
        pb[0] = cvtpk(x[8], x[9]); pb[1] = cvtpk(x[10], x[11]); PIN2(pb[0], pb[1]); ls0 += x[14]; ls1 += x[15]; PIN2(ls0, ls1); SB();
        vf[6] = VRD(6, SV);
        o[1] = MFMA(vf[1], p0f, o[1]); if (S == 2) PIECE(true, 4, soA, mW, SK);
        pb[2] = cvtpk(x[12], x[13]); pb[3] = cvtpk(x[14], x[15]); PIN2(pb[2], pb[3]); l += ls0; PIN1(l); SB();
        PIN2(vf[2], vf[3]);
        vf[7] = VRD(7, SV);
        o[2] = MFMA(vf[2], p0f, o[2]); if (S == 2) PIECE(true, 5, soA, mW, SK);
        l += ls1; PIN1(l); SB();
        const bf16x8 p1f = __builtin_bit_cast(bf16x8, pb);
        vf[8] = VRD(8, SV);
        o[3] = MFMA(vf[3], p0f, o[3]); SB();
        float ma = max3f(s[0], s[1], s[2]), mb = max3f(s[8], s[9], s[10]);
        ma = max3f(ma, s[3], s[4]); mb = max3f(mb, s[11], s[12]);
        ma = max3f(ma, s[5], s[6]); mb = max3f(mb, s[13], s[14]); PIN2(ma, mb); SB();
        PIN2(vf[4], vf[5]);
        vf[9] = VRD(9, SV);
        o[4] = MFMA(vf[4], p0f, o[4]);
        ma = max3f(ma, s[7], s[15]);
        float pm = swap_max(max2f(ma, mb)); PIN1(pm); SB();
        vf[10] = VRD(10, SV); vf[11] = VRD(11, SV);
        o[5] = MFMA(vf[5], p0f, o[5]); SB();
        if (__builtin_expect(__any(pm > mthr), 0)) {
            const float mn = max2f(m, pm);
            alpha = __builtin_amdgcn_exp2f((m - mn) * SCALE_LOG2E);
            m = mn; mthr = m + THR_RAW; mc = -m * SCALE_LOG2E; pend = 1;
        }
        PIN2(vf[6], vf[7]);
        o[0] = MFMA(vf[6], p1f, o[0]); FMAX2(0, 1); FMAX2(2, 3); FMAX2(4, 5); SB();
        o[1] = MFMA(vf[7], p1f, o[1]); FMAX2(6, 7); FMAX2(8, 9); FMAX2(10, 11); SB();
        GATE(mW, SV);
        kf[0] = KRD(0, RKN); SB();
        o[2] = MFMA(vf[8], p1f, o[2]); kf[1] = KRD(1, RKN); PIECE(S == 1, 0, soB, mW, SV); FMAX2(12, 13); SB();
        o[3] = MFMA(vf[9], p1f, o[3]); kf[2] = KRD(2, RKN); PIECE(S == 1, 1, soB, mW, SV); FMAX2(14, 15); SB();
        o[4] = MFMA(vf[10], p1f, o[4]); PIECE(S == 1, 2, soB, mW, SV); kf[3] = KRD(3, RKN); if (S == 2) PIECE(false, 3, soB, mW, SV); SB();
        o[5] = MFMA(vf[11], p1f, o[5]); if (S == 2) PIECE(false, 4, soB, mW, SV); kf[4] = KRD(4, RKN); if (S == 2) PIECE(false, 5, soB, mW, SV); SB();
        soA -= TSTEP; soB -= TSTEP;
        if (__builtin_expect(pend, 0)) {
            l *= alpha;
#pragma unroll
            for (int c = 0; c < 6; ++c) o[c] *= alpha;
            pend = 0;
        }
        ++it;
    };
    bool skip0 = false;
    if (S == 1 && late) {
        constexpr unsigned SK = SLOT, SV = 2 * SLOT, RKN = 3 * SLOT - KB;
        kf[5] = KRD(5, 0); kf[6] = KRD(6, 0); kf[7] = KRD(7, 0); kf[8] = KRD(8, 0);
        s = MFMA(kf[0], q[0], f32x16{});
        kf[9] = KRD(9, 0); kf[10] = KRD(10, 0); kf[11] = KRD(11, 0);
#pragma unroll
        for (int i = 1; i < 8; ++i) s = MFMA(kf[i], q[i], s);
        GATE(mW, SK);
        s = MFMA(kf[8], q[8], s); SB();
        PIECE(false, 0, soA, mW, SK); s = MFMA(kf[9], q[9], s); SB();
        PIECE(false, 1, soA, mW, SK); s = MFMA(kf[10], q[10], s); SB();
        PIECE(false, 2, soA, mW, SK); s = MFMA(kf[11], q[11], s); SB();
        GATE(mW, SV);
        kf[0] = KRD(0, RKN); PIECE(true, 0, soB, mW, SV); kf[1] = KRD(1, RKN); PIECE(true, 1, soB, mW, SV); kf[2] = KRD(2, RKN); PIECE(true, 2, soB, mW, SV);
        kf[3] = KRD(3, RKN); kf[4] = KRD(4, RKN);
        mask_diag();
        first_max();
        soA -= TSTEP; soB -= TSTEP;
        it = 1; skip0 = true;
    }
    if (act && it < itend) {
        for (;;) {
            if (!skip0) { iter(IC<0>{}); if (it >= itend) break; }
            skip0 = false;
            iter(IC<1>{}); if (it >= itend) break;
            iter(IC<2>{}); if (it >= itend) break;
        }
    }
    STAMP(7);
    const int ph = it % 3;
    unsigned tk = ((S == 1) ? 1 + 2 * ph : (ph == 0 ? 1 : ph == 1 ? 0 : 2)) * SLOT;
    unsigned tv = ((S == 1) ? (ph == 2 ? 0 : 2 + 2 * ph) : 2 - ph) * SLOT;
    auto adv2 = [&](unsigned a) { const unsigned t = a + 2 * SLOT; return t >= NS * SLOT ? t - NS * SLOT : t; };
    if (act) {
        const unsigned mk = mW + tk, mv = mW + tv;
        GATE(mk, 0);
        const unsigned va = vA + tv, vb = vB + tv;
        bf16x8 vf[12];
#define VRT(i) ldv(((((i) % 6) & 1) ? vb : va) + (((i) % 6) >> 1) * 4096 + ((i) / 6) * 2048)
        vf[0] = VRT(0); PIECE(S == 2, 0, soA, mk, 0); vf[1] = VRT(1); PIECE(S == 2, 1, soA, mk, 0); vf[2] = VRT(2); PIECE(S == 2, 2, soA, mk, 0);
        vf[3] = VRT(3); if (S == 2) PIECE(true, 3, soA, mk, 0); vf[4] = VRT(4); if (S == 2) PIECE(true, 4, soA, mk, 0); vf[5] = VRT(5); if (S == 2) PIECE(true, 5, soA, mk, 0);
        float ls0 = 0.f, ls1 = 0.f;
#pragma unroll
        for (int i = 0; i < 16; i += 2) { x[i] = __builtin_amdgcn_exp2f(x[i]); x[i + 1] = __builtin_amdgcn_exp2f(x[i + 1]); ls0 += x[i]; ls1 += x[i + 1]; }
        l += ls0 + ls1;
#pragma unroll
        for (int i = 0; i < 4; ++i) { pa[i] = cvtpk(x[2 * i], x[2 * i + 1]); pb[i] = cvtpk(x[8 + 2 * i], x[9 + 2 * i]); }
        PIN4(pa[0], pa[1], pa[2], pa[3]); PIN4(pb[0], pb[1], pb[2], pb[3]);
        const bf16x8 p0f = __builtin_bit_cast(bf16x8, pa), p1f = __builtin_bit_cast(bf16x8, pb);
        SB();
        vf[6] = VRT(6); vf[7] = VRT(7);
        o[0] = MFMA(vf[0], p0f, o[0]); SB();
        vf[8] = VRT(8); vf[9] = VRT(9);
        o[1] = MFMA(vf[1], p0f, o[1]); SB();
        vf[10] = VRT(10); vf[11] = VRT(11);
        o[2] = MFMA(vf[2], p0f, o[2]); SB();
        o[3] = MFMA(vf[3], p0f, o[3]); o[4] = MFMA(vf[4], p0f, o[4]); o[5] = MFMA(vf[5], p0f, o[5]);
        o[0] = MFMA(vf[6], p1f, o[0]); o[1] = MFMA(vf[7], p1f, o[1]); SB();
        if (it + 1 < steps) {
            GATE(mv, 0);
            NOP0();
            PIECE(S == 1, 0, soB, mv, 0); NOP0(); PIECE(S == 1, 1, soB, mv, 0); NOP0(); PIECE(S == 1, 2, soB, mv, 0);
            if (S == 2) { NOP0(); PIECE(false, 3, soB, mv, 0); NOP0(); PIECE(false, 4, soB, mv, 0); NOP0(); PIECE(false, 5, soB, mv, 0); }
        }
        o[2] = MFMA(vf[8], p1f, o[2]); o[3] = MFMA(vf[9], p1f, o[3]); o[4] = MFMA(vf[10], p1f, o[4]); o[5] = MFMA(vf[11], p1f, o[5]);
        tk = adv2(tk); tv = adv2(tv); soA -= TSTEP; soB -= TSTEP;
        ++it;
    }
    STAMP(8);
    for (; it < steps; ++it) {
        const unsigned mk = mW + tk, mv = mW + tv;
        GATE(mk, 0);
        NOP0();
        PIECE(S == 2, 0, soA, mk, 0); NOP0(); PIECE(S == 2, 1, soA, mk, 0); NOP0(); PIECE(S == 2, 2, soA, mk, 0);
        if (S == 2) { NOP0(); PIECE(true, 3, soA, mk, 0); NOP0(); PIECE(true, 4, soA, mk, 0); NOP0(); PIECE(true, 5, soA, mk, 0); }
        if (it + 1 < steps) {
            GATE(mv, 0);
            NOP0();
            PIECE(S == 1, 0, soB, mv, 0); NOP0(); PIECE(S == 1, 1, soB, mv, 0); NOP0(); PIECE(S == 1, 2, soB, mv, 0);
            if (S == 2) { NOP0(); PIECE(false, 3, soB, mv, 0); NOP0(); PIECE(false, 4, soB, mv, 0); NOP0(); PIECE(false, 5, soB, mv, 0); }
        }
        tk = adv2(tk); tv = adv2(tv); soA -= TSTEP; soB -= TSTEP;
    }

    STAMP(10);
    l = swap_add(l);
    const __amdgpu_buffer_rsrc_t orsrc = __builtin_amdgcn_make_buffer_rsrc((void*)(op + (size_t)s0 * QSTRIDE), 0, (unsigned)L * QSTRIDE, 0x00020000);
    const unsigned stw = r32 * ORS + 8 * h;
    auto stage_block = [&](unsigned base, int c, const f32x16& z) {
#pragma unroll
        for (int g = 0; g < 4; ++g) {
            u32x2 v = {cvtpk(z[4 * g], z[4 * g + 1]), cvtpk(z[4 * g + 2], z[4 * g + 3])};
            *(LDSP(u32x2))(size_t)(base + (32 * c + 8 * g) * 2) = v;
        }
    };
    unsigned lrd[3], gof[3];
#pragma unroll
    for (int j = 0; j < 3; ++j) { const unsigned idx = 64 * j + lane, row = (idx * 2731) >> 16, ch = idx - 24 * row; lrd[j] = row * ORS + ch * 16; gof[j] = row * QSTRIDE + ch * 16; }
    wait_vm<0>();
    barrier();
    STAMP(11);
    if (S == 1) {
        if (act) {
            const unsigned stg = lds0 + w * (32 * ORS);
            const float inv = __builtin_amdgcn_rcpf(l);
#pragma unroll
            for (int c = 0; c < 6; ++c) { const f32x16 z = o[c] * inv; stage_block(stg + stw, c, z); }
            STAMP(15);
            const unsigned obase = (unsigned)pw * QSTRIDE + (unsigned)(2 * grp + hh) * ROWB;
#pragma unroll
            for (int k = 0; k < 4; ++k)
#pragma unroll
                for (int j = 0; j < 3; ++j) {
                    const u32x4 v = *(LDSP(u32x4))(size_t)(stg + lrd[j] + 8 * k * ORS);
                    __builtin_amdgcn_raw_buffer_store_b128(v, orsrc, gof[j], obase + 8 * k * QSTRIDE, STAUX);
                }
        }
    } else {
        const unsigned mine = lds0 + w * SLOT + lane * 16, other = lds0 + (w ^ 2) * SLOT + lane * 16;
        {
            f32x2 ml = {m, l};
            *(LDSP(f32x2))(size_t)(lds0 + 4 * SLOT + w * 512 + lane * 8) = ml;
        }
        auto send = [&](const f32x16& z, int c) {
#pragma unroll
            for (int g = 0; g < 4; ++g) { f32x4 y = {z[4 * g], z[4 * g + 1], z[4 * g + 2], z[4 * g + 3]}; *(LDSP(f32x4))(size_t)(mine + (c * 4 + g) * 1024) = y; }
        };
        if (sig) { send(o[0], 0); send(o[1], 1); send(o[2], 2); } else { send(o[3], 0); send(o[4], 1); send(o[5], 2); }
        STAMP(12);
        barrier();
        STAMP(13);
        const f32x2 mlo = *(LDSP(f32x2))(size_t)(lds0 + 4 * SLOT + (w ^ 2) * 512 + lane * 8);
        const float mx = max2f(m, mlo[0]);
        const float a0 = __builtin_amdgcn_exp2f((m - mx) * SCALE_LOG2E), a1 = __builtin_amdgcn_exp2f((mlo[0] - mx) * SCALE_LOG2E);
        const float inv = __builtin_amdgcn_rcpf(l * a0 + mlo[1] * a1);
        const float f0 = a0 * inv, f1 = a1 * inv;
        const unsigned stg = lds0 + 51200 + hh * (32 * ORS);
        auto merge = [&](const f32x16& xo, int c, int cd) {
            f32x16 z;
#pragma unroll
            for (int g = 0; g < 4; ++g) {
                const f32x4 y = *(LDSP(f32x4))(size_t)(other + (c * 4 + g) * 1024);
                const f32x4 t = {xo[4 * g], xo[4 * g + 1], xo[4 * g + 2], xo[4 * g + 3]};
                const f32x4 u = t * f0 + y * f1;
                z[4 * g] = u[0]; z[4 * g + 1] = u[1]; z[4 * g + 2] = u[2]; z[4 * g + 3] = u[3];
            }
            stage_block(stg + stw, cd, z);
        };
        if (sig) { merge(o[3], 0, 3); merge(o[4], 1, 4); merge(o[5], 2, 5); } else { merge(o[0], 0, 0); merge(o[1], 1, 1); merge(o[2], 2, 2); }
        STAMP(14);
        barrier();
        STAMP(15);
        const unsigned obase = (unsigned)(pw + 16 * sig) * QSTRIDE + (unsigned)(2 * grp + hh) * ROWB;
        const unsigned stgr = stg + 16 * sig * ORS;
#pragma unroll
        for (int k = 0; k < 2; ++k)
#pragma unroll
            for (int j = 0; j < 3; ++j) {
                const u32x4 v = *(LDSP(u32x4))(size_t)(stgr + lrd[j] + 8 * k * ORS);
                __builtin_amdgcn_raw_buffer_store_b128(v, orsrc, gof[j], obase + 8 * k * QSTRIDE, STAUX);
            }
    }
    STAMP(16);
}

template <int CTRL> DI int dpp_mov(int old, int x) { return __builtin_amdgcn_update_dpp(old, x, CTRL, 0xf, 0xf, false); }
DI int wave_incl_scan(int x) {
    x += dpp_mov<0x111>(0, x);
    x += dpp_mov<0x112>(0, x);
    x += dpp_mov<0x114>(0, x);
    x += dpp_mov<0x118>(0, x);
    x += __builtin_amdgcn_update_dpp(0, x, 0x142, 0xa, 0xf, false);
    x += __builtin_amdgcn_update_dpp(0, x, 0x143, 0xc, 0xf, false);
    return x;
}
DI int wave_max_nonneg(int x) {
    x = max(x, dpp_mov<0x111>(0, x));
    x = max(x, dpp_mov<0x112>(0, x));
    x = max(x, dpp_mov<0x114>(0, x));
    x = max(x, dpp_mov<0x118>(0, x));
    x = max(x, __builtin_amdgcn_update_dpp(0, x, 0x142, 0xa, 0xf, false));
    x = max(x, __builtin_amdgcn_update_dpp(0, x, 0x143, 0xc, 0xf, false));
    return __builtin_amdgcn_readlane(x, 63);
}

DI bool decode_slow(const int* __restrict__ cu, int nseq, int k, int& s0, int& L, int& p0, int& isx) {
    int nmax = 0;
    for (int s = 0; s < nseq; ++s) nmax = max(nmax, (cu[s + 1] - cu[s] + 31) >> 5);
    const int B = BSPLIT;
    int N = 0;
    for (int s = 0; s < nseq; ++s) { const int n = (cu[s + 1] - cu[s] + 31) >> 5; N += max(n - B, 0) + min(B >> 1, (n + 1) >> 1); }
    int rank = k;
    if (k < CUS_PER_XCD) { const int N2 = min(max(N - CUS_PER_XCD, 0), CUS_PER_XCD); rank = (k < N2) ? CUS_PER_XCD - 1 - k : k - N2; }
    if (rank >= N) return false;
    int jx = nmax, my = min(B >> 1, (nmax + 1) >> 1);
    for (;;) {
        const int x = (jx > B) && (((jx + 1) >> 1) >= 2 * my);
        const int thr = x ? 32 * (jx - 1) : 64 * (my - 1);
        for (int s = 0; s < nseq; ++s) {
            const int len = cu[s + 1] - cu[s];
            if (len > thr) { if (rank == 0) { s0 = cu[s]; L = len; p0 = thr; isx = x; return true; } --rank; }
        }
        if (x) --jx; else --my;
    }
}

__global__ __launch_bounds__(256, 2) void attn_varlen_kernel(const char* __restrict__ qp, const char* __restrict__ kp, const char* __restrict__ vp,
                                                             const int* __restrict__ cu, char* __restrict__ op, int nseq, int T
                                                             ) {
    __shared__ __attribute__((aligned(16))) char smem_[LDS_BYTES];
    if (CODEPF > 0 && (blockIdx.x >> 3) < CODEWG) {
        unsigned long long pc; asm volatile("s_getpc_b64 %0" : "=s"(pc));
        const char* cb = (const char*)(pc & ~127ull);
        const unsigned mscr0 = rfl((unsigned)(size_t)(lds_char_ptr)smem_ + 73728 + 1024 + (threadIdx.x >> 6) * 256);
        asm volatile("s_mov_b32 m0, %0" :: "s"(mscr0) : "memory");
        for (unsigned o = threadIdx.x * 128; o < CODEPF; o += 256 * 128)
            asm volatile("global_load_lds_dword %0, off" :: "v"(cb + o) : "memory");
    }
    const int grp = blockIdx.x & 7;
    const int kraw = blockIdx.x >> 3;
    const int lane = threadIdx.x & 63;

    asm volatile("" :: "s"(qp), "s"(kp), "s"(vp), "s"(op), "s"(cu), "s"(nseq));
    const int k = kraw;
    typedef unsigned u32x16 __attribute__((ext_vector_type(16)));
    u32x16 cus;
    {
        const u32x4 cr = make_srd(cu, (unsigned)(nseq + 1) * 4);
        asm volatile("s_buffer_load_dwordx16 %0, %1, 0" : "=s"(cus) : "s"(cr) : "memory");
    }
    if (EARLY != 0 && k < 64) {
        const unsigned mscr = rfl((unsigned)(size_t)(lds_char_ptr)smem_ + 73728 + (threadIdx.x >> 6) * 256);
        asm volatile("s_mov_b32 m0, %0" :: "s"(mscr) : "memory");
        const u32x4 qr = make_srd(qp + grp * 2 * ROWB, (unsigned)(T - 1) * QSTRIDE + 2 * ROWB);
        const u32x4 kr = make_srd(kp + grp * ROWB, (unsigned)(T - 1) * KSTRIDE + ROWB);
        asm volatile("s_nop 4" :: "s"(qr), "s"(kr) : "memory");
        for (unsigned base = 0; base < (unsigned)T * 6; base += 64 * 256) {
            const unsigned j = base + k * 256 + threadIdx.x;
            if (EARLY & 1) { const unsigned t6 = j / 6; const unsigned offq = t6 * QSTRIDE + (j - 6 * t6) * 128;
                asm volatile("buffer_load_dword %0, %1, 0 offen " EPOL " lds" :: "v"(offq), "s"(qr) : "memory"); }
            if (EARLY & 2) { const unsigned t3 = j / 3; const unsigned offk = t3 * KSTRIDE + (j - 3 * t3) * 128;
                asm volatile("buffer_load_dword %0, %1, 0 offen " EPOL " lds" :: "v"(offk), "s"(kr) : "memory"); }
        }
    }
    const int l3 = (lane * 43) >> 7, r3 = lane - 3 * l3, st = l3 + 1;
    const int jx = 2 * st - 1 + r3;
    const bool okx = (r3 < 2) & (jx > BSPLIT) & (lane < 63), oky = (r3 == 2) & !(st & 1) & (st <= BSPLIT);
    int thr = oky ? 32 * (st - 2) : 0x7fffffff;
    thr = okx ? 32 * (jx - 1) : thr;
    asm volatile("s_waitcnt lgkmcnt(0)" : "+s"(cus) :: "memory");
    int len = 0, c0 = 0, cnt = 0;
#define SEQ(s) do { const int ls = (int)cus[(s) + 1] - (int)cus[s];                         \
        cnt += (ls > thr) ? 1 : 0;                                                                                                      \
        asm("v_writelane_b32 %0, %1, %2" : "+v"(len) : "s"(ls), "i"(s)); asm("v_writelane_b32 %0, %1, %2" : "+v"(c0) : "s"(cus[s]), "i"(s)); } while (0)
    SEQ(0); SEQ(1); SEQ(2); SEQ(3); SEQ(4); SEQ(5); SEQ(6); SEQ(7);
    if (__builtin_expect(nseq > 8, 0)) { SEQ(8); SEQ(9); SEQ(10); SEQ(11); SEQ(12); SEQ(13); SEQ(14); }
    const int incl = wave_incl_scan(cnt);
    const int N = __builtin_amdgcn_readlane(incl, 63);
    const int N2 = min(max(N - CUS_PER_XCD, 0), CUS_PER_XCD);
    (void)N2;
    const int rank = (k >= CUS_PER_XCD && N <= 2 * CUS_PER_XCD) ? (k < N ? N - 1 - (k - CUS_PER_XCD) : N) : k;
    bool valid = rank < N;
    const int a = N - rank;
    const int lv = (__ffsll((long long)__ballot(incl >= a)) - 1) & 63;
    const int idx = __builtin_amdgcn_readlane(incl, lv) - a;
    int p0 = __builtin_amdgcn_readlane(thr, lv);
    int isx = __builtin_amdgcn_readlane(r3, lv) < 2;
    const bool has = len > p0;
    const unsigned long long mk = __ballot(has);
    const int below = __builtin_amdgcn_mbcnt_hi((unsigned)(mk >> 32), __builtin_amdgcn_mbcnt_lo((unsigned)mk, 0));
    const int sq = (__ffsll((long long)__ballot(has && below == idx)) - 1) & 63;
    int s0 = __builtin_amdgcn_readlane(c0, sq), L = __builtin_amdgcn_readlane(len, sq);
    if (__builtin_expect(nseq > 15 || __ballot(len > 32 * 42) != 0, 0)) {
        int a0 = 0, a1 = 0, a2 = 0, a3 = 0;
        const int v = decode_slow(cu, nseq, k, a0, a1, a2, a3);
        valid = rfl(v) != 0; s0 = rfl(a0); L = rfl(a1); p0 = rfl(a2); isx = rfl(a3);
    }
    if (!valid) return;
    s0 = rfl(s0); L = rfl(L); p0 = rfl(p0); isx = rfl(isx);
    if (PRIO && k < CUS_PER_XCD) __builtin_amdgcn_s_setprio(PRIO);
    const unsigned lds0 = (unsigned)(size_t)(lds_char_ptr)smem_;
    if (isx) {
        const int n = (min(p0 + 32, L) + 31) >> 5;
        attn_item<2>(qp, kp, vp, op, grp, s0, L, p0, n, lds0 STPASS);
    } else {
        const int n = (min(p0 + 64, L) + 31) >> 5;
        attn_item<1>(qp, kp, vp, op, grp, s0, L, p0, n, lds0 STPASS);
    }
}

extern "C" void kernel_launch(void* const* d_in, const int* in_sizes, int n_in, void* d_out, int out_size, void* d_ws, size_t ws_size,
                              hipStream_t stream) {
    (void)n_in; (void)out_size; (void)d_ws; (void)ws_size;
    const int T = in_sizes[0] / (HQ * D);
    const int nseq = in_sizes[3] - 1;
    const int per_head = T / 32 + nseq + 1;
    attn_varlen_kernel<<<dim3(8 * per_head), dim3(256), 0, stream>>>((const char*)d_in[0], (const char*)d_in[1], (const char*)d_in[2],
                                                                    (const int*)d_in[3], (char*)d_out, nseq, T
                                                                    );
}
```

```cpp
#include <hip/hip_runtime.h>
#include <hip/hip_bf16.h>
#include <stdint.h>

typedef __bf16 bf16x8 __attribute__((ext_vector_type(8)));
typedef __bf16 bf16x2 __attribute__((ext_vector_type(2)));
typedef short s16x4 __attribute__((ext_vector_type(4)));
typedef float f32x2 __attribute__((ext_vector_type(2)));
typedef float f32x4 __attribute__((ext_vector_type(4)));
typedef float f32x16 __attribute__((ext_vector_type(16)));
typedef int i32x4 __attribute__((ext_vector_type(4)));
typedef unsigned u32x2 __attribute__((ext_vector_type(2)));
typedef unsigned u32x4 __attribute__((ext_vector_type(4)));
#define LDSP(T) __attribute__((address_space(3))) T*
#define DI __device__ __forceinline__

constexpr int HQ = 16, HKV = 8, D = 192;
constexpr unsigned QSTRIDE = HQ * D * 2;
constexpr unsigned KSTRIDE = HKV * D * 2;
constexpr unsigned ROWB = D * 2;
constexpr unsigned SLOT = 32 * ROWB;
constexpr unsigned ORS = 400;
constexpr unsigned LDS_BYTES = 77824;
constexpr float SCALE_LOG2E = 0.07216878364870322f * 1.4426950408889634f;
constexpr float THR_RAW = 8.0f / SCALE_LOG2E;
constexpr int CUS_PER_XCD = 32;
constexpr int STAUX = 16;
constexpr int CODEPF = 24576;
constexpr int DP3 = 6, DP2 = 4, DP1 = 2;
constexpr int BSPLIT = 8;

DI int rfl(int x) { return __builtin_amdgcn_readfirstlane(x); }
DI unsigned cvtpk(float lo, float hi) { f32x2 v = {lo, hi}; return __builtin_bit_cast(unsigned, __builtin_convertvector(v, bf16x2)); }
DI float max2f(float a, float b) { return __builtin_elementwise_maximum(a, b); }
DI float max3f(float a, float b, float c) { return max2f(max2f(a, b), c); }
DI float swap_max(float x) {
    auto r = __builtin_amdgcn_permlane32_swap(__float_as_uint(x), __float_as_uint(x), false, false);
    return max2f(__uint_as_float(r[0]), __uint_as_float(r[1]));
}
DI float swap_add(float x) {
    auto r = __builtin_amdgcn_permlane32_swap(__float_as_uint(x), __float_as_uint(x), false, false);
    return __uint_as_float(r[0]) + __uint_as_float(r[1]);
}
DI u32x4 make_srd(const void* base, unsigned nbytes) {
    unsigned long long a = (unsigned long long)base;
    u32x4 s; s[0] = rfl((unsigned)a); s[1] = rfl((unsigned)(a >> 32) & 0xffffu); s[2] = rfl(nbytes); s[3] = 0x00020000u;
    return s;
}
template <int IMM, int NEXT> DI void dma_c(unsigned voff, u32x4 srd, unsigned soff, unsigned nbase) {
    asm volatile("buffer_load_dwordx4 %0, %1, %2 offen offset:%3 lds\n\ts_add_u32 m0, %4, %5"
                 :: "v"(voff), "s"(srd), "s"(soff), "i"(IMM), "s"(nbase), "i"(NEXT) : "memory", "scc");
}
template <int IMM> DI void dma_e(unsigned voff, u32x4 srd, unsigned soff) {
    asm volatile("buffer_load_dwordx4 %0, %1, %2 offen offset:%3 lds" :: "v"(voff), "s"(srd), "s"(soff), "i"(IMM) : "memory");
}
template <int N> DI void wait_vm() { asm volatile("s_waitcnt vmcnt(%0)" :: "i"(N) : "memory"); }
DI void barrier() { asm volatile("s_waitcnt lgkmcnt(0)\n\ts_barrier" ::: "memory"); }
#define SB() __builtin_amdgcn_sched_barrier(0)
#define MFMA(a, b, c) __builtin_amdgcn_mfma_f32_32x32x16_bf16((a), (b), (c), 0, 0, 0)
#define NOP0() asm volatile("s_nop 0" ::: "memory")
#define SETM0(mb) asm volatile("s_mov_b32 m0, %0\n\ts_nop 0" :: "s"(mb) : "memory")
#define PIN1(a) asm volatile("" : "+v"(a))
#define PIN2(a, b) asm volatile("" : "+v"(a), "+v"(b))
#define PIN4(a, b, c, d) asm volatile("" : "+v"(a), "+v"(b), "+v"(c), "+v"(d))

DI bf16x8 ldk(unsigned addr) { return *(LDSP(bf16x8))(size_t)addr; }
DI bf16x8 ldv(unsigned addr) {
    const s16x4 lo = __builtin_amdgcn_ds_read_tr16_b64_v4i16((LDSP(s16x4))(size_t)addr);
    const s16x4 hi = __builtin_amdgcn_ds_read_tr16_b64_v4i16((LDSP(s16x4))(size_t)(addr + 1024));
    return __builtin_bit_cast(bf16x8, __builtin_shufflevector(lo, hi, 0, 1, 2, 3, 4, 5, 6, 7));
}
template <int V> struct IC { static constexpr int value = V; };

template <int S>
DI void attn_item(const char* __restrict__ qp, const char* __restrict__ kp, const char* __restrict__ vp, char* __restrict__ op,
                  int grp, int s0, int L, int p0, int n, unsigned lds0) {
    constexpr int PW = (S == 1) ? 3 : 6;
    constexpr unsigned TSTEP = 32 * S * KSTRIDE;
    const int tid = threadIdx.x, lane = tid & 63;
    const int w = rfl(tid >> 6);
    const int hh = w & 1, up = w >> 1;
    const int sig = (S == 1) ? 0 : up;
    const int pw = (S == 1) ? p0 + 32 * up : p0;
    const int steps = (S == 1) ? n : (n + 1) >> 1;
    const int kend = min(L, 32 * n);
    const u32x4 ksrd = make_srd(kp + (size_t)s0 * KSTRIDE + grp * ROWB, (unsigned)(kend - 1) * KSTRIDE + ROWB);
    const u32x4 vsrd = make_srd(vp + (size_t)s0 * KSTRIDE + grp * ROWB, (unsigned)(kend - 1) * KSTRIDE + ROWB);
    const u32x4 qsrd = make_srd(qp + (size_t)s0 * QSTRIDE, (unsigned)L * QSTRIDE);
    const unsigned ring = lds0 + sig * 3 * SLOT;
    const unsigned mW = ring + ((S == 1) ? w * 1024 : hh * 1024);
    const unsigned drow = (unsigned)(8 * hh + (lane >> 3) + ((S == 1) ? 16 * up : 0)) * KSTRIDE;
    const unsigned voffK = drow + 16u * ((lane & 7) ^ ((hh << 2) | (lane >> 4)));
    const unsigned voffV = drow + 16u * ((lane & 7) ^ (((lane >> 4) & 1) << 2));
    const unsigned voffK2 = voffK + 16 * KSTRIDE, voffV2 = voffV + 16 * KSTRIDE;
#define PIECE(ISV, j, so, mb) do { const u32x4 sr_ = (ISV) ? vsrd : ksrd;                                                                \
        const unsigned v1_ = (ISV) ? voffV : voffK, v2_ = (ISV) ? voffV2 : voffK2;                                                       \
        if (j == 0) dma_c<0, 4096 - 128>(v1_, sr_, so, mb);                                                                              \
        else if (j == 1) dma_c<128, 8192 - 256>(v1_, sr_, so, mb);                                                                       \
        else if (j == 2) { if (S == 1) dma_e<256>(v1_, sr_, so); else dma_c<256, 2048>(v1_, sr_, so, mb); }                               \
        else if (j == 3) dma_c<0, 2048 + 4096 - 128>(v2_, sr_, so, mb);                                                                  \
        else if (j == 4) dma_c<128, 2048 + 8192 - 256>(v2_, sr_, so, mb);                                                                \
        else dma_e<256>(v2_, sr_, so); } while (0)
#define PIECES(ISV, so, mb) do { PIECE(ISV, 0, so, mb); NOP0(); PIECE(ISV, 1, so, mb); NOP0(); PIECE(ISV, 2, so, mb);                     \
        if (S == 2) { NOP0(); PIECE(ISV, 3, so, mb); NOP0(); PIECE(ISV, 4, so, mb); NOP0(); PIECE(ISV, 5, so, mb); } } while (0)
    if (steps >= DP3) __builtin_amdgcn_s_setprio(3); else if (steps >= DP2) __builtin_amdgcn_s_setprio(2); else if (steps >= DP1) __builtin_amdgcn_s_setprio(1);
#define UPDPRIO() do { const int r_ = steps - it; if (__builtin_expect(((1u << (DP3 - 1) | 1u << (DP2 - 1) | 1u << (DP1 - 1)) >> (r_ & 31)) & 1u, 0)) {   \
        if (r_ == DP3 - 1) __builtin_amdgcn_s_setprio(2); else if (r_ == DP2 - 1) __builtin_amdgcn_s_setprio(1); else if (r_ == DP1 - 1) __builtin_amdgcn_s_setprio(0); } } while (0)
    const unsigned so0 = (unsigned)(32 * (n - 1 - sig)) * KSTRIDE;
    { SETM0(mW); PIECES(false, so0, mW); }
    const unsigned qslot = (S == 1) ? lds0 + (2 + w) * SLOT : lds0 + (3 * hh + 2) * SLOT;
    {
        const unsigned qrow = (unsigned)(2 * grp + hh) * ROWB + (unsigned)pw * QSTRIDE;
        const unsigned l8 = (unsigned)(lane >> 3) * QSTRIDE;
        if (S == 1) {
            const unsigned vq0 = l8 + 16u * ((lane & 7) ^ (lane >> 4)), vq1 = l8 + 8 * QSTRIDE + 16u * ((lane & 7) ^ (4 | (lane >> 4)));
            const unsigned so2 = qrow + 16 * QSTRIDE;
            SETM0(qslot);
            dma_c<0, 4096 - 128>(vq0, qsrd, qrow, qslot); NOP0(); dma_c<128, 8192 - 256>(vq0, qsrd, qrow, qslot); NOP0(); dma_c<256, 1024>(vq0, qsrd, qrow, qslot); NOP0();
            dma_c<0, 1024 + 4096 - 128>(vq1, qsrd, qrow, qslot); NOP0(); dma_c<128, 1024 + 8192 - 256>(vq1, qsrd, qrow, qslot); NOP0(); dma_c<256, 2048>(vq1, qsrd, qrow, qslot); NOP0();
            dma_c<0, 2048 + 4096 - 128>(vq0, qsrd, so2, qslot); NOP0(); dma_c<128, 2048 + 8192 - 256>(vq0, qsrd, so2, qslot); NOP0(); dma_c<256, 3072>(vq0, qsrd, so2, qslot); NOP0();
            dma_c<0, 3072 + 4096 - 128>(vq1, qsrd, so2, qslot); NOP0(); dma_c<128, 3072 + 8192 - 256>(vq1, qsrd, so2, qslot); NOP0(); dma_e<256>(vq1, qsrd, so2);
        } else {
            const unsigned vq = l8 + (unsigned)(8 * up) * QSTRIDE + 16u * ((lane & 7) ^ ((up << 2) | (lane >> 4))), vq2 = vq + 16 * QSTRIDE;
            const unsigned qm = qslot + up * 1024;
            SETM0(qm);
            dma_c<0, 4096 - 128>(vq, qsrd, qrow, qm); NOP0(); dma_c<128, 8192 - 256>(vq, qsrd, qrow, qm); NOP0(); dma_c<256, 2048>(vq, qsrd, qrow, qm); NOP0();
            dma_c<0, 2048 + 4096 - 128>(vq2, qsrd, qrow, qm); NOP0(); dma_c<128, 2048 + 8192 - 256>(vq2, qsrd, qrow, qm); NOP0(); dma_e<256>(vq2, qsrd, qrow);
        }
    }

    const int r32 = lane & 31, h = lane >> 5;
    const int tdiag = pw >> 5;
    const bool act = (S == 2) || pw < L;
    const bool late = (S == 1) && act && tdiag != n - 1;
    const int itend = (S == 1) ? n - 1 : ((n - sig + 1) >> 1) - 1;
    unsigned kb0 = r32 * 128 + 16 * (h ^ ((r32 >> 1) & 7));
    const int q4 = (lane & 15) >> 2, p4 = lane & 3, blk = (lane >> 4) & 1;
    unsigned vb0 = ((4 * h + q4) * 128 + 64 * (q4 >> 1) + 32 * blk + 8 * p4) ^ (64 * sig);
    f32x16 o[6];
#pragma unroll
    for (int c = 0; c < 6; ++c) { o[c] = f32x16{}; asm volatile("" : "+v"(o[c])); }
    float m = -1e30f, l = 0.f, alpha = 1.f, mthr = -1e30f, mc = 0.f;
    int pend = 0;
    int dq = pw + r32 - 4 * h - 32 * tdiag;
    asm volatile("" : "+v"(kb0), "+v"(vb0), "+v"(dq));
    f32x16 s;
    {
        const bool domask = (S == 1) || sig == 0;
#pragma unroll
        for (int i = 0; i < 16; ++i) { const int c = (i & 3) + 8 * (i >> 2); s[i] = (c <= dq || !domask) ? 0.f : -__builtin_inff(); }
        asm volatile("" : "+v"(s));
    }
    unsigned stw = (lane & 31) * ORS + 8 * (lane >> 5);
    unsigned lrd0, lrd1, lrd2, gof0, gof1, gof2;
    { unsigned idx = lane, row = (idx * 2731) >> 16, ch = idx - 24 * row; lrd0 = row * ORS + ch * 16; gof0 = row * QSTRIDE + ch * 16;
      idx = 64 + lane; row = (idx * 2731) >> 16; ch = idx - 24 * row; lrd1 = row * ORS + ch * 16; gof1 = row * QSTRIDE + ch * 16;
      idx = 128 + lane; row = (idx * 2731) >> 16; ch = idx - 24 * row; lrd2 = row * ORS + ch * 16; gof2 = row * QSTRIDE + ch * 16; }
    asm volatile("" : "+v"(stw), "+v"(lrd0), "+v"(lrd1), "+v"(lrd2), "+v"(gof0), "+v"(gof1), "+v"(gof2));
    float x[16];
#define KRD(i, a) ldk(((a) ^ (32 * ((i) & 3))) + ((i) >> 2) * 4096)
#define SETK(slot) do { k0a = (slot) + kbw; k1a = k0a ^ 32; k2a = k0a ^ 64; k3a = k0a ^ 96; } while (0)
#define KRC(i) ldk((((i) & 3) == 0 ? k0a : ((i) & 3) == 1 ? k1a : ((i) & 3) == 2 ? k2a : k3a) + ((i) >> 2) * 4096)
#define VRD(i, a, b) ldv(((((i) % 6) & 1) ? (b) : (a)) + (((i) % 6) >> 1) * 4096 + ((i) / 6) * 2048)

    wait_vm<0>();
    barrier();
    bf16x8 q[12], kf[12];
    {
        const unsigned a0 = qslot + kb0, a1 = ring + kb0;
#pragma unroll
        for (int i = 0; i < 12; ++i) q[i] = KRD(i, a0);
#pragma unroll
        for (int i = 0; i < 6; ++i) kf[i] = KRD(i, a1);
    }
    if (steps > 1) { const unsigned m1 = mW + SLOT; SETM0(m1); PIECES(false, so0 - TSTEP, m1); }
    const unsigned mV0 = mW + 2 * SLOT;
    asm volatile("s_waitcnt lgkmcnt(0)\n\ts_barrier\n\ts_mov_b32 m0, %0" :: "s"(mV0) : "memory");

#define GATE(N, mb) do { __builtin_amdgcn_s_waitcnt((N) | 0x70);                                                                         \
        asm volatile("s_barrier\n\ts_mov_b32 m0, %0" :: "s"(mb) : "memory"); } while (0)
#define GATE0(N) do { __builtin_amdgcn_s_waitcnt((N) | 0x70); asm volatile("s_barrier" ::: "memory"); } while (0)
    auto first_max = [&]() {
        float a = max3f(s[0], s[1], s[2]), b = max3f(s[8], s[9], s[10]);
        a = max3f(a, s[3], s[4]); b = max3f(b, s[11], s[12]);
        a = max3f(a, s[5], s[6]); b = max3f(b, s[13], s[14]);
        a = max3f(a, s[7], s[15]);
        m = max2f(m, swap_max(max2f(a, b)));
        mthr = m + THR_RAW; mc = -m * SCALE_LOG2E;
#pragma unroll
        for (int i = 0; i < 16; ++i) x[i] = fmaf(s[i], SCALE_LOG2E, mc);
    };

    if (act && !late) {
        const unsigned a0 = ring + kb0;
        kf[6] = KRD(6, a0);
        s = MFMA(kf[0], q[0], s); PIECE(true, 0, so0, mV0); kf[7] = KRD(7, a0); SB();
        s = MFMA(kf[1], q[1], s); PIECE(true, 1, so0, mV0); kf[8] = KRD(8, a0); SB();
        s = MFMA(kf[2], q[2], s); PIECE(true, 2, so0, mV0); kf[9] = KRD(9, a0); SB();
        s = MFMA(kf[3], q[3], s); if (S == 2) PIECE(true, 3, so0, mV0); kf[10] = KRD(10, a0); SB();
        s = MFMA(kf[4], q[4], s); if (S == 2) PIECE(true, 4, so0, mV0); kf[11] = KRD(11, a0); SB();
        s = MFMA(kf[5], q[5], s); if (S == 2) PIECE(true, 5, so0, mV0); SB();
#pragma unroll
        for (int i = 6; i < 12; ++i) s = MFMA(kf[i], q[i], s);
        first_max();
    } else {
        NOP0();
        PIECES(true, so0, mV0);
    }
    unsigned sK = mW + SLOT, sV = mW + 2 * SLOT, sN = mW;
    const unsigned kbw = kb0 - (mW - ring), vbw = vb0 - (mW - ring);
    unsigned k0a, k1a, k2a, k3a;
    unsigned soA = so0 - TSTEP, soB = so0 - 3 * TSTEP;
    if (steps > 1) {
        GATE(PW, mW);
        SETK(sK);
        kf[0] = KRC(0); kf[1] = KRC(1); kf[2] = KRC(2); kf[3] = KRC(3); kf[4] = KRC(4);
        if (steps > 2) PIECES(false, so0 - 2 * TSTEP, mW);
    }

    int it = 0;
    u32x4 pa, pb;
#define EXP2X(i, j) do { x[i] = __builtin_amdgcn_exp2f(x[i]); x[j] = __builtin_amdgcn_exp2f(x[j]); PIN2(x[i], x[j]); } while (0)
#define FMAX2(i, j) do { x[i] = fmaf(s[i], SCALE_LOG2E, mc); x[j] = fmaf(s[j], SCALE_LOG2E, mc); PIN2(x[i], x[j]); } while (0)
    auto iter = [&](auto KC) {
        constexpr int KIND = decltype(KC)::value;
        float ls0, ls1;
        PIN2(kf[0], kf[1]);
        kf[5] = KRC(5);
        s = MFMA(kf[0], q[0], f32x16{}); EXP2X(0, 1); SB();
        kf[6] = KRC(6);
        s = MFMA(kf[1], q[1], s); EXP2X(2, 3); SB();
        PIN2(kf[2], kf[3]);
        kf[7] = KRC(7);
        s = MFMA(kf[2], q[2], s); EXP2X(4, 5); SB();
        kf[8] = KRC(8);
        s = MFMA(kf[3], q[3], s); EXP2X(6, 7); SB();
        PIN2(kf[4], kf[5]);
        kf[9] = KRC(9);
        s = MFMA(kf[4], q[4], s); EXP2X(8, 9); ls0 = x[0] + x[2]; ls1 = x[1] + x[3]; PIN2(ls0, ls1); SB();
        kf[10] = KRC(10); kf[11] = KRC(11);
        s = MFMA(kf[5], q[5], s); EXP2X(10, 11); ls0 += x[4]; ls1 += x[5]; PIN2(ls0, ls1); SB();
        PIN2(kf[6], kf[7]);
        s = MFMA(kf[6], q[6], s); EXP2X(12, 13); pa[0] = cvtpk(x[0], x[1]); pa[1] = cvtpk(x[2], x[3]); PIN2(pa[0], pa[1]); SB();
        s = MFMA(kf[7], q[7], s); EXP2X(14, 15); pa[2] = cvtpk(x[4], x[5]); pa[3] = cvtpk(x[6], x[7]); PIN2(pa[2], pa[3]); SB();
        const unsigned mA = sK;
        GATE(KIND == 2 ? 0 : PW, mA);
        const unsigned va = sV + vbw, vb = va ^ 64;
        bf16x8 vf[12];
        vf[0] = VRD(0, va, vb); SB();
        s = MFMA(kf[8], q[8], s); vf[1] = VRD(1, va, vb); ls0 += x[6]; ls1 += x[7]; PIN2(ls0, ls1); SB();
        s = MFMA(kf[9], q[9], s); vf[2] = VRD(2, va, vb); PIECE(true, 0, soA, mA); ls0 += x[8]; ls1 += x[9]; PIN2(ls0, ls1); SB();
        s = MFMA(kf[10], q[10], s); vf[3] = VRD(3, va, vb); PIECE(true, 1, soA, mA); ls0 += x[10]; ls1 += x[11]; PIN2(ls0, ls1); SB();
        s = MFMA(kf[11], q[11], s); vf[4] = VRD(4, va, vb); PIECE(true, 2, soA, mA); ls0 += x[12]; ls1 += x[13]; PIN2(ls0, ls1); SB();
        const bf16x8 p0f = __builtin_bit_cast(bf16x8, pa);
        PIN2(vf[0], vf[1]);
        vf[5] = VRD(5, va, vb);
        o[0] = MFMA(vf[0], p0f, o[0]); if (S == 2) PIECE(true, 3, soA, mA);
        pb[0] = cvtpk(x[8], x[9]); pb[1] = cvtpk(x[10], x[11]); PIN2(pb[0], pb[1]); ls0 += x[14]; ls1 += x[15]; PIN2(ls0, ls1); SB();
        vf[6] = VRD(6, va, vb);
        o[1] = MFMA(vf[1], p0f, o[1]); if (S == 2) PIECE(true, 4, soA, mA);
        pb[2] = cvtpk(x[12], x[13]); pb[3] = cvtpk(x[14], x[15]); PIN2(pb[2], pb[3]); l += ls0; PIN1(l); SB();
        PIN2(vf[2], vf[3]);
        vf[7] = VRD(7, va, vb);
        o[2] = MFMA(vf[2], p0f, o[2]); if (S == 2) PIECE(true, 5, soA, mA);
        l += ls1; PIN1(l); SB();
        const bf16x8 p1f = __builtin_bit_cast(bf16x8, pb);
        vf[8] = VRD(8, va, vb);
        o[3] = MFMA(vf[3], p0f, o[3]);
        float ma = max3f(s[0], s[1], s[2]), mb = max3f(s[8], s[9], s[10]);
        ma = max3f(ma, s[3], s[4]); mb = max3f(mb, s[11], s[12]);
        ma = max3f(ma, s[5], s[6]); mb = max3f(mb, s[13], s[14]); PIN2(ma, mb); SB();
        PIN2(vf[4], vf[5]);
        vf[9] = VRD(9, va, vb);
        o[4] = MFMA(vf[4], p0f, o[4]);
        ma = max3f(ma, s[7], s[15]);
        float pm = max2f(ma, mb); PIN1(pm); SB();
        vf[10] = VRD(10, va, vb); vf[11] = VRD(11, va, vb);
        o[5] = MFMA(vf[5], p0f, o[5]); SB();
        if (__builtin_expect(__any(pm > mthr), 0)) {
            const float mn = max2f(m, swap_max(pm));
            alpha = __builtin_amdgcn_exp2f((m - mn) * SCALE_LOG2E);
            m = mn; mthr = m + THR_RAW; mc = -m * SCALE_LOG2E; pend = 1;
        }
        PIN2(vf[6], vf[7]);
        o[0] = MFMA(vf[6], p1f, o[0]); FMAX2(0, 1); FMAX2(2, 3); FMAX2(4, 5); SB();
        o[1] = MFMA(vf[7], p1f, o[1]); FMAX2(6, 7); FMAX2(8, 9); FMAX2(10, 11); SB();
        if constexpr (KIND < 2) {
            const unsigned mB = sV;
            if (KIND == 0) GATE(PW, mB); else GATE0(PW);
            SETK(sN);
            kf[0] = KRC(0); SB();
            o[2] = MFMA(vf[8], p1f, o[2]); kf[1] = KRC(1); if (KIND == 0) PIECE(false, 0, soB, mB); FMAX2(12, 13); SB();
            o[3] = MFMA(vf[9], p1f, o[3]); kf[2] = KRC(2); if (KIND == 0) PIECE(false, 1, soB, mB); FMAX2(14, 15); SB();
            o[4] = MFMA(vf[10], p1f, o[4]); if (KIND == 0) PIECE(false, 2, soB, mB); kf[3] = KRC(3); if (KIND == 0 && S == 2) PIECE(false, 3, soB, mB); SB();
            o[5] = MFMA(vf[11], p1f, o[5]); if (KIND == 0 && S == 2) PIECE(false, 4, soB, mB); kf[4] = KRC(4); if (KIND == 0 && S == 2) PIECE(false, 5, soB, mB); SB();
        } else {
            o[2] = MFMA(vf[8], p1f, o[2]); FMAX2(12, 13); SB();
            o[3] = MFMA(vf[9], p1f, o[3]); FMAX2(14, 15); SB();
            o[4] = MFMA(vf[10], p1f, o[4]); o[5] = MFMA(vf[11], p1f, o[5]); SB();
        }
        { const unsigned t = sK; sK = sN; sN = sV; sV = t; }
        soA -= TSTEP; soB -= TSTEP;
        if (__builtin_expect(pend, 0)) {
            l *= alpha;
#pragma unroll
            for (int c = 0; c < 6; ++c) o[c] *= alpha;
            pend = 0;
        }
        ++it;
        UPDPRIO();
    };
    auto light_iter = [&](bool first_tile) {
        const unsigned mA = sK, mB = sV;
        if (first_tile) {
#pragma unroll
            for (int i = 5; i < 12; ++i) kf[i] = KRC(i);
#pragma unroll
            for (int i = 0; i < 12; ++i) s = MFMA(kf[i], q[i], s);
        }
        if (it + 2 < steps) GATE(PW, mA); else GATE(0, mA);
        NOP0();
        if (it + 1 < steps) PIECES(true, soA, mA);
        if (it + 2 < steps) {
            GATE(PW, mB);
            SETK(sN);
            kf[0] = KRC(0); kf[1] = KRC(1); kf[2] = KRC(2); kf[3] = KRC(3); kf[4] = KRC(4);
            if (it + 3 < steps) PIECES(false, soB, mB);
        }
        if (first_tile) first_max();
        { const unsigned t = sK; sK = sN; sN = sV; sV = t; }
        soA -= TSTEP; soB -= TSTEP;
        ++it;
        UPDPRIO();
    };
    if (S == 1 && late) light_iter(true);
    if (act) {
        const int nfull = min(itend, steps - 3);
        while (it < nfull) iter(IC<0>{});
        if (it < itend && it + 3 == steps) iter(IC<1>{});
        if (it < itend) iter(IC<2>{});
    }
    if (act) {
        const unsigned mA = sK, mB = sV;
        if (it + 2 < steps) GATE(PW, mA); else GATE(0, mA);
        const unsigned va = sV + vbw, vb = va ^ 64;
        bf16x8 vf[12];
        vf[0] = VRD(0, va, vb); vf[1] = VRD(1, va, vb); vf[2] = VRD(2, va, vb);
        vf[3] = VRD(3, va, vb); vf[4] = VRD(4, va, vb); vf[5] = VRD(5, va, vb);
        if (it + 1 < steps) PIECES(true, soA, mA);
        float ls0, ls1;
        EXP2X(0, 1); EXP2X(2, 3); EXP2X(4, 5); EXP2X(6, 7);
        pa[0] = cvtpk(x[0], x[1]); pa[1] = cvtpk(x[2], x[3]); pa[2] = cvtpk(x[4], x[5]); pa[3] = cvtpk(x[6], x[7]);
        PIN4(pa[0], pa[1], pa[2], pa[3]);
        const bf16x8 p0f = __builtin_bit_cast(bf16x8, pa);
        SB();
        vf[6] = VRD(6, va, vb);
        o[0] = MFMA(vf[0], p0f, o[0]); EXP2X(8, 9); ls0 = x[0] + x[2]; ls1 = x[1] + x[3]; PIN2(ls0, ls1); SB();
        vf[7] = VRD(7, va, vb);
        o[1] = MFMA(vf[1], p0f, o[1]); EXP2X(10, 11); ls0 += x[4]; ls1 += x[5]; PIN2(ls0, ls1); SB();
        vf[8] = VRD(8, va, vb);
        o[2] = MFMA(vf[2], p0f, o[2]); EXP2X(12, 13); ls0 += x[6]; ls1 += x[7]; PIN2(ls0, ls1); SB();
        vf[9] = VRD(9, va, vb);
        o[3] = MFMA(vf[3], p0f, o[3]); EXP2X(14, 15); pb[0] = cvtpk(x[8], x[9]); pb[1] = cvtpk(x[10], x[11]); PIN2(pb[0], pb[1]); SB();
        vf[10] = VRD(10, va, vb);
        o[4] = MFMA(vf[4], p0f, o[4]); pb[2] = cvtpk(x[12], x[13]); pb[3] = cvtpk(x[14], x[15]); PIN2(pb[2], pb[3]); ls0 += x[8]; ls1 += x[9]; PIN2(ls0, ls1); SB();
        vf[11] = VRD(11, va, vb);
        o[5] = MFMA(vf[5], p0f, o[5]); ls0 += x[10]; ls1 += x[11]; ls0 += x[12]; ls1 += x[13]; PIN2(ls0, ls1); SB();
        const bf16x8 p1f = __builtin_bit_cast(bf16x8, pb);
        o[0] = MFMA(vf[6], p1f, o[0]); ls0 += x[14]; ls1 += x[15]; l += ls0 + ls1; PIN1(l); SB();
        o[1] = MFMA(vf[7], p1f, o[1]);
        o[2] = MFMA(vf[8], p1f, o[2]); o[3] = MFMA(vf[9], p1f, o[3]); o[4] = MFMA(vf[10], p1f, o[4]); o[5] = MFMA(vf[11], p1f, o[5]);
        if (it + 2 < steps) {
            GATE(PW, mB);
            NOP0();
            if (it + 3 < steps) PIECES(false, soB, mB);
        }
        { const unsigned t = sK; sK = sN; sN = sV; sV = t; }
        soA -= TSTEP; soB -= TSTEP;
        ++it;
    }
    while (it < steps) light_iter(false);

    l = swap_add(l);
    const __amdgpu_buffer_rsrc_t orsrc = __builtin_amdgcn_make_buffer_rsrc((void*)(op + (size_t)s0 * QSTRIDE), 0, (unsigned)L * QSTRIDE, 0x00020000);
    auto stage_block = [&](unsigned base, int c, const f32x16& z) {
#pragma unroll
        for (int g = 0; g < 4; ++g) {
            u32x2 v = {cvtpk(z[4 * g], z[4 * g + 1]), cvtpk(z[4 * g + 2], z[4 * g + 3])};
            *(LDSP(u32x2))(size_t)(base + (32 * c + 8 * g) * 2) = v;
        }
    };
    auto store_rows = [&](auto NGC, unsigned stg, unsigned obase) {
        constexpr int NG = decltype(NGC)::value;
        const unsigned r0 = stg + lrd0, r1 = stg + lrd1, r2 = stg + lrd2;
        u32x4 v[3 * NG];
#pragma unroll
        for (int k = 0; k < NG; ++k) {
            v[3 * k] = *(LDSP(u32x4))(size_t)(r0 + 8 * k * ORS); v[3 * k + 1] = *(LDSP(u32x4))(size_t)(r1 + 8 * k * ORS); v[3 * k + 2] = *(LDSP(u32x4))(size_t)(r2 + 8 * k * ORS);
        }
        SB();
#pragma unroll
        for (int k = 0; k < NG; ++k) {
            __builtin_amdgcn_raw_buffer_store_b128(v[3 * k], orsrc, gof0, obase + 8 * k * QSTRIDE, STAUX);
            __builtin_amdgcn_raw_buffer_store_b128(v[3 * k + 1], orsrc, gof1, obase + 8 * k * QSTRIDE, STAUX);
            __builtin_amdgcn_raw_buffer_store_b128(v[3 * k + 2], orsrc, gof2, obase + 8 * k * QSTRIDE, STAUX);
        }
        SB();
    };
    wait_vm<0>();
    barrier();
    if (S == 1) {
        if (act) {
            const unsigned stg = lds0 + w * (32 * ORS);
            const float inv = __builtin_amdgcn_rcpf(l);
#pragma unroll
            for (int c = 0; c < 6; ++c) { const f32x16 z = o[c] * inv; stage_block(stg + stw, c, z); }
            asm volatile("" ::: "memory");
            const unsigned obase = (unsigned)pw * QSTRIDE + (unsigned)(2 * grp + hh) * ROWB;
            store_rows(IC<4>{}, stg, obase);
        }
    } else {
        const unsigned mine = lds0 + w * SLOT + lane * 16, other = lds0 + (w ^ 2) * SLOT + lane * 16;
        {
            f32x2 ml = {m, l};
            *(LDSP(f32x2))(size_t)(lds0 + 4 * SLOT + w * 512 + lane * 8) = ml;
        }
#pragma unroll
        for (int j = 0; j < 3; ++j)
#pragma unroll
            for (int g = 0; g < 4; ++g) {
                const f32x16& z = o[2 * j + 1];
                f32x4 y = {z[4 * g], z[4 * g + 1], z[4 * g + 2], z[4 * g + 3]};
                *(LDSP(f32x4))(size_t)(mine + (j * 4 + g) * 1024) = y;
            }
        barrier();
        const f32x2 mlo = *(LDSP(f32x2))(size_t)(lds0 + 4 * SLOT + (w ^ 2) * 512 + lane * 8);
        f32x4 y[12];
#pragma unroll
        for (int i = 0; i < 12; ++i) y[i] = *(LDSP(f32x4))(size_t)(other + i * 1024);
        const float mx = max2f(m, mlo[0]);
        const float a0 = __builtin_amdgcn_exp2f((m - mx) * SCALE_LOG2E), a1 = __builtin_amdgcn_exp2f((mlo[0] - mx) * SCALE_LOG2E);
        const float inv = __builtin_amdgcn_rcpf(l * a0 + mlo[1] * a1);
        const float f0 = a0 * inv, f1 = a1 * inv;
        SB();
        const unsigned stg = lds0 + 51200 + hh * (32 * ORS);
#pragma unroll
        for (int j = 0; j < 3; ++j) {
            f32x16 z;
#pragma unroll
            for (int g = 0; g < 4; ++g) {
                const f32x16& xo = o[2 * j];
                const f32x4 t = {xo[4 * g], xo[4 * g + 1], xo[4 * g + 2], xo[4 * g + 3]};
                const f32x4 u = t * f0 + y[4 * j + g] * f1;
                z[4 * g] = u[0]; z[4 * g + 1] = u[1]; z[4 * g + 2] = u[2]; z[4 * g + 3] = u[3];
            }
            stage_block(stg + stw + 64 * sig, 2 * j, z);
        }
        barrier();
        const unsigned obase = (unsigned)(pw + 16 * sig) * QSTRIDE + (unsigned)(2 * grp + hh) * ROWB;
        const unsigned stgr = stg + 16 * sig * ORS;
        store_rows(IC<2>{}, stgr, obase);
    }
}

__device__ __attribute__((noinline, cold)) i32x4 decode_slow(const int* __restrict__ cu, int nseq, int k) {
    int nmax = 0;
    for (int s = 0; s < nseq; ++s) nmax = max(nmax, (cu[s + 1] - cu[s] + 31) >> 5);
    const int B = BSPLIT;
    int N = 0;
    for (int s = 0; s < nseq; ++s) { const int n = (cu[s + 1] - cu[s] + 31) >> 5; N += max(n - B, 0) + min(B >> 1, (n + 1) >> 1); }
    int rank = k;
    if (rank >= N) return i32x4{0, 0, 0, 0};
    int jx = nmax, my = min(B >> 1, (nmax + 1) >> 1);
    for (;;) {
        const int x = (jx > B) && (((jx + 1) >> 1) >= 2 * my);
        const int thr = x ? 32 * (jx - 1) : 64 * (my - 1);
        for (int s = 0; s < nseq; ++s) {
            const int len = cu[s + 1] - cu[s];
            if (len > thr) { if (rank == 0) return i32x4{cu[s], len, thr, x}; --rank; }
        }
        if (x) --jx; else --my;
    }
}

__global__ __launch_bounds__(256, 2) void attn_varlen_kernel(const char* __restrict__ q_arg, const char* __restrict__ k_arg, const char* __restrict__ v_arg,
                                                             const int* __restrict__ cu_arg, char* __restrict__ op_arg, long long nseq_arg) {
    __shared__ __attribute__((aligned(128))) char smem_[LDS_BYTES];
    const unsigned lds0 = (unsigned)(size_t)(LDSP(char))smem_;
    (void)q_arg; (void)k_arg; (void)v_arg; (void)cu_arg; (void)op_arg; (void)nseq_arg;
    const int grp = blockIdx.x & 7;
    const int k = blockIdx.x >> 3;
    unsigned long long qa, ka_, va, ca, oa;
    int nseq, s0, L, p0, rt, valid_i, slow_i;
    {
        int vlen, vc0, vcnt, vtmp, vthr, vr3;
        asm volatile(
            "s_load_dwordx8 s[48:55], %[kp], 0x0\n\t"
            "s_load_dwordx4 s[56:59], %[kp], 0x20\n\t"
            "s_cmp_ge_u32 %[k], %[cwg]\n\t"
            "s_cbranch_scc1 3f\n\t"
            "v_readfirstlane_b32 s60, %[tid]\n\t"
            "s_getpc_b64 s[62:63]\n\t"
            "v_lshlrev_b32 %[tmp], 7, %[tid]\n\t"
            "s_lshl_b32 s60, s60, 2\n\t"
            "v_min_u32 %[tmp], %[cmax], %[tmp]\n\t"
            "s_add_i32 m0, s60, %[scr]\n\t"
            "s_and_b32 s62, s62, 0xffffff80\n\t"
            "s_nop 0\n\t"
            "global_load_lds_dword %[tmp], s[62:63]\n"
            "3:\n\t"
            "v_and_b32 %[cnt], 63, %[tid]\n\t"
            "v_mul_u32_u24 %[tmp], 43, %[cnt]\n\t"
            "v_lshrrev_b32 %[tmp], 7, %[tmp]\n\t"
            "v_mul_u32_u24 %[c0], 3, %[tmp]\n\t"
            "v_sub_u32 %[r3], %[cnt], %[c0]\n\t"
            "v_lshl_add_u32 %[c0], %[tmp], 1, %[r3]\n\t"
            "v_cmp_gt_u32_e64 s[80:81], 2, %[r3]\n\t"
            "v_cmp_le_u32_e64 s[82:83], %[B], %[c0]\n\t"
            "v_and_b32 %[len], 1, %[tmp]\n\t"
            "v_cmp_eq_u32_e64 s[84:85], 2, %[r3]\n\t"
            "v_cmp_gt_u32_e64 s[86:87], %[B], %[tmp]\n\t"
            "v_cmp_eq_u32_e64 s[88:89], 1, %[len]\n\t"
            "v_lshlrev_b32 %[c0], 5, %[c0]\n\t"
            "v_add_u32 %[tmp], -1, %[tmp]\n\t"
            "v_lshlrev_b32 %[tmp], 5, %[tmp]\n\t"
            "s_and_b64 s[80:81], s[80:81], s[82:83]\n\t"
            "s_and_b64 s[84:85], s[84:85], s[86:87]\n\t"
            "s_and_b64 s[84:85], s[84:85], s[88:89]\n\t"
            "v_bfrev_b32 %[thr], -2\n\t"
            "v_cndmask_b32_e64 %[thr], %[thr], %[tmp], s[84:85]\n\t"
            "v_cndmask_b32_e64 %[thr], %[thr], %[c0], s[80:81]\n\t"
            "s_mov_b32 s99, 0\n"
            "1:\n\t"
            "s_sub_i32 s40, s65, s64\n\ts_sub_i32 s41, s66, s65\n\ts_sub_i32 s42, s67, s66\n\ts_sub_i32 s43, s68, s67\n\t"
            "s_sub_i32 s44, s69, s68\n\ts_sub_i32 s45, s70, s69\n\ts_sub_i32 s46, s71, s70\n\ts_sub_i32 s47, s72, s71\n\t"
            "v_cmp_gt_i32_e64 s[80:81], s40, %[thr]\n\tv_cmp_gt_i32_e64 s[82:83], s41, %[thr]\n\tv_cmp_gt_i32_e64 s[84:85], s42, %[thr]\n\tv_cmp_gt_i32_e64 s[86:87], s43, %[thr]\n\t"
            "v_cmp_gt_i32_e64 s[88:89], s44, %[thr]\n\tv_cmp_gt_i32_e64 s[90:91], s45, %[thr]\n\tv_cmp_gt_i32_e64 s[92:93], s46, %[thr]\n\tv_cmp_gt_i32_e64 s[94:95], s47, %[thr]\n\t"
            "v_mov_b32 %[cnt], 0\n\tv_mov_b32 %[len], 0\n\tv_mov_b32 %[c0], 0\n\t"
            "v_addc_co_u32_e64 %[cnt], vcc, %[cnt], 0, s[80:81]\n\tv_addc_co_u32_e64 %[cnt], vcc, %[cnt], 0, s[82:83]\n\t"
            "v_addc_co_u32_e64 %[cnt], vcc, %[cnt], 0, s[84:85]\n\tv_addc_co_u32_e64 %[cnt], vcc, %[cnt], 0, s[86:87]\n\t"
            "v_addc_co_u32_e64 %[cnt], vcc, %[cnt], 0, s[88:89]\n\tv_addc_co_u32_e64 %[cnt], vcc, %[cnt], 0, s[90:91]\n\t"
            "v_addc_co_u32_e64 %[cnt], vcc, %[cnt], 0, s[92:93]\n\tv_addc_co_u32_e64 %[cnt], vcc, %[cnt], 0, s[94:95]\n\t"
            "v_writelane_b32 %[len], s40, 0\n\tv_writelane_b32 %[c0], s64, 0\n\t"
            "v_add_u32_dpp %[cnt], %[cnt], %[cnt] row_shr:1 row_mask:0xf bank_mask:0xf bound_ctrl:1\n\t"
            "v_writelane_b32 %[len], s41, 1\n\tv_writelane_b32 %[c0], s65, 1\n\t"
            "v_add_u32_dpp %[cnt], %[cnt], %[cnt] row_shr:2 row_mask:0xf bank_mask:0xf bound_ctrl:1\n\t"
            "v_writelane_b32 %[len], s42, 2\n\tv_writelane_b32 %[c0], s66, 2\n\t"
            "v_add_u32_dpp %[cnt], %[cnt], %[cnt] row_shr:4 row_mask:0xf bank_mask:0xf bound_ctrl:1\n\t"
            "v_writelane_b32 %[len], s43, 3\n\tv_writelane_b32 %[c0], s67, 3\n\t"
            "v_add_u32_dpp %[cnt], %[cnt], %[cnt] row_shr:8 row_mask:0xf bank_mask:0xf bound_ctrl:1\n\t"
            "v_writelane_b32 %[len], s44, 4\n\tv_writelane_b32 %[c0], s68, 4\n\t"
            "v_add_u32_dpp %[cnt], %[cnt], %[cnt] row_bcast:15 row_mask:0xa bank_mask:0xf\n\t"
            "v_writelane_b32 %[len], s45, 5\n\tv_writelane_b32 %[c0], s69, 5\n\t"
            "v_add_u32_dpp %[cnt], %[cnt], %[cnt] row_bcast:31 row_mask:0xc bank_mask:0xf\n\t"
            "v_writelane_b32 %[len], s46, 6\n\tv_writelane_b32 %[c0], s70, 6\n\t"
            "v_writelane_b32 %[len], s47, 7\n\tv_writelane_b32 %[c0], s71, 7\n\t"
            "v_readlane_b32 s96, %[cnt], 63\n\t"
            "v_cmp_lt_i32_e32 vcc, %[lmax], %[len]\n\t"
            "s_nop 0\n\t"
            "s_sub_i32 s97, s96, %[k]\n\t"
            "s_add_i32 s97, s97, 31\n\t"
            "s_cmp_lg_u64 vcc, 0\n\t"
            "s_cselect_b32 %[slow], 1, 0\n\t"
            "s_cmp_ge_i32 %[k], 32\n\ts_cselect_b32 s97, s97, %[k]\n\t"
            "s_cmp_lt_i32 %[k], s96\n\ts_cselect_b32 s97, s97, %[k]\n\t"
            "s_cmp_le_i32 s96, 64\n\ts_cselect_b32 s97, s97, %[k]\n\t"
            "s_cmp_lt_i32 s97, s96\n\ts_cselect_b32 %[valid], 1, 0\n\t"
            "s_sub_i32 s97, s96, s97\n\t"
            "v_cmp_le_i32_e32 vcc, s97, %[cnt]\n\t"
            "s_nop 0\n\t"
            "s_ff1_i32_b64 s98, vcc\n\t"
            "s_and_b32 s98, s98, 63\n\t"
            "v_readlane_b32 s96, %[cnt], s98\n\t"
            "v_readlane_b32 %[p0], %[thr], s98\n\t"
            "v_readlane_b32 %[rt], %[r3], s98\n\t"
            "s_sub_i32 s96, s96, s97\n\t"
            "s_nop 0\n\t"
            "v_cmp_lt_i32_e32 vcc, %[p0], %[len]\n\t"
            "s_nop 1\n\t"
            "v_mbcnt_lo_u32_b32 %[tmp], vcc_lo, 0\n\t"
            "v_mbcnt_hi_u32_b32 %[tmp], vcc_hi, %[tmp]\n\t"
            "v_cmp_eq_u32_e64 s[80:81], s96, %[tmp]\n\t"
            "s_nop 0\n\t"
            "s_and_b64 s[80:81], s[80:81], vcc\n\t"
            "s_ff1_i32_b64 s98, s[80:81]\n\t"
            "s_and_b32 s98, s98, 63\n\t"
            "v_readlane_b32 %[s0], %[c0], s98\n\t"
            "v_readlane_b32 %[L], %[len], s98\n\t"
            "s_cmp_lg_u32 s99, 0\n\t"
            "s_cbranch_scc1 2f\n\t"
            "s_waitcnt lgkmcnt(0)\n\t"
            "s_mov_b32 s60, s54\n\ts_and_b32 s61, s55, 0xffff\n\ts_lshl_b32 s62, s58, 2\n\ts_add_i32 s62, s62, 4\n\ts_mov_b32 s63, 0x20000\n\t"
            "s_buffer_load_dwordx16 s[64:79], s[60:63], 0\n\t"
            "s_mov_b32 s99, 1\n\t"
            "s_waitcnt lgkmcnt(0)\n\t"
            "s_branch 1b\n"
            "2:\n\t"
            "s_mov_b64 %[q], s[48:49]\n\ts_mov_b64 %[kk], s[50:51]\n\ts_mov_b64 %[v], s[52:53]\n\ts_mov_b64 %[cu], s[54:55]\n\ts_mov_b64 %[o], s[56:57]\n\t"
            "s_mov_b32 %[ns], s58\n\t"
            "s_nop 3"
            : [q] "=&s"(qa), [kk] "=&s"(ka_), [v] "=&s"(va), [cu] "=&s"(ca), [o] "=&s"(oa), [s0] "=&s"(s0), [L] "=&s"(L), [p0] "=&s"(p0), [rt] "=&s"(rt),
              [valid] "=&s"(valid_i), [slow] "=&s"(slow_i), [ns] "=&s"(nseq), [len] "=&v"(vlen), [c0] "=&v"(vc0), [cnt] "=&v"(vcnt), [tmp] "=&v"(vtmp), [thr] "=&v"(vthr), [r3] "=&v"(vr3)
            : [kp] "s"(__builtin_amdgcn_kernarg_segment_ptr()), [k] "s"(k), [lmax] "s"(32 * 42), [tid] "v"(threadIdx.x), [B] "i"(BSPLIT),
              [cwg] "i"(CODEPF > 0 ? 8 : 0), [cmax] "s"(CODEPF > 0 ? CODEPF - 128 : 0), [scr] "s"(lds0 + 76800)
            : "memory", "scc", "vcc",
              "s40", "s41", "s42", "s43", "s44", "s45", "s46", "s47", "s48", "s49", "s50", "s51", "s52", "s53", "s54", "s55", "s56", "s57", "s58", "s59",
              "s60", "s61", "s62", "s63", "s64", "s65", "s66", "s67", "s68", "s69", "s70", "s71", "s72", "s73", "s74", "s75", "s76", "s77", "s78", "s79",
              "s80", "s81", "s82", "s83", "s84", "s85", "s86", "s87", "s88", "s89", "s90", "s91", "s92", "s93", "s94", "s95", "s96", "s97", "s98", "s99");
    }
    const char* qp = (const char*)qa; const char* kp = (const char*)ka_; const char* vp = (const char*)va; const int* cu = (const int*)ca; char* op = (char*)oa;
    int isx = rt < 2;
    bool valid = valid_i != 0;
    if (__builtin_expect(nseq > 8 || slow_i != 0, 0)) {
        const i32x4 r = decode_slow(cu, nseq, k);
        s0 = rfl(r[0]); L = rfl(r[1]); p0 = rfl(r[2]); isx = rfl(r[3]); valid = L > 0;
    }
    if (!valid) return;
    s0 = rfl(s0); L = rfl(L); p0 = rfl(p0); isx = rfl(isx);
    if (isx) {
        const int n = (min(p0 + 32, L) + 31) >> 5;
        attn_item<2>(qp, kp, vp, op, grp, s0, L, p0, n, lds0);
    } else {
        const int n = (min(p0 + 64, L) + 31) >> 5;
        attn_item<1>(qp, kp, vp, op, grp, s0, L, p0, n, lds0);
    }
}

extern "C" void kernel_launch(void* const* d_in, const int* in_sizes, int n_in, void* d_out, int out_size, void* d_ws, size_t ws_size,
                              hipStream_t stream) {
    (void)n_in; (void)out_size; (void)d_ws; (void)ws_size;
    const int T = in_sizes[0] / (HQ * D);
    const int nseq = in_sizes[3] - 1;
    const int per_head = T / 32 + nseq + 1;
    attn_varlen_kernel<<<dim3(8 * per_head), dim3(256), 0, stream>>>((const char*)d_in[0], (const char*)d_in[1], (const char*)d_in[2],
                                                                    (const int*)d_in[3], (char*)d_out, (long long)nseq);
}
```
